```python
import torch

def _softmax(x, axis=-1):
    return torch.softmax(x, dim=axis)
def _gelu(x):
    return torch.nn.functional.gelu(x, approximate="tanh")
def _silu(x):
    return torch.nn.functional.silu(x)
def _sigmoid(x):
    return torch.sigmoid(x)
def _layernorm(x, axis=-1, eps=1e-5):
    m = x.mean(dim=axis, keepdim=True)
    v = x.var(dim=axis, keepdim=True, unbiased=False)
    return (x - m) / torch.sqrt(v + eps)
def _scatter_add(tgt, idx, src, axis=0):
    return tgt.clone().index_add_(axis, idx, src)
def _segment_sum(src, seg, n):
    out = torch.zeros((n,) + src.shape[1:], dtype=src.dtype)
    return out.index_add_(0, seg, src)
def _pad(x, pw):
    flat = tuple(v for pair in reversed(pw) for v in pair)
    return torch.nn.functional.pad(x, flat)

kernel_name = 'gated_mix__spectral_gfnet__ssm_conv_s4__prompt'

def setup_inputs():

    _w = (lambda a: (lambda t: t.to(torch.bfloat16) if t.is_floating_point() else t)(torch.as_tensor(a)))
    def _A_setup():
        B, S, D = 1, 2048, 448
        Df = D // 2 + 1
        return {
            "x": _w((lambda *s: torch.randn(*s, dtype=torch.bfloat16) * 0.05)(B, S, D)),
            "w_r": _w((lambda *s: torch.randn(*s, dtype=torch.bfloat16) * 0.8)(S, Df) * 0.02),
            "w_i": _w((lambda *s: torch.randn(*s, dtype=torch.bfloat16) * 0.8)(S, Df) * 0.02),
        }
    _A_in = _A_setup()

    def _B_setup():
        B, L, D, N = 1, 2048, 448, 256
        A_re = -(lambda _x: torch.exp(torch.as_tensor(_x)))((lambda *s: torch.randn(*s, dtype=torch.bfloat16) * 3.2)(D, N))
        A_im = torch.pi * (lambda *s: torch.randn(*s, dtype=torch.bfloat16) * 0.05)(D, N)
        B_re = (lambda *s: torch.randn(*s, dtype=torch.bfloat16) * 0.05)(D, N)
        B_im = (lambda *s: torch.randn(*s, dtype=torch.bfloat16) * 0.05)(D, N)
        C_re = (lambda *s: torch.randn(*s, dtype=torch.bfloat16) * 0.05)(D, N)
        C_im = (lambda *s: torch.randn(*s, dtype=torch.bfloat16) * 0.1)(D, N)
        log_dt = (lambda *s: torch.randn(*s, dtype=torch.bfloat16) * 0.05)(D) * 0.5 - 4.0
        Dskip = (lambda *s: torch.randn(*s, dtype=torch.bfloat16) * 0.05)(D)
        return {
            "u": _w((lambda *s: torch.randn(*s, dtype=torch.bfloat16) * 0.05)(B, L, D)),
            "A_re": _w(A_re),
            "A_im": _w(A_im),
            "B_re": _w(B_re),
            "B_im": _w(B_im),
            "C_re": _w(C_re),
            "C_im": _w(C_im),
            "log_dt": _w(log_dt),
            "Dskip": _w(Dskip),
        }
    _B_in = _B_setup()

    return {
        "A_x": _A_in["x"],
        "A_w_r": _A_in["w_r"],
        "A_w_i": _A_in["w_i"],
        "B_u": _B_in["u"],
        "B_A_re": _B_in["A_re"],
        "B_A_im": _B_in["A_im"],
        "B_B_re": _B_in["B_re"],
        "B_B_im": _B_in["B_im"],
        "B_C_re": _B_in["C_re"],
        "B_C_im": _B_in["C_im"],
        "B_log_dt": _B_in["log_dt"],
        "B_Dskip": _B_in["Dskip"],
        "gate_w": _w((lambda *s: torch.randn(*s, dtype=torch.bfloat16) * 0.05)(448) * 16.0),
    }

def reference(A_x, A_w_r, A_w_i, B_u, B_A_re, B_A_im, B_B_re, B_B_im, B_C_re, B_C_im, B_log_dt, B_Dskip, gate_w):
    def _A_ref(x, w_r, w_i):
        B, S, D = x.shape
        X = (lambda x, axes=(-2,-1): torch.fft.rfft2(x.float(), dim=axes))(x, axes=(-2, -1))
        W = w_r + 1j * w_i
        X = X * W
        y = (lambda x, s, axes=(-2,-1): torch.fft.irfft2(x, s=s, dim=axes).to(torch.bfloat16))(X, (S, D), axes=(-2, -1))
        return y

    def _B_ref(u, A_re, A_im, B_re, B_im, C_re, C_im, log_dt, Dskip):
        Bsz, L, D = u.shape
        dt = torch.exp(log_dt)[:, None]
        A = A_re + 1j * A_im
        Bc = B_re + 1j * B_im
        Cc = C_re + 1j * C_im
        logdA = dt * A
        dB = dt * Bc
        l = (lambda x, dt: x.to(dt))(torch.arange(L), A_re.dtype)
        P = torch.exp(logdA[:, :, None] * l[None, None, :])
        K = torch.einsum("dn,dnl,dn->dl", Cc, P, dB).real
        L2 = 2 * L
        Kp = _pad(K, ((0, 0), (0, L)))
        up = (lambda x, *d: x.permute(*d))(u, 0, 2, 1)
        up = _pad(up, ((0, 0), (0, 0), (0, L)))
        Kf = (lambda x, axes=(-2,-1): torch.fft.rfft2(x.float(), dim=axes))(Kp[:, None, :], axes=(-2, -1))
        Uf = (lambda x, axes=(-2,-1): torch.fft.rfft2(x.float(), dim=axes))(up[:, :, None, :], axes=(-2, -1))
        Yf = Uf * Kf[None, :, :, :]
        y = (lambda x, s, axes=(-2,-1): torch.fft.irfft2(x, s=s, dim=axes).to(torch.bfloat16))(Yf, (1, L2), axes=(-2, -1))[:, :, 0, :L]
        y = (lambda x, *d: x.permute(*d))(y, 0, 2, 1) + u * Dskip[None, None, :]
        return y

    x = A_x
    g = _sigmoid((x * gate_w).sum(-1, keepdims=True))
    a = _A_ref(x, A_w_r, A_w_i)
    b = _B_ref(x, B_A_re, B_A_im, B_B_re, B_B_im, B_C_re, B_C_im, B_log_dt, B_Dskip)
    return g * a + (1 - g) * b
```

```cpp
#define PF_CODE 440
#include <hip/hip_runtime.h>
#include <stdint.h>

typedef unsigned short u16;
typedef unsigned long long u64;
typedef float f2 __attribute__((ext_vector_type(2)));
typedef float f4 __attribute__((ext_vector_type(4)));
typedef short s8 __attribute__((ext_vector_type(8)));
typedef short s4 __attribute__((ext_vector_type(4)));
typedef unsigned u4 __attribute__((ext_vector_type(4)));
typedef unsigned u2 __attribute__((ext_vector_type(2)));
typedef __bf16 b2 __attribute__((ext_vector_type(2)));
#define LAS __attribute__((address_space(3)))
#define GAS __attribute__((address_space(1)))
#define DI __device__ __forceinline__
typedef LAS char* lptr;
typedef GAS u64 gu64;
#define RLX_AGENT __ATOMIC_RELAXED, __HIP_MEMORY_SCOPE_AGENT

constexpr int TAB_T1 = 0, TAB_T3 = 7168, TAB_F8A = 15360, TAB_F8I = 15872, TAB_G = 17920, TAB_H = 18432;
__device__ const unsigned g_tab[18944] __attribute__((aligned(16))) = {
0x3f803f80,0x3f803f80,0x3f803f80,0x3f803f80,0x3f803f80,0x3f7c3f7e,0x3f763f7a,0x3f6d3f72,0x3f7e3f80,0x3f723f7a,0x3f593f67,0x3f353f48,0x3f7c3f80,0x3f603f72,0x3f2b3f48,0x3ec43f08,
0x3f7a3f80,0x3f483f67,0x3ede3f20,0x3e64,0x3f763f80,0x3f2b3f59,0x3e2c3ede,0xbec4bde5,0x3f723f80,0x3f083f48,0xbde53e64,0xbf35bede,0x3f6d3f80,0x3ec43f35,0xbec40000,0xbf6dbf35,
0x3f673f80,0x3e643f20,0xbf20be64,0xbf80bf67,0x3f603f80,0x3d663f08,0xbf51bede,0xbf6dbf7e,0x3f593f80,0xbde53ede,0xbf72bf20,0xbf35bf7a,0x3f513f80,0xbe8e3ea9,0xbf80bf48,0xbec4bf59,
0x3f483f80,0xbede3e64,0xbf7abf67,0xbf20,0x3f3f3f80,0xbf143de5,0xbf60bf7a,0x3ec4bea9,0x3f353f80,0xbf350000,0xbf35bf80,0x3f350000,0x3f2b3f80,0xbf51bde5,0xbef8bf7a,0x3f6d3ea9,
0x3f803f80,0x3f803f80,0x3f803f80,0x3f803f80,0x3f603f67,0x3f513f59,0x3f3f3f48,0x3f2b3f35,0x3f083f20,0x3ea93ede,0x3de53e64,0xbde50000,0x3d663e64,0xbe8ebde5,0xbf14bede,0xbf51bf35,
0xbedebe64,0xbf48bf20,0xbf7abf67,0xbf7abf80,0xbf51bf20,0xbf80bf72,0xbf60bf7a,0xbef8bf35,0xbf7ebf67,0xbf59bf7a,0xbea9bf20,0x3ea90000,0xbf6dbf80,0xbec4bf35,0x3ec40000,0x3f6d3f35,
0xbf20bf67,0x3e64be64,0x3f673f20,0x3f673f80,0xbe2cbf20,0x3f3f3ea9,0x3f763f7a,0x3e8e3f35,0x3ea9be64,0x3f7e3f48,0x3f083f67,0xbf080000,0x3f3f3e64,0x3f603f7e,0xbe2c3ede,0xbf7cbf35,
0x3f7a3f20,0x3ede3f67,0xbf48be64,0xbf48bf80,0x3f763f67,0xbe2c3f08,0xbf80bf48,0xbd66bf35,0x3f353f80,0xbf350000,0xbf35bf80,0x3f350000,0x3e8e3f67,0xbf7cbf08,0xbd66bf48,0x3f803f35,
0x3f803f80,0x3f803f80,0x3f803f80,0x3f803f80,0x3f143f20,0x3ef83f08,0x3ec43ede,0x3e8e3ea9,0xbea9be64,0xbf08bede,0xbf35bf20,0xbf59bf48,0xbf76bf67,0xbf80bf7e,0xbf6dbf7a,0xbf3fbf59,
0xbf48bf67,0xbedebf20,0xbe64,0x3ede3e64,0x3d66be64,0x3f143ea9,0x3f6d3f48,0x3f7c3f7e,0x3f593f20,0x3f7e3f7a,0x3f353f67,0x3de53ede,0x3f6d3f80,0x3ec43f35,0xbec40000,0xbf6dbf35,
0x3e643f20,0xbf20be64,0xbf80bf67,0xbf20bf67,0xbf2bbe64,0xbf7cbf72,0xbec4bf48,0x3f143de5,0xbf7ebf67,0xbea9bf48,0x3f353e64,0x3f723f7a,0xbef8bf67,0x3f2b3de5,0x3f6d3f7a,0xbd663f08,
0x3edebe64,0x3f7a3f67,0x3f20,0xbf7abf20,0x3f7c3f20,0x3e8e3f59,0xbf6dbede,0xbef8bf72,0x3f353f80,0xbf350000,0xbf35bf80,0x3f350000,0xbe2c3f20,0xbf76bf59,0x3ec4bede,0x3f603f72,
0x3f803f80,0x3f803f80,0x3f803f80,0x3f803f80,0x3e2c3e64,0x3d663de5,0xbd660000,0xbe2cbde5,0xbf72bf67,0xbf7ebf7a,0xbf7ebf80,0xbf72bf7a,0xbef8bf20,0xbe2cbea9,0x3e2c0000,0x3ef83ea9,
0x3f483f20,0x3f7a3f67,0x3f7a3f80,0x3f483f67,0x3f3f3f67,0x3e8e3f08,0xbe8e0000,0xbf3fbf08,0xbf08be64,0xbf72bf48,0xbf72bf80,0xbf08bf48,0xbf6dbf80,0xbec4bf35,0x3ec40000,0x3f6d3f35,
0x3e64be64,0x3f673f20,0x3f673f80,0x3e643f20,0x3f803f67,0x3ef83f59,0xbef80000,0xbf80bf59,0x3de53f20,0xbf59bede,0xbf59bf80,0x3de5bede,0xbf76bf20,0xbf14bf72,0x3f140000,0x3f763f72,
0xbedebf67,0x3f483e64,0x3f483f80,0xbede3e64,0x3f513e64,0x3f2b3f7e,0xbf2b0000,0xbf51bf7e,0x3f353f80,0xbf350000,0xbf35bf80,0x3f350000,0xbf143e64,0xbf3fbf7e,0x3f3f0000,0x3f143f7e,
0x3f803f80,0x3f803f80,0x3f803f80,0x3f803f80,0xbe8ebe64,0xbec4bea9,0xbef8bede,0xbf14bf08,0xbf59bf67,0xbf35bf48,0xbf08bf20,0xbea9bede,0x3f3f3f20,0x3f6d3f59,0x3f803f7a,0x3f763f7e,
0x3ede3f20,0x3e64,0xbedebe64,0xbf48bf20,0xbf7cbf67,0xbf6dbf7e,0xbf14bf48,0xbd66bea9,0x3de5be64,0x3f353ede,0x3f7e3f67,0x3f593f7a,0x3f6d3f80,0x3ec43f35,0xbec40000,0xbf6dbf35,
0xbf20be64,0xbf80bf67,0xbf20bf67,0x3e64be64,0xbf14bf67,0x3ec4bde5,0x3f7c3f48,0x3f2b3f72,0x3f723f20,0x3f353f7a,0xbea93e64,0xbf7ebf48,0x3d663f20,0xbf6dbf08,0xbf2bbf7a,0x3ef8bde5,
0xbf7abf67,0xbf20,0x3f7a3f20,0x3ede3f67,0x3ef8be64,0x3f6d3f72,0xbe8e3ede,0xbf7cbf59,0x3f353f80,0xbf350000,0xbf35bf80,0x3f350000,0xbf60be64,0xbec4bf72,0x3f763ede,0x3e2c3f59,
0x3f803f80,0x3f803f80,0x3f803f80,0x3f803f80,0xbf2bbf20,0xbf3fbf35,0xbf51bf48,0xbf60bf59,0xbde5be64,0x3de50000,0x3ea93e64,0x3f083ede,0x3f513f67,0x3f143f35,0x3e8e3ede,0xbd663de5,
0xbf7abf67,0xbf7abf80,0xbf48bf67,0xbedebf20,0x3ef83e64,0x3f603f35,0x3f803f7a,0x3f513f72,0x3ea93f20,0xbea90000,0xbf59bf20,0xbf7ebf7a,0xbf6dbf80,0xbec4bf35,0x3ec40000,0x3f6d3f35,
0x3f673f20,0x3f673f80,0x3e643f20,0xbf20be64,0xbe8e3e64,0xbf76bf35,0xbf3fbf7a,0x3e2cbea9,0xbf08bf67,0x3f080000,0x3f7e3f67,0x3ea93f48,0x3f7c3f67,0x3e2c3f35,0xbf60bede,0xbf3fbf7e,
0xbf48be64,0xbf48bf80,0x3edebe64,0x3f7a3f67,0x3d66bf20,0x3f803f35,0x3e2c3f48,0xbf76bf08,0x3f353f80,0xbf350000,0xbf35bf80,0x3f350000,0xbf80bf20,0x3d66bf35,0x3f7c3f48,0xbe8e3f08,
0x3f803f80,0x3f803f80,0x3f803f80,0x3f803f80,0xbf6dbf67,0xbf76bf72,0xbf7cbf7a,0xbf80bf7e,0x3f353f20,0x3f593f48,0x3f723f67,0x3f7e3f7a,0xbec4be64,0xbf2bbf08,0xbf60bf48,0xbf7cbf72,
0xbe64,0x3ede3e64,0x3f483f20,0x3f7a3f67,0x3ec43f20,0xbe2c3de5,0xbf2bbede,0xbf76bf59,0xbf35bf67,0xbde5bede,0x3f083e64,0x3f723f48,0x3f6d3f80,0x3ec43f35,0xbec40000,0xbf6dbf35,
0xbf80bf67,0xbf20bf67,0x3e64be64,0x3f673f20,0x3f6d3f20,0x3f513f7e,0xbd663ede,0xbf60bf08,0xbf35be64,0xbf72bf7a,0xbde5bf20,0x3f593ede,0x3ec4be64,0x3f803f59,0x3e8e3f48,0xbf51bea9,
0x3f20,0xbf7abf20,0xbedebf67,0x3f483e64,0xbec4bf67,0x3f603ea9,0x3f143f7a,0xbf3fbde5,0x3f353f80,0xbf350000,0xbf35bf80,0x3f350000,0xbf6dbf67,0x3ef8bea9,0x3f513f7a,0xbf2b3de5,
0x3f803f80,0x3f803f80,0x3f803f80,0x3f803f80,0xbf80bf80,0xbf7cbf7e,0xbf76bf7a,0xbf6dbf72,0x3f7e3f80,0x3f723f7a,0x3f593f67,0x3f353f48,0xbf7cbf80,0xbf60bf72,0xbf2bbf48,0xbec4bf08,
0x3f7a3f80,0x3f483f67,0x3ede3f20,0x3e64,0xbf76bf80,0xbf2bbf59,0xbe2cbede,0x3ec43de5,0x3f723f80,0x3f083f48,0xbde53e64,0xbf35bede,0xbf6dbf80,0xbec4bf35,0x3ec40000,0x3f6d3f35,
0x3f673f80,0x3e643f20,0xbf20be64,0xbf80bf67,0xbf60bf80,0xbd66bf08,0x3f513ede,0x3f6d3f7e,0x3f593f80,0xbde53ede,0xbf72bf20,0xbf35bf7a,0xbf51bf80,0x3e8ebea9,0x3f803f48,0x3ec43f59,
0x3f483f80,0xbede3e64,0xbf7abf67,0xbf20,0xbf3fbf80,0x3f14bde5,0x3f603f7a,0xbec43ea9,0x3f353f80,0xbf350000,0xbf35bf80,0x3f350000,0xbf2bbf80,0x3f513de5,0x3ef83f7a,0xbf6dbea9,
0x3f803f80,0x3f803f80,0x3f803f80,0x3f803f80,0xbf60bf67,0xbf51bf59,0xbf3fbf48,0xbf2bbf35,0x3f083f20,0x3ea93ede,0x3de53e64,0xbde50000,0xbd66be64,0x3e8e3de5,0x3f143ede,0x3f513f35,
0xbedebe64,0xbf48bf20,0xbf7abf67,0xbf7abf80,0x3f513f20,0x3f803f72,0x3f603f7a,0x3ef83f35,0xbf7ebf67,0xbf59bf7a,0xbea9bf20,0x3ea90000,0x3f6d3f80,0x3ec43f35,0xbec40000,0xbf6dbf35,
0xbf20bf67,0x3e64be64,0x3f673f20,0x3f673f80,0x3e2c3f20,0xbf3fbea9,0xbf76bf7a,0xbe8ebf35,0x3ea9be64,0x3f7e3f48,0x3f083f67,0xbf080000,0xbf3fbe64,0xbf60bf7e,0x3e2cbede,0x3f7c3f35,
0x3f7a3f20,0x3ede3f67,0xbf48be64,0xbf48bf80,0xbf76bf67,0x3e2cbf08,0x3f803f48,0x3d663f35,0x3f353f80,0xbf350000,0xbf35bf80,0x3f350000,0xbe8ebf67,0x3f7c3f08,0x3d663f48,0xbf80bf35,
0x3f803f80,0x3f803f80,0x3f803f80,0x3f803f80,0xbf14bf20,0xbef8bf08,0xbec4bede,0xbe8ebea9,0xbea9be64,0xbf08bede,0xbf35bf20,0xbf59bf48,0x3f763f67,0x3f803f7e,0x3f6d3f7a,0x3f3f3f59,
0xbf48bf67,0xbedebf20,0xbe64,0x3ede3e64,0xbd663e64,0xbf14bea9,0xbf6dbf48,0xbf7cbf7e,0x3f593f20,0x3f7e3f7a,0x3f353f67,0x3de53ede,0xbf6dbf80,0xbec4bf35,0x3ec40000,0x3f6d3f35,
0x3e643f20,0xbf20be64,0xbf80bf67,0xbf20bf67,0x3f2b3e64,0x3f7c3f72,0x3ec43f48,0xbf14bde5,0xbf7ebf67,0xbea9bf48,0x3f353e64,0x3f723f7a,0x3ef83f67,0xbf2bbde5,0xbf6dbf7a,0x3d66bf08,
0x3edebe64,0x3f7a3f67,0x3f20,0xbf7abf20,0xbf7cbf20,0xbe8ebf59,0x3f6d3ede,0x3ef83f72,0x3f353f80,0xbf350000,0xbf35bf80,0x3f350000,0x3e2cbf20,0x3f763f59,0xbec43ede,0xbf60bf72,
0x3f803f80,0x3f803f80,0x3f803f80,0x3f803f80,0xbe2cbe64,0xbd66bde5,0x3d660000,0x3e2c3de5,0xbf72bf67,0xbf7ebf7a,0xbf7ebf80,0xbf72bf7a,0x3ef83f20,0x3e2c3ea9,0xbe2c0000,0xbef8bea9,
0x3f483f20,0x3f7a3f67,0x3f7a3f80,0x3f483f67,0xbf3fbf67,0xbe8ebf08,0x3e8e0000,0x3f3f3f08,0xbf08be64,0xbf72bf48,0xbf72bf80,0xbf08bf48,0x3f6d3f80,0x3ec43f35,0xbec40000,0xbf6dbf35,
0x3e64be64,0x3f673f20,0x3f673f80,0x3e643f20,0xbf80bf67,0xbef8bf59,0x3ef80000,0x3f803f59,0x3de53f20,0xbf59bede,0xbf59bf80,0x3de5bede,0x3f763f20,0x3f143f72,0xbf140000,0xbf76bf72,
0xbedebf67,0x3f483e64,0x3f483f80,0xbede3e64,0xbf51be64,0xbf2bbf7e,0x3f2b0000,0x3f513f7e,0x3f353f80,0xbf350000,0xbf35bf80,0x3f350000,0x3f14be64,0x3f3f3f7e,0xbf3f0000,0xbf14bf7e,
0x3f803f80,0x3f803f80,0x3f803f80,0x3f803f80,0x3e8e3e64,0x3ec43ea9,0x3ef83ede,0x3f143f08,0xbf59bf67,0xbf35bf48,0xbf08bf20,0xbea9bede,0xbf3fbf20,0xbf6dbf59,0xbf80bf7a,0xbf76bf7e,
0x3ede3f20,0x3e64,0xbedebe64,0xbf48bf20,0x3f7c3f67,0x3f6d3f7e,0x3f143f48,0x3d663ea9,0x3de5be64,0x3f353ede,0x3f7e3f67,0x3f593f7a,0xbf6dbf80,0xbec4bf35,0x3ec40000,0x3f6d3f35,
0xbf20be64,0xbf80bf67,0xbf20bf67,0x3e64be64,0x3f143f67,0xbec43de5,0xbf7cbf48,0xbf2bbf72,0x3f723f20,0x3f353f7a,0xbea93e64,0xbf7ebf48,0xbd66bf20,0x3f6d3f08,0x3f2b3f7a,0xbef83de5,
0xbf7abf67,0xbf20,0x3f7a3f20,0x3ede3f67,0xbef83e64,0xbf6dbf72,0x3e8ebede,0x3f7c3f59,0x3f353f80,0xbf350000,0xbf35bf80,0x3f350000,0x3f603e64,0x3ec43f72,0xbf76bede,0xbe2cbf59,
0x3f803f80,0x3f803f80,0x3f2b3f20,0x3f3f3f35,0xbde5be64,0x3de50000,0xbf51bf67,0xbf14bf35,0xbf7abf67,0xbf7abf80,0xbef8be64,0xbf60bf35,0x3ea93f20,0xbea90000,0x3f6d3f80,0x3ec43f35,
0x3f673f20,0x3f673f80,0x3e8ebe64,0x3f763f35,0xbf08bf67,0x3f080000,0xbf7cbf67,0xbe2cbf35,0xbf48be64,0xbf48bf80,0xbd663f20,0xbf80bf35,0x3f353f80,0xbf350000,0x3f803f20,0xbd663f35,
0x3f803f80,0x3f803f80,0x3f513f48,0x3f603f59,0x3ea93e64,0x3f083ede,0xbe8ebede,0x3d66bde5,0xbf48bf67,0xbedebf20,0xbf80bf7a,0xbf51bf72,0xbf59bf20,0xbf7ebf7a,0xbec40000,0xbf6dbf35,
0x3e643f20,0xbf20be64,0x3f3f3f7a,0xbe2c3ea9,0x3f7e3f67,0x3ea93f48,0x3f603ede,0x3f3f3f7e,0x3edebe64,0x3f7a3f67,0xbe2cbf48,0x3f763f08,0xbf35bf80,0x3f350000,0xbf7cbf48,0x3e8ebf08,
0x3f803f80,0x3f803f80,0x3f6d3f67,0x3f763f72,0x3f353f20,0x3f593f48,0x3ec43e64,0x3f2b3f08,0xbe64,0x3ede3e64,0xbec4bf20,0x3e2cbde5,0xbf35bf67,0xbde5bede,0xbf6dbf80,0xbec4bf35,
0xbf80bf67,0xbf20bf67,0xbf6dbf20,0xbf51bf7e,0xbf35be64,0xbf72bf7a,0xbec43e64,0xbf80bf59,0x3f20,0xbf7abf20,0x3ec43f67,0xbf60bea9,0x3f353f80,0xbf350000,0x3f6d3f67,0xbef83ea9,
0x3f803f80,0x3f803f80,0x3f7c3f7a,0x3f803f7e,0x3f723f67,0x3f7e3f7a,0x3f603f48,0x3f7c3f72,0x3f483f20,0x3f7a3f67,0x3f2b3ede,0x3f763f59,0x3f083e64,0x3f723f48,0x3ec40000,0x3f6d3f35,
0x3e64be64,0x3f673f20,0x3d66bede,0x3f603f08,0xbde5bf20,0x3f593ede,0xbe8ebf48,0x3f513ea9,0xbedebf67,0x3f483e64,0xbf14bf7a,0x3f3f3de5,0xbf35bf80,0x3f350000,0xbf51bf7a,0x3f2bbde5,
0x80008000,0x80008000,0x80008000,0x80008000,0xbd668000,0xbe2cbde5,0xbe8ebe64,0xbec4bea9,0xbde58000,0xbea9be64,0xbf08bede,0xbf35bf20,0xbe2c8000,0xbef8bea9,0xbf3fbf20,0xbf6dbf59,
0xbe648000,0xbf20bede,0xbf67bf48,0xbf80bf7a,0xbe8e8000,0xbf3fbf08,0xbf7cbf67,0xbf6dbf7e,0xbea98000,0xbf59bf20,0xbf7ebf7a,0xbf35bf67,0xbec48000,0xbf6dbf35,0xbf6dbf80,0xbec4bf35,
0xbede8000,0xbf7abf48,0xbf48bf7a,0x8000bede,0xbef88000,0xbf80bf59,0xbf14bf67,0x3ec4bde5,0xbf088000,0xbf7ebf67,0xbea9bf48,0x3f353e64,0xbf148000,0xbf76bf72,0xbd66bf20,0x3f6d3f08,
0xbf208000,0xbf67bf7a,0x3e64bede,0x3f803f48,0xbf2b8000,0xbf51bf7e,0x3ef8be64,0x3f6d3f72,0xbf358000,0xbf35bf80,0x3f358000,0x3f353f80,0xbf3f8000,0xbf14bf7e,0x3f603e64,0x3ec43f72,
0x80008000,0x80008000,0x80008000,0x80008000,0xbef8bede,0xbf14bf08,0xbf2bbf20,0xbf3fbf35,0xbf59bf48,0xbf72bf67,0xbf7ebf7a,0xbf7ebf80,0xbf80bf7a,0xbf76bf7e,0xbf51bf67,0xbf14bf35,
0xbf67bf7a,0xbf20bf48,0xbe64bede,0x3e648000,0xbf14bf48,0xbd66bea9,0x3ef83e64,0x3f603f35,0xbde5bede,0x3f083e64,0x3f723f48,0x3f723f80,0x3ec48000,0x3f6d3f35,0x3f6d3f80,0x3ec43f35,
0x3f483ede,0x3f7a3f7a,0x3ede3f48,0xbede8000,0x3f7c3f48,0x3f2b3f72,0xbe8e3e64,0xbf76bf35,0x3f723f7a,0x3de53f20,0xbf59bede,0xbf59bf80,0x3f2b3f7a,0xbef83de5,0xbf7cbf67,0xbe2cbf35,
0x3e643f48,0xbf67bede,0xbf20bf7a,0x3f208000,0xbe8e3ede,0xbf7cbf59,0x3d66bf20,0x3f803f35,0xbf358000,0xbf35bf80,0x3f358000,0x3f353f80,0xbf76bede,0xbe2cbf59,0x3f803f20,0xbd663f35,
0x80008000,0x80008000,0x80008000,0x80008000,0xbf51bf48,0xbf60bf59,0xbf6dbf67,0xbf76bf72,0xbf72bf7a,0xbf59bf67,0xbf35bf48,0xbf08bf20,0xbe8ebede,0x3d66bde5,0x3ec43e64,0x3f2b3f08,
0x3f203ede,0x3f673f48,0x3f803f7a,0x3f673f7a,0x3f803f7a,0x3f513f72,0x3ec43f20,0xbe2c3de5,0x3f083f48,0xbde53e64,0xbf35bede,0xbf7ebf67,0xbec48000,0xbf6dbf35,0xbf6dbf80,0xbec4bf35,
0xbf7abf48,0xbf48bf7a,0x8000bede,0x3f483ede,0xbf3fbf7a,0x3e2cbea9,0x3f6d3f20,0x3f513f7e,0x3de5bede,0x3f723f20,0x3f353f7a,0xbea93e64,0x3f603ede,0x3f3f3f7e,0xbec43e64,0xbf80bf59,
0x3f673f7a,0xbe643ede,0xbf80bf48,0xbe64bf48,0x3e2c3f48,0xbf76bf08,0xbec4bf67,0x3f603ea9,0xbf358000,0xbf35bf80,0x3f358000,0x3f353f80,0xbf7cbf48,0x3e8ebf08,0x3f6d3f67,0xbef83ea9,
0x80008000,0x80008000,0x80008000,0x80008000,0xbf7cbf7a,0xbf80bf7e,0xbf80bf80,0xbf7cbf7e,0xbea9bede,0xbde5be64,0x3de58000,0x3ea93e64,0x3f603f48,0x3f7c3f72,0x3f7c3f80,0x3f603f72,
0x3f203f48,0x3e643ede,0xbe648000,0xbf20bede,0xbf2bbede,0xbf76bf59,0xbf76bf80,0xbf2bbf59,0xbf59bf7a,0xbea9bf20,0x3ea98000,0x3f593f20,0x3ec48000,0x3f6d3f35,0x3f6d3f80,0x3ec43f35,
0x3f7a3f7a,0x3ede3f48,0xbede8000,0xbf7abf48,0xbd663ede,0xbf60bf08,0xbf60bf80,0xbd66bf08,0xbf7ebf48,0xbf08bf67,0x3f088000,0x3f7e3f67,0xbe8ebf48,0x3f513ea9,0x3f513f80,0xbe8e3ea9,
0x3f673ede,0x3f203f7a,0xbf208000,0xbf67bf7a,0x3f143f7a,0xbf3fbde5,0xbf3fbf80,0x3f14bde5,0xbf358000,0xbf35bf80,0x3f358000,0x3f353f80,0xbf51bf7a,0x3f2bbde5,0x3f2b3f80,0xbf51bde5,
0x80008000,0x80008000,0x80008000,0x80008000,0xbf76bf7a,0xbf6dbf72,0xbf60bf67,0xbf51bf59,0x3f083ede,0x3f353f20,0x3f593f48,0x3f723f67,0x3f2b3f48,0x3ec43f08,0x3d663e64,0xbe8ebde5,
0xbf67bf48,0xbf80bf7a,0xbf67bf7a,0xbf20bf48,0xbe2cbede,0x3ec43de5,0x3f513f20,0x3f803f72,0x3f7e3f7a,0x3f353f67,0x3de53ede,0xbf08be64,0xbec48000,0xbf6dbf35,0xbf6dbf80,0xbec4bf35,
0xbf48bf7a,0x8000bede,0x3f483ede,0x3f7a3f7a,0x3f513ede,0x3f6d3f7e,0x3e2c3f20,0xbf3fbea9,0x3ea93f48,0xbf35be64,0xbf72bf7a,0xbde5bf20,0xbf80bf48,0xbec4bf59,0x3f3f3e64,0x3f603f7e,
0x3e64bede,0x3f803f48,0x3e643f48,0xbf67bede,0x3f603f7a,0xbec43ea9,0xbf76bf67,0x3e2cbf08,0xbf358000,0xbf35bf80,0x3f358000,0x3f353f80,0xbef8bf7a,0x3f6d3ea9,0x3e8e3f67,0xbf7cbf08,
0x80008000,0x80008000,0x80008000,0x80008000,0xbf3fbf48,0xbf2bbf35,0xbf14bf20,0xbef8bf08,0x3f7e3f7a,0x3f7e3f80,0x3f723f7a,0x3f593f67,0xbf14bede,0xbf51bf35,0xbf76bf67,0xbf80bf7e,
0xbe64bede,0x3e648000,0x3f203ede,0x3f673f48,0x3f603f7a,0x3ef83f35,0xbd663e64,0xbf14bea9,0xbf72bf48,0xbf72bf80,0xbf08bf48,0x3de5be64,0x3ec48000,0x3f6d3f35,0x3f6d3f80,0x3ec43f35,
0x3ede3f48,0xbede8000,0xbf7abf48,0xbf48bf7a,0xbf76bf7a,0xbe8ebf35,0x3f2b3e64,0x3f7c3f72,0x3f593ede,0x3f593f80,0xbde53ede,0xbf72bf20,0xbe2c3ede,0xbf7cbf35,0xbef8bf67,0x3f2b3de5,
0xbf20bf7a,0x3f208000,0x3f673f7a,0xbe643ede,0x3f803f48,0x3d663f35,0xbf7cbf20,0xbe8ebf59,0xbf358000,0xbf35bf80,0x3f358000,0x3f353f80,0xbd66bf48,0x3f803f35,0xbe2c3f20,0xbf76bf59,
0x80008000,0x80008000,0x80008000,0x80008000,0xbec4bede,0xbe8ebea9,0xbe2cbe64,0xbd66bde5,0x3f353f48,0x3f083f20,0x3ea93ede,0x3de53e64,0xbf6dbf7a,0xbf3fbf59,0xbef8bf20,0xbe2cbea9,
0x3f803f7a,0x3f673f7a,0x3f203f48,0x3e643ede,0xbf6dbf48,0xbf7cbf7e,0xbf3fbf67,0xbe8ebf08,0x3f353ede,0x3f7e3f67,0x3f593f7a,0x3ea93f20,0xbec48000,0xbf6dbf35,0xbf6dbf80,0xbec4bf35,
0x8000bede,0x3f483ede,0x3f7a3f7a,0x3ede3f48,0x3ec43f48,0xbf14bde5,0xbf80bf67,0xbef8bf59,0xbf35bf7a,0x3ea9be64,0x3f7e3f48,0x3f083f67,0x3f6d3f7a,0xbd663f08,0xbf76bf20,0xbf14bf72,
0xbf80bf48,0xbe64bf48,0x3f673ede,0x3f203f7a,0x3f6d3ede,0x3ef83f72,0xbf51be64,0xbf2bbf7e,0xbf358000,0xbf35bf80,0x3f358000,0x3f353f80,0x3ec4bede,0x3f603f72,0xbf143e64,0xbf3fbf7e,
0x80008000,0x80008000,0x80008000,0x80008000,0x3d668000,0x3e2c3de5,0x3e8e3e64,0x3ec43ea9,0xbde58000,0xbea9be64,0xbf08bede,0xbf35bf20,0x3e2c8000,0x3ef83ea9,0x3f3f3f20,0x3f6d3f59,
0xbe648000,0xbf20bede,0xbf67bf48,0xbf80bf7a,0x3e8e8000,0x3f3f3f08,0x3f7c3f67,0x3f6d3f7e,0xbea98000,0xbf59bf20,0xbf7ebf7a,0xbf35bf67,0x3ec48000,0x3f6d3f35,0x3f6d3f80,0x3ec43f35,
0xbede8000,0xbf7abf48,0xbf48bf7a,0x8000bede,0x3ef88000,0x3f803f59,0x3f143f67,0xbec43de5,0xbf088000,0xbf7ebf67,0xbea9bf48,0x3f353e64,0x3f148000,0x3f763f72,0x3d663f20,0xbf6dbf08,
0xbf208000,0xbf67bf7a,0x3e64bede,0x3f803f48,0x3f2b8000,0x3f513f7e,0xbef83e64,0xbf6dbf72,0xbf358000,0xbf35bf80,0x3f358000,0x3f353f80,0x3f3f8000,0x3f143f7e,0xbf60be64,0xbec4bf72,
0x80008000,0x80008000,0x80008000,0x80008000,0x3ef83ede,0x3f143f08,0x3f2b3f20,0x3f3f3f35,0xbf59bf48,0xbf72bf67,0xbf7ebf7a,0xbf7ebf80,0x3f803f7a,0x3f763f7e,0x3f513f67,0x3f143f35,
0xbf67bf7a,0xbf20bf48,0xbe64bede,0x3e648000,0x3f143f48,0x3d663ea9,0xbef8be64,0xbf60bf35,0xbde5bede,0x3f083e64,0x3f723f48,0x3f723f80,0xbec48000,0xbf6dbf35,0xbf6dbf80,0xbec4bf35,
0x3f483ede,0x3f7a3f7a,0x3ede3f48,0xbede8000,0xbf7cbf48,0xbf2bbf72,0x3e8ebe64,0x3f763f35,0x3f723f7a,0x3de53f20,0xbf59bede,0xbf59bf80,0xbf2bbf7a,0x3ef8bde5,0x3f7c3f67,0x3e2c3f35,
0x3e643f48,0xbf67bede,0xbf20bf7a,0x3f208000,0x3e8ebede,0x3f7c3f59,0xbd663f20,0xbf80bf35,0xbf358000,0xbf35bf80,0x3f358000,0x3f353f80,0x3f763ede,0x3e2c3f59,0xbf80bf20,0x3d66bf35,
0x80008000,0x80008000,0x80008000,0x80008000,0x3f513f48,0x3f603f59,0x3f6d3f67,0x3f763f72,0xbf72bf7a,0xbf59bf67,0xbf35bf48,0xbf08bf20,0x3e8e3ede,0xbd663de5,0xbec4be64,0xbf2bbf08,
0x3f203ede,0x3f673f48,0x3f803f7a,0x3f673f7a,0xbf80bf7a,0xbf51bf72,0xbec4bf20,0x3e2cbde5,0x3f083f48,0xbde53e64,0xbf35bede,0xbf7ebf67,0x3ec48000,0x3f6d3f35,0x3f6d3f80,0x3ec43f35,
0xbf7abf48,0xbf48bf7a,0x8000bede,0x3f483ede,0x3f3f3f7a,0xbe2c3ea9,0xbf6dbf20,0xbf51bf7e,0x3de5bede,0x3f723f20,0x3f353f7a,0xbea93e64,0xbf60bede,0xbf3fbf7e,0x3ec4be64,0x3f803f59,
0x3f673f7a,0xbe643ede,0xbf80bf48,0xbe64bf48,0xbe2cbf48,0x3f763f08,0x3ec43f67,0xbf60bea9,0xbf358000,0xbf35bf80,0x3f358000,0x3f353f80,0x3f7c3f48,0xbe8e3f08,0xbf6dbf67,0x3ef8bea9,
0x80008000,0x80008000,0x80008000,0x80008000,0x3f7c3f7a,0x3f803f7e,0x3f803f80,0x3f7c3f7e,0xbea9bede,0xbde5be64,0x3de58000,0x3ea93e64,0xbf60bf48,0xbf7cbf72,0xbf7cbf80,0xbf60bf72,
0x3f203f48,0x3e643ede,0xbe648000,0xbf20bede,0x3f2b3ede,0x3f763f59,0x3f763f80,0x3f2b3f59,0xbf59bf7a,0xbea9bf20,0x3ea98000,0x3f593f20,0xbec48000,0xbf6dbf35,0xbf6dbf80,0xbec4bf35,
0x3f7a3f7a,0x3ede3f48,0xbede8000,0xbf7abf48,0x3d66bede,0x3f603f08,0x3f603f80,0x3d663f08,0xbf7ebf48,0xbf08bf67,0x3f088000,0x3f7e3f67,0x3e8e3f48,0xbf51bea9,0xbf51bf80,0x3e8ebea9,
0x3f673ede,0x3f203f7a,0xbf208000,0xbf67bf7a,0xbf14bf7a,0x3f3f3de5,0x3f3f3f80,0xbf143de5,0xbf358000,0xbf35bf80,0x3f358000,0x3f353f80,0x3f513f7a,0xbf2b3de5,0xbf2bbf80,0x3f513de5,
0x80008000,0x80008000,0x80008000,0x80008000,0x3f763f7a,0x3f6d3f72,0x3f603f67,0x3f513f59,0x3f083ede,0x3f353f20,0x3f593f48,0x3f723f67,0xbf2bbf48,0xbec4bf08,0xbd66be64,0x3e8e3de5,
0xbf67bf48,0xbf80bf7a,0xbf67bf7a,0xbf20bf48,0x3e2c3ede,0xbec4bde5,0xbf51bf20,0xbf80bf72,0x3f7e3f7a,0x3f353f67,0x3de53ede,0xbf08be64,0x3ec48000,0x3f6d3f35,0x3f6d3f80,0x3ec43f35,
0xbf48bf7a,0x8000bede,0x3f483ede,0x3f7a3f7a,0xbf51bede,0xbf6dbf7e,0xbe2cbf20,0x3f3f3ea9,0x3ea93f48,0xbf35be64,0xbf72bf7a,0xbde5bf20,0x3f803f48,0x3ec43f59,0xbf3fbe64,0xbf60bf7e,
0x3e64bede,0x3f803f48,0x3e643f48,0xbf67bede,0xbf60bf7a,0x3ec4bea9,0x3f763f67,0xbe2c3f08,0xbf358000,0xbf35bf80,0x3f358000,0x3f353f80,0x3ef83f7a,0xbf6dbea9,0xbe8ebf67,0x3f7c3f08,
0x80008000,0x80008000,0x3f3f3f48,0x3f2b3f35,0x3f7e3f7a,0x3f7e3f80,0x3f143ede,0x3f513f35,0xbe64bede,0x3e648000,0xbf60bf7a,0xbef8bf35,0xbf72bf48,0xbf72bf80,0xbec48000,0xbf6dbf35,
0x3ede3f48,0xbede8000,0x3f763f7a,0x3e8e3f35,0x3f593ede,0x3f593f80,0x3e2cbede,0x3f7c3f35,0xbf20bf7a,0x3f208000,0xbf80bf48,0xbd66bf35,0xbf358000,0xbf35bf80,0x3d663f48,0xbf80bf35,
0x80008000,0x80008000,0x3f143f20,0x3ef83f08,0x3f723f7a,0x3f593f67,0x3f763f67,0x3f803f7e,0x3f203ede,0x3f673f48,0x3d66be64,0x3f143ea9,0xbf08bf48,0x3de5be64,0xbf6dbf80,0xbec4bf35,
0xbf7abf48,0xbf48bf7a,0xbf2bbe64,0xbf7cbf72,0xbde53ede,0xbf72bf20,0x3ef83f67,0xbf2bbde5,0x3f673f7a,0xbe643ede,0x3f7c3f20,0x3e8e3f59,0x3f358000,0x3f353f80,0x3e2cbf20,0x3f763f59,
0x80008000,0x80008000,0x3ec43ede,0x3e8e3ea9,0x3f353f48,0x3f083f20,0x3f6d3f7a,0x3f3f3f59,0x3f803f7a,0x3f673f7a,0x3f6d3f48,0x3f7c3f7e,0x3f353ede,0x3f7e3f67,0x3ec48000,0x3f6d3f35,
0x8000bede,0x3f483ede,0xbec4bf48,0x3f143de5,0xbf35bf7a,0x3ea9be64,0xbf6dbf7a,0x3d66bf08,0xbf80bf48,0xbe64bf48,0xbf6dbede,0xbef8bf72,0xbf358000,0xbf35bf80,0xbec43ede,0xbf60bf72,
0x80008000,0x80008000,0x3e2c3e64,0x3d663de5,0x3ea93ede,0x3de53e64,0x3ef83f20,0x3e2c3ea9,0x3f203f48,0x3e643ede,0x3f3f3f67,0x3e8e3f08,0x3f593f7a,0x3ea93f20,0x3f6d3f80,0x3ec43f35,
0x3f7a3f7a,0x3ede3f48,0x3f803f67,0x3ef83f59,0x3f7e3f48,0x3f083f67,0x3f763f20,0x3f143f72,0x3f673ede,0x3f203f7a,0x3f513e64,0x3f2b3f7e,0x3f358000,0x3f353f80,0x3f14be64,0x3f3f3f7e,
0x3f203f80,0xbf67be64,0xbe64bf67,0x3f803f20,0x3f143f80,0xbf76bea9,0x3d66bf48,0x3f6d3f59,0x3f083f80,0xbf7ebede,0x3ea9bf20,0x3f353f7a,0x3ef83f80,0xbf80bf08,0x3f14bede,0x3ec43f7e,
0x3ede3f80,0xbf7abf20,0x3f48be64,0x3f67,0x3ec43f80,0xbf6dbf35,0x3f6d0000,0xbec43f35,0x3ea93f80,0xbf59bf48,0x3f7e3e64,0xbf353ede,0x3e8e3f80,0xbf3fbf59,0x3f7c3ede,0xbf6d3de5,
0x3e643f80,0xbf20bf67,0x3f673f20,0xbf80be64,0x3e2c3f80,0xbef8bf72,0x3f3f3f48,0xbf6dbf08,0x3de53f80,0xbea9bf7a,0x3f083f67,0xbf35bf48,0x3d663f80,0xbe2cbf7e,0x3e8e3f7a,0xbec4bf72,
0x3f80,0xbf80,0x3f80,0xbf80,0xbd663f80,0x3e2cbf7e,0xbe8e3f7a,0x3ec4bf72,0xbde53f80,0x3ea9bf7a,0xbf083f67,0x3f35bf48,0xbe2c3f80,0x3ef8bf72,0xbf3f3f48,0x3f6dbf08,
0xbe643f20,0xbf67bf67,0x3f20be64,0x3f203f80,0xbf2b3e64,0xbef8bf7e,0x3f7c3ede,0xbe2c3f35,0xbf72be64,0x3de5bf48,0x3f593f67,0xbf590000,0xbf7cbf20,0x3f2bbea9,0x3e8e3f7a,0xbf76bf35,
0xbf48bf67,0x3f7a3e64,0xbede3f20,0xbedebf80,0xbec4bf80,0x3f6d3f35,0xbf6d0000,0x3ec4bf35,0x3de5bf67,0x3f083f7a,0xbf72bf20,0x3f720000,0x3f14bf20,0xbd663f72,0xbef8bf7a,0x3f603f35,
0x3f67be64,0xbf203f20,0x3e64bf67,0x3e643f80,0x3f803e64,0xbf763de5,0x3f51bede,0xbf143f35,0x3f593f20,0xbf72bede,0x3f7e3e64,0xbf7e0000,0x3ef83f67,0xbf14bf59,0x3f2b3f48,0xbf3fbf35,
0x3f80,0xbf80,0x3f80,0xbf80,0xbef83f67,0x3f14bf59,0xbf2b3f48,0x3f3fbf35,0xbf593f20,0x3f72bede,0xbf7e3e64,0x3f7e0000,0xbf803e64,0x3f763de5,0xbf51bede,0x3f143f35,
0xbf67be64,0xbe64bf67,0x3f803f20,0xbe643f20,0xbf60bf67,0x3f3fbde5,0x3ec43f7a,0xbf80bf08,0xbde5bf67,0x3f723f48,0xbf353e64,0xbea9bf7a,0x3f3fbe64,0x3e2c3f72,0xbf6dbf48,0x3f51bde5,
0x3f7a3f20,0xbf483e64,0xbf67,0x3f483f67,0x3ec43f80,0xbf6dbf35,0x3f6d0000,0xbec43f35,0xbf083f20,0xbde5bf7a,0x3f353f67,0xbf7ebede,0xbf80be64,0x3f51bea9,0xbec43f48,0xbe2cbf7e,
0xbf20bf67,0x3f673f20,0xbf80be64,0x3f67be64,0x3e8ebf67,0x3d663f7e,0xbec4bf7a,0x3f2b3f59,0x3f72be64,0xbf593ede,0x3f35bf20,0xbf083f48,0x3f513f20,0xbf60bf08,0x3f6d3ede,0xbf76bea9,
0x3f80,0xbf80,0x3f80,0xbf80,0xbf513f20,0x3f60bf08,0xbf6d3ede,0x3f76bea9,0xbf72be64,0x3f593ede,0xbf35bf20,0x3f083f48,0xbe8ebf67,0xbd663f7e,0x3ec4bf7a,0xbf2b3f59,
0xbf67bf67,0x3f20be64,0x3f203f80,0xbf67be64,0x3e8ebf20,0x3f513f72,0xbf510000,0xbe8ebf72,0x3f7e3f20,0xbf083ede,0xbf08bf80,0x3f7e3ede,0x3d663f67,0xbf60bf59,0x3f600000,0xbd663f59,
0xbf7abe64,0x3edebf20,0x3ede3f80,0xbf7abf20,0xbec4bf80,0x3f6d3f35,0xbf6d0000,0x3ec4bf35,0x3f59be64,0xbea93f48,0xbea9bf80,0x3f593f48,0x3f2b3f67,0xbf76bf08,0x3f760000,0xbf2b3f08,
0xbf203f20,0x3e64bf67,0x3e643f80,0xbf20bf67,0xbf60bf20,0x3f7c3ea9,0xbf7c0000,0x3f60bea9,0x3ea9bf67,0xbde53f7a,0xbde5bf80,0x3ea93f7a,0x3f7c3e64,0xbf80bde5,0x3f800000,0xbf7c3de5,
0x3f80,0xbf80,0x3f80,0xbf80,0xbf7c3e64,0x3f80bde5,0xbf800000,0x3f7c3de5,0xbea9bf67,0x3de53f7a,0x3de5bf80,0xbea93f7a,0x3f60bf20,0xbf7c3ea9,0x3f7c0000,0xbf60bea9,
0xbe64bf67,0x3f803f20,0xbe643f20,0xbf67bf67,0x3f803f20,0xbec43f08,0xbf3fbf7a,0x3f603de5,0xbea93f20,0xbf35bf7a,0x3f723e64,0xbde53f48,0xbf51bf67,0x3f6d3de5,0xbe2c3f48,0xbf3fbf72,
0x3f48be64,0x3f67,0xbf48bf67,0x3f7a3e64,0x3ec43f80,0xbf6dbf35,0x3f6d0000,0xbec43f35,0xbf7ebe64,0x3f35bede,0xbde53f67,0xbf08bf7a,0x3e2cbf67,0x3ec43f7e,0xbf51bf48,0x3f803ea9,
0x3f673f20,0xbf80be64,0x3f67be64,0xbf203f20,0xbf2b3f20,0x3ec4bf59,0xbd663f7a,0xbe8ebf7e,0xbf08bf67,0x3f353f48,0xbf59bf20,0x3f723ede,0x3f76be64,0xbf6d3ea9,0x3f60bede,0xbf513f08,
0x3f80,0xbf80,0x3f80,0xbf80,0xbf76be64,0x3f6d3ea9,0xbf60bede,0x3f513f08,0x3f08bf67,0xbf353f48,0x3f59bf20,0xbf723ede,0x3f2b3f20,0xbec4bf59,0x3d663f7a,0x3e8ebf7e,
0x3f20be64,0x3f203f80,0xbf67be64,0xbe64bf67,0x3e2c3f67,0xbf7cbf35,0x3ef8bede,0x3f2b3f7e,0xbf59bf67,0x3f590000,0x3de53f67,0xbf72bf48,0x3f763e64,0xbe8e3f35,0xbf2bbf7a,0x3f7c3ea9,
0xbede3f20,0xbedebf80,0x3f7a3f20,0xbf483e64,0xbec4bf80,0x3f6d3f35,0xbf6d0000,0x3ec4bf35,0x3f723f20,0xbf720000,0x3f08bf20,0x3de53f7a,0xbf603e64,0x3ef8bf35,0x3d663f7a,0xbf14bf72,
0x3e64bf67,0x3e643f80,0xbf20bf67,0x3f673f20,0x3f143f67,0xbf51bf35,0x3f763ede,0xbf80bde5,0xbf7ebe64,0x3f7e0000,0xbf723e64,0x3f59bede,0x3f3fbf20,0xbf2b3f35,0x3f14bf48,0xbef83f59,
0x3f80,0xbf80,0x3f80,0xbf80,0xbf3fbf20,0x3f2b3f35,0xbf14bf48,0x3ef83f59,0x3f7ebe64,0xbf7e0000,0x3f723e64,0xbf59bede,0xbf143f67,0x3f51bf35,0xbf763ede,0x3f80bde5,
0x3f803f20,0xbe643f20,0xbf67bf67,0x3f20be64,0xbf6dbe64,0xbd66bf59,0x3f763f48,0xbf143ea9,0x3f35be64,0x3ea93f7a,0xbf7ebf20,0x3f08bede,0xbec43f20,0xbf14bf7e,0x3f803ede,0xbef83f08,
0xbf67,0x3f483f67,0xbf7abe64,0x3edebf20,0x3ec43f80,0xbf6dbf35,0x3f6d0000,0xbec43f35,0xbf35bf67,0x3f7e3ede,0xbf593e64,0x3ea9bf48,0x3f6d3f20,0xbf7cbde5,0x3f3fbede,0xbe8e3f59,
0xbf80be64,0x3f67be64,0xbf203f20,0x3e64bf67,0x3f6dbe64,0xbf3f3f08,0x3ef8bf48,0xbe2c3f72,0xbf353f20,0x3f08bf48,0xbea93f67,0x3de5bf7a,0x3ec4bf67,0xbe8e3f72,0x3e2cbf7a,0xbd663f7e,
0x3f80,0xbf80,0x3f80,0xbf80,0xbec4bf67,0x3e8e3f72,0xbe2cbf7a,0x3d663f7e,0x3f353f20,0xbf08bf48,0x3ea93f67,0xbde5bf7a,0xbf6dbe64,0x3f3f3f08,0xbef8bf48,0x3e2c3f72,
0x3f203f80,0xbf67be64,0xbe64bf67,0x3f803f20,0xbf14bf80,0x3f763ea9,0xbd663f48,0xbf6dbf59,0x3f083f80,0xbf7ebede,0x3ea9bf20,0x3f353f7a,0xbef8bf80,0x3f803f08,0xbf143ede,0xbec4bf7e,
0x3ede3f80,0xbf7abf20,0x3f48be64,0x3f67,0xbec4bf80,0x3f6d3f35,0xbf6d0000,0x3ec4bf35,0x3ea93f80,0xbf59bf48,0x3f7e3e64,0xbf353ede,0xbe8ebf80,0x3f3f3f59,0xbf7cbede,0x3f6dbde5,
0x3e643f80,0xbf20bf67,0x3f673f20,0xbf80be64,0xbe2cbf80,0x3ef83f72,0xbf3fbf48,0x3f6d3f08,0x3de53f80,0xbea9bf7a,0x3f083f67,0xbf35bf48,0xbd66bf80,0x3e2c3f7e,0xbe8ebf7a,0x3ec43f72,
0x3f80,0xbf80,0x3f80,0xbf80,0x3d66bf80,0xbe2c3f7e,0x3e8ebf7a,0xbec43f72,0xbde53f80,0x3ea9bf7a,0xbf083f67,0x3f35bf48,0x3e2cbf80,0xbef83f72,0x3f3fbf48,0xbf6d3f08,
0xbe643f20,0xbf67bf67,0x3f20be64,0x3f203f80,0x3f2bbe64,0x3ef83f7e,0xbf7cbede,0x3e2cbf35,0xbf72be64,0x3de5bf48,0x3f593f67,0xbf590000,0x3f7c3f20,0xbf2b3ea9,0xbe8ebf7a,0x3f763f35,
0xbf48bf67,0x3f7a3e64,0xbede3f20,0xbedebf80,0x3ec43f80,0xbf6dbf35,0x3f6d0000,0xbec43f35,0x3de5bf67,0x3f083f7a,0xbf72bf20,0x3f720000,0xbf143f20,0x3d66bf72,0x3ef83f7a,0xbf60bf35,
0x3f67be64,0xbf203f20,0x3e64bf67,0x3e643f80,0xbf80be64,0x3f76bde5,0xbf513ede,0x3f14bf35,0x3f593f20,0xbf72bede,0x3f7e3e64,0xbf7e0000,0xbef8bf67,0x3f143f59,0xbf2bbf48,0x3f3f3f35,
0x3f80,0xbf80,0x3f80,0xbf80,0x3ef8bf67,0xbf143f59,0x3f2bbf48,0xbf3f3f35,0xbf593f20,0x3f72bede,0xbf7e3e64,0x3f7e0000,0x3f80be64,0xbf76bde5,0x3f513ede,0xbf14bf35,
0xbf67be64,0xbe64bf67,0x3f803f20,0xbe643f20,0x3f603f67,0xbf3f3de5,0xbec4bf7a,0x3f803f08,0xbde5bf67,0x3f723f48,0xbf353e64,0xbea9bf7a,0xbf3f3e64,0xbe2cbf72,0x3f6d3f48,0xbf513de5,
0x3f7a3f20,0xbf483e64,0xbf67,0x3f483f67,0xbec4bf80,0x3f6d3f35,0xbf6d0000,0x3ec4bf35,0xbf083f20,0xbde5bf7a,0x3f353f67,0xbf7ebede,0x3f803e64,0xbf513ea9,0x3ec4bf48,0x3e2c3f7e,
0xbf20bf67,0x3f673f20,0xbf80be64,0x3f67be64,0xbe8e3f67,0xbd66bf7e,0x3ec43f7a,0xbf2bbf59,0x3f72be64,0xbf593ede,0x3f35bf20,0xbf083f48,0xbf51bf20,0x3f603f08,0xbf6dbede,0x3f763ea9,
0x3f80,0xbf80,0x3f80,0xbf80,0x3f51bf20,0xbf603f08,0x3f6dbede,0xbf763ea9,0xbf72be64,0x3f593ede,0xbf35bf20,0x3f083f48,0x3e8e3f67,0x3d66bf7e,0xbec43f7a,0x3f2bbf59,
0xbf67bf67,0x3f20be64,0x3f203f80,0xbf67be64,0xbe8e3f20,0xbf51bf72,0x3f510000,0x3e8e3f72,0x3f7e3f20,0xbf083ede,0xbf08bf80,0x3f7e3ede,0xbd66bf67,0x3f603f59,0xbf600000,0x3d66bf59,
0xbf7abe64,0x3edebf20,0x3ede3f80,0xbf7abf20,0x3ec43f80,0xbf6dbf35,0x3f6d0000,0xbec43f35,0x3f59be64,0xbea93f48,0xbea9bf80,0x3f593f48,0xbf2bbf67,0x3f763f08,0xbf760000,0x3f2bbf08,
0xbf203f20,0x3e64bf67,0x3e643f80,0xbf20bf67,0x3f603f20,0xbf7cbea9,0x3f7c0000,0xbf603ea9,0x3ea9bf67,0xbde53f7a,0xbde5bf80,0x3ea93f7a,0xbf7cbe64,0x3f803de5,0xbf800000,0x3f7cbde5,
0x3f80,0xbf80,0x3f80,0xbf80,0x3f7cbe64,0xbf803de5,0x3f800000,0xbf7cbde5,0xbea9bf67,0x3de53f7a,0x3de5bf80,0xbea93f7a,0xbf603f20,0x3f7cbea9,0xbf7c0000,0x3f603ea9,
0xbe64bf67,0x3f803f20,0xbe643f20,0xbf67bf67,0xbf80bf20,0x3ec4bf08,0x3f3f3f7a,0xbf60bde5,0xbea93f20,0xbf35bf7a,0x3f723e64,0xbde53f48,0x3f513f67,0xbf6dbde5,0x3e2cbf48,0x3f3f3f72,
0x3f48be64,0x3f67,0xbf48bf67,0x3f7a3e64,0xbec4bf80,0x3f6d3f35,0xbf6d0000,0x3ec4bf35,0xbf7ebe64,0x3f35bede,0xbde53f67,0xbf08bf7a,0xbe2c3f67,0xbec4bf7e,0x3f513f48,0xbf80bea9,
0x3f673f20,0xbf80be64,0x3f67be64,0xbf203f20,0x3f2bbf20,0xbec43f59,0x3d66bf7a,0x3e8e3f7e,0xbf08bf67,0x3f353f48,0xbf59bf20,0x3f723ede,0xbf763e64,0x3f6dbea9,0xbf603ede,0x3f51bf08,
0x3f80,0xbf80,0x3f80,0xbf80,0x3f763e64,0xbf6dbea9,0x3f603ede,0xbf51bf08,0x3f08bf67,0xbf353f48,0x3f59bf20,0xbf723ede,0xbf2bbf20,0x3ec43f59,0xbd66bf7a,0xbe8e3f7e,
0x3f20be64,0x3f203f80,0xbe2cbf67,0x3f7c3f35,0xbf59bf67,0x3f590000,0xbf76be64,0x3e8ebf35,0xbede3f20,0xbedebf80,0x3ec43f80,0xbf6dbf35,0x3f723f20,0xbf720000,0x3f60be64,0xbef83f35,
0x3e64bf67,0x3e643f80,0xbf14bf67,0x3f513f35,0xbf7ebe64,0x3f7e0000,0xbf3f3f20,0x3f2bbf35,0x3f80,0xbf80,0x3f3f3f20,0xbf2bbf35,0x3f7ebe64,0xbf7e0000,0x3f14bf67,0xbf513f35,
0xbf67be64,0xbe64bf67,0xbef83ede,0xbf2bbf7e,0x3de53f67,0xbf72bf48,0x3f2b3f7a,0xbf7cbea9,0x3f7a3f20,0xbf483e64,0x3f6d0000,0xbec43f35,0x3f08bf20,0x3de53f7a,0xbd66bf7a,0x3f143f72,
0xbf20bf67,0x3f673f20,0xbf76bede,0x3f803de5,0xbf723e64,0x3f59bede,0xbf143f48,0x3ef8bf59,0x3f80,0xbf80,0x3f143f48,0xbef8bf59,0x3f723e64,0xbf59bede,0x3f76bede,0xbf803de5,
0x3f803f20,0xbe643f20,0x3f6d3e64,0x3d663f59,0x3f35be64,0x3ea93f7a,0x3ec4bf20,0x3f143f7e,0xbf67,0x3f483f67,0xbec4bf80,0x3f6d3f35,0xbf35bf67,0x3f7e3ede,0xbf6dbf20,0x3f7c3de5,
0xbf80be64,0x3f67be64,0xbf6d3e64,0x3f3fbf08,0xbf353f20,0x3f08bf48,0xbec43f67,0x3e8ebf72,0x3f80,0xbf80,0x3ec43f67,0xbe8ebf72,0x3f353f20,0xbf08bf48,0x3f6d3e64,0xbf3fbf08,
0xbf67bf67,0x3f20be64,0xbf76bf48,0x3f14bea9,0xbf7ebf20,0x3f08bede,0xbf80bede,0x3ef8bf08,0xbf7abe64,0x3edebf20,0xbf6d0000,0x3ec4bf35,0xbf593e64,0x3ea9bf48,0xbf3f3ede,0x3e8ebf59,
0xbf203f20,0x3e64bf67,0xbef83f48,0x3e2cbf72,0xbea93f67,0x3de5bf7a,0xbe2c3f7a,0x3d66bf7e,0x3f80,0xbf80,0x3e2c3f7a,0xbd66bf7e,0x3ea93f67,0xbde5bf7a,0x3ef83f48,0xbe2cbf72,
0xbf488000,0xbedebf7a,0x3f7a3ede,0x80003f48,0xbf518000,0xbe8ebf72,0x3f803f20,0xbec43f08,0xbf598000,0xbde5bf67,0x3f723f48,0xbf353e64,0xbf608000,0x3d66bf59,0x3f513f67,0xbf6dbde5,
0xbf678000,0x3e64bf48,0x3f203f7a,0xbf80bede,0xbf6d8000,0x3ec4bf35,0x3ec43f80,0xbf6dbf35,0xbf728000,0x3f08bf20,0x3de53f7a,0xbf35bf67,0xbf768000,0x3f2bbf08,0xbe2c3f67,0xbec4bf7e,
0xbf7a8000,0x3f48bede,0xbede3f48,0x8000bf7a,0xbf7c8000,0x3f60bea9,0xbf2b3f20,0x3ec4bf59,0xbf7e8000,0x3f72be64,0xbf593ede,0x3f35bf20,0xbf808000,0x3f7cbde5,0xbf763e64,0x3f6dbea9,
0xbf808000,0x3f808000,0xbf808000,0x3f808000,0xbf808000,0x3f7c3de5,0xbf76be64,0x3f6d3ea9,0xbf7e8000,0x3f723e64,0xbf59bede,0x3f353f20,0xbf7c8000,0x3f603ea9,0xbf2bbf20,0x3ec43f59,
0xbf7abf48,0x3edebede,0x3f483f7a,0xbf488000,0xbf3fbf7a,0x3f603de5,0x3e2c3f67,0xbf7cbf35,0xbea9bf7a,0x3f7e3f20,0xbf083ede,0xbf08bf80,0x3e2cbf48,0x3f3f3f72,0xbf76be64,0x3e8ebf35,
0x3f20bede,0x3e643f7a,0xbf67bf48,0x3f678000,0x3f6d8000,0xbec43f35,0xbec4bf80,0x3f6d3f35,0x3f7e3ede,0xbf593e64,0x3ea9bf48,0x3ea93f80,0x3f513f48,0xbf80bea9,0x3f60be64,0xbef83f35,
0x3ede3f7a,0xbf48bf48,0x3f7a3ede,0xbf7a8000,0xbd663f7a,0xbe8ebf7e,0x3f143f67,0xbf51bf35,0xbf083f48,0x3ea9bf67,0xbde53f7a,0xbde5bf80,0xbf603ede,0x3f51bf08,0xbf3f3f20,0x3f2bbf35,
0xbf808000,0x3f808000,0xbf808000,0x3f808000,0xbf60bede,0x3f513f08,0xbf3fbf20,0x3f2b3f35,0xbf08bf48,0x3ea93f67,0xbde5bf7a,0xbde53f80,0xbd66bf7a,0xbe8e3f7e,0x3f14bf67,0xbf513f35,
0xbedebf7a,0x3f7a3ede,0x80003f48,0xbf7abf48,0x3ef8bede,0x3f2b3f7e,0xbf6dbe64,0xbd66bf59,0x3f7e3ede,0xbea93f20,0xbf35bf7a,0x3f723e64,0x3f2b3f7a,0xbf7cbea9,0x3ec4bf20,0x3f143f7e,
0xbe643f48,0xbf20bf7a,0x3f803ede,0xbf203ede,0xbf6d8000,0x3ec4bf35,0x3ec43f80,0xbf6dbf35,0xbf59bf48,0x3f7e3e64,0xbf353ede,0x3de5bf67,0xbd66bf7a,0x3f143f72,0xbf6dbf20,0x3f7c3de5,
0x3f48bede,0xbede3f48,0x8000bf7a,0x3ede3f7a,0x3f763ede,0xbf80bde5,0x3f6dbe64,0xbf3f3f08,0x3ea93f7a,0xbf08bf67,0x3f353f48,0xbf59bf20,0xbf143f48,0x3ef8bf59,0xbec43f67,0x3e8ebf72,
0xbf808000,0x3f808000,0xbf808000,0x3f808000,0xbf14bf48,0x3ef83f59,0xbec4bf67,0x3e8e3f72,0x3ea9bf7a,0xbf083f67,0x3f35bf48,0xbf593f20,0x3f76bede,0xbf803de5,0x3f6d3e64,0xbf3fbf08,
0x3edebede,0x3f483f7a,0xbf488000,0xbedebf7a,0x3f763f48,0xbf143ea9,0xbf14bf80,0x3f763ea9,0xbde53f48,0xbf59bf67,0x3f598000,0x3de53f67,0xbf80bede,0x3ef8bf08,0x3ef83f80,0xbf80bf08,
0xbe64bf7a,0x3f673f48,0xbf678000,0x3e64bf48,0x3f6d8000,0xbec43f35,0xbec4bf80,0x3f6d3f35,0x3f083f7a,0xbf72bf20,0x3f728000,0xbf083f20,0xbf3f3ede,0x3e8ebf59,0x3e8e3f80,0xbf3fbf59,
0xbf48bf48,0x3f7a3ede,0xbf7a8000,0x3f48bede,0x3ef8bf48,0xbe2c3f72,0xbe2cbf80,0x3ef83f72,0x3f723ede,0xbf7ebe64,0x3f7e8000,0xbf723e64,0xbe2c3f7a,0x3d66bf7e,0x3d663f80,0xbe2cbf7e,
0xbf808000,0x3f808000,0xbf808000,0x3f808000,0xbe2cbf7a,0x3d663f7e,0x3d66bf80,0xbe2c3f7e,0x3f72bede,0xbf7e3e64,0x3f7e8000,0xbf72be64,0x3ef83f48,0xbe2cbf72,0xbe2c3f80,0x3ef8bf72,
0x3f7a3ede,0x80003f48,0xbf7abf48,0x3edebede,0xbd663f48,0xbf6dbf59,0x3f2bbe64,0x3ef83f7e,0xbf72bf48,0x3f35be64,0x3ea93f7a,0xbf7ebf20,0x3f14bede,0x3ec43f7e,0xbf7cbf20,0x3f2bbea9,
0x3f203f7a,0xbf80bede,0x3f20bede,0x3e643f7a,0xbf6d8000,0x3ec4bf35,0x3ec43f80,0xbf6dbf35,0xbde5bf7a,0x3f353f67,0xbf7ebede,0x3f59be64,0x3f7c3ede,0xbf6d3de5,0x3f14bf20,0xbd663f72,
0xbede3f48,0x8000bf7a,0x3ede3f7a,0xbf48bf48,0xbf3fbf48,0x3f6d3f08,0xbf80be64,0x3f76bde5,0x3f59bede,0xbf353f20,0x3f08bf48,0xbea93f67,0x3e8e3f7a,0xbec4bf72,0x3ef83f67,0xbf14bf59,
0xbf808000,0x3f808000,0xbf808000,0x3f808000,0x3e8ebf7a,0xbec43f72,0x3ef8bf67,0xbf143f59,0x3f593ede,0xbf35bf20,0x3f083f48,0xbea9bf67,0xbf3f3f48,0x3f6dbf08,0xbf803e64,0x3f763de5,
0x3f483f7a,0xbf488000,0xbedebf7a,0x3f7a3ede,0xbf7cbede,0x3e2cbf35,0x3f603f67,0xbf3f3de5,0x3f08bede,0x3f083f80,0xbf7ebede,0x3ea9bf20,0x3e8e3f7a,0xbf76bf35,0x3f3fbe64,0x3e2c3f72,
0xbf67bf48,0x3f678000,0xbe643f48,0xbf20bf7a,0x3f6d8000,0xbec43f35,0xbec4bf80,0x3f6d3f35,0xbea93f48,0xbea9bf80,0x3f593f48,0xbf7ebe64,0xbef8bf7a,0x3f603f35,0xbf80be64,0x3f51bea9,
0x3f7a3ede,0xbf7a8000,0x3f48bede,0xbede3f48,0xbf513ede,0x3f14bf35,0xbe8e3f67,0xbd66bf7e,0x3de5bf7a,0x3de53f80,0xbea9bf7a,0x3f083f67,0x3f2b3f48,0xbf3fbf35,0x3f513f20,0xbf60bf08,
0xbf808000,0x3f808000,0xbf808000,0x3f808000,0x3f2bbf48,0xbf3f3f35,0x3f51bf20,0xbf603f08,0x3de53f7a,0x3de5bf80,0xbea93f7a,0x3f08bf67,0xbf51bede,0x3f143f35,0xbe8ebf67,0xbd663f7e,
0x80003f48,0xbf7abf48,0x3edebede,0x3f483f7a,0xbec4bf7a,0x3f803f08,0xbe8e3f20,0xbf51bf72,0x3f353f7a,0xbf72be64,0x3de5bf48,0x3f593f67,0xbf6dbf48,0x3f51bde5,0x3d663f67,0xbf60bf59,
0x3f803ede,0xbf203ede,0xbe64bf7a,0x3f673f48,0xbf6d8000,0x3ec4bf35,0x3ec43f80,0xbf6dbf35,0x3f35bede,0xbde53f67,0xbf08bf7a,0x3f723f20,0xbec43f48,0xbe2cbf7e,0x3f2b3f67,0xbf76bf08,
0x8000bf7a,0x3ede3f7a,0xbf48bf48,0x3f7a3ede,0x3ec43f7a,0xbf2bbf59,0x3f603f20,0xbf7cbea9,0xbf35bf48,0x3f593f20,0xbf72bede,0x3f7e3e64,0x3f6d3ede,0xbf76bea9,0x3f7c3e64,0xbf80bde5,
0xbf808000,0x3f808000,0xbf808000,0x3f808000,0x3f6dbede,0xbf763ea9,0x3f7cbe64,0xbf803de5,0xbf353f48,0x3f59bf20,0xbf723ede,0x3f7ebe64,0x3ec4bf7a,0xbf2b3f59,0x3f60bf20,0xbf7c3ea9,
0xbf488000,0xbedebf7a,0x3f7a3ede,0x80003f48,0x3f518000,0x3e8e3f72,0xbf80bf20,0x3ec4bf08,0xbf598000,0xbde5bf67,0x3f723f48,0xbf353e64,0x3f608000,0xbd663f59,0xbf51bf67,0x3f6d3de5,
0xbf678000,0x3e64bf48,0x3f203f7a,0xbf80bede,0x3f6d8000,0xbec43f35,0xbec4bf80,0x3f6d3f35,0xbf728000,0x3f08bf20,0x3de53f7a,0xbf35bf67,0x3f768000,0xbf2b3f08,0x3e2cbf67,0x3ec43f7e,
0xbf7a8000,0x3f48bede,0xbede3f48,0x8000bf7a,0x3f7c8000,0xbf603ea9,0x3f2bbf20,0xbec43f59,0xbf7e8000,0x3f72be64,0xbf593ede,0x3f35bf20,0x3f808000,0xbf7c3de5,0x3f76be64,0xbf6d3ea9,
0xbf808000,0x3f808000,0xbf808000,0x3f808000,0x3f808000,0xbf7cbde5,0x3f763e64,0xbf6dbea9,0xbf7e8000,0x3f723e64,0xbf59bede,0x3f353f20,0x3f7c8000,0xbf60bea9,0x3f2b3f20,0xbec4bf59,
0xbf7abf48,0x3edebede,0x3f483f7a,0xbf488000,0x3f3f3f7a,0xbf60bde5,0xbe2cbf67,0x3f7c3f35,0xbea9bf7a,0x3f7e3f20,0xbf083ede,0xbf08bf80,0xbe2c3f48,0xbf3fbf72,0x3f763e64,0xbe8e3f35,
0x3f20bede,0x3e643f7a,0xbf67bf48,0x3f678000,0xbf6d8000,0x3ec4bf35,0x3ec43f80,0xbf6dbf35,0x3f7e3ede,0xbf593e64,0x3ea9bf48,0x3ea93f80,0xbf51bf48,0x3f803ea9,0xbf603e64,0x3ef8bf35,
0x3ede3f7a,0xbf48bf48,0x3f7a3ede,0xbf7a8000,0x3d66bf7a,0x3e8e3f7e,0xbf14bf67,0x3f513f35,0xbf083f48,0x3ea9bf67,0xbde53f7a,0xbde5bf80,0x3f60bede,0xbf513f08,0x3f3fbf20,0xbf2b3f35,
0xbf808000,0x3f808000,0xbf808000,0x3f808000,0x3f603ede,0xbf51bf08,0x3f3f3f20,0xbf2bbf35,0xbf08bf48,0x3ea93f67,0xbde5bf7a,0xbde53f80,0x3d663f7a,0x3e8ebf7e,0xbf143f67,0x3f51bf35,
0xbedebf7a,0x3f7a3ede,0x80003f48,0xbf7abf48,0xbef83ede,0xbf2bbf7e,0x3f6d3e64,0x3d663f59,0x3f7e3ede,0xbea93f20,0xbf35bf7a,0x3f723e64,0xbf2bbf7a,0x3f7c3ea9,0xbec43f20,0xbf14bf7e,
0xbe643f48,0xbf20bf7a,0x3f803ede,0xbf203ede,0x3f6d8000,0xbec43f35,0xbec4bf80,0x3f6d3f35,0xbf59bf48,0x3f7e3e64,0xbf353ede,0x3de5bf67,0x3d663f7a,0xbf14bf72,0x3f6d3f20,0xbf7cbde5,
0x3f48bede,0xbede3f48,0x8000bf7a,0x3ede3f7a,0xbf76bede,0x3f803de5,0xbf6d3e64,0x3f3fbf08,0x3ea93f7a,0xbf08bf67,0x3f353f48,0xbf59bf20,0x3f14bf48,0xbef83f59,0x3ec4bf67,0xbe8e3f72,
0xbf808000,0x3f808000,0xbf808000,0x3f808000,0x3f143f48,0xbef8bf59,0x3ec43f67,0xbe8ebf72,0x3ea9bf7a,0xbf083f67,0x3f35bf48,0xbf593f20,0xbf763ede,0x3f80bde5,0xbf6dbe64,0x3f3f3f08,
0x3edebede,0x3f483f7a,0xbf488000,0xbedebf7a,0xbf76bf48,0x3f14bea9,0x3f143f80,0xbf76bea9,0xbde53f48,0xbf59bf67,0x3f598000,0x3de53f67,0x3f803ede,0xbef83f08,0xbef8bf80,0x3f803f08,
0xbe64bf7a,0x3f673f48,0xbf678000,0x3e64bf48,0xbf6d8000,0x3ec4bf35,0x3ec43f80,0xbf6dbf35,0x3f083f7a,0xbf72bf20,0x3f728000,0xbf083f20,0x3f3fbede,0xbe8e3f59,0xbe8ebf80,0x3f3f3f59,
0xbf48bf48,0x3f7a3ede,0xbf7a8000,0x3f48bede,0xbef83f48,0x3e2cbf72,0x3e2c3f80,0xbef8bf72,0x3f723ede,0xbf7ebe64,0x3f7e8000,0xbf723e64,0x3e2cbf7a,0xbd663f7e,0xbd66bf80,0x3e2c3f7e,
0xbf808000,0x3f808000,0xbf808000,0x3f808000,0x3e2c3f7a,0xbd66bf7e,0xbd663f80,0x3e2cbf7e,0x3f72bede,0xbf7e3e64,0x3f7e8000,0xbf72be64,0xbef8bf48,0x3e2c3f72,0x3e2cbf80,0xbef83f72,
0x3f7a3ede,0x80003f48,0xbf7abf48,0x3edebede,0x3d66bf48,0x3f6d3f59,0xbf2b3e64,0xbef8bf7e,0xbf72bf48,0x3f35be64,0x3ea93f7a,0xbf7ebf20,0xbf143ede,0xbec4bf7e,0x3f7c3f20,0xbf2b3ea9,
0x3f203f7a,0xbf80bede,0x3f20bede,0x3e643f7a,0x3f6d8000,0xbec43f35,0xbec4bf80,0x3f6d3f35,0xbde5bf7a,0x3f353f67,0xbf7ebede,0x3f59be64,0xbf7cbede,0x3f6dbde5,0xbf143f20,0x3d66bf72,
0xbede3f48,0x8000bf7a,0x3ede3f7a,0xbf48bf48,0x3f3f3f48,0xbf6dbf08,0x3f803e64,0xbf763de5,0x3f59bede,0xbf353f20,0x3f08bf48,0xbea93f67,0xbe8ebf7a,0x3ec43f72,0xbef8bf67,0x3f143f59,
0xbf808000,0x3f808000,0xbf808000,0x3f808000,0xbe8e3f7a,0x3ec4bf72,0xbef83f67,0x3f14bf59,0x3f593ede,0xbf35bf20,0x3f083f48,0xbea9bf67,0x3f3fbf48,0xbf6d3f08,0x3f80be64,0xbf76bde5,
0x3f483f7a,0xbf488000,0x3f7c3ede,0xbe2c3f35,0x3f08bede,0x3f083f80,0xbe8ebf7a,0x3f763f35,0xbf67bf48,0x3f678000,0xbf6d8000,0x3ec4bf35,0xbea93f48,0xbea9bf80,0x3ef83f7a,0xbf60bf35,
0x3f7a3ede,0xbf7a8000,0x3f51bede,0xbf143f35,0x3de5bf7a,0x3de53f80,0xbf2bbf48,0x3f3f3f35,0xbf808000,0x3f808000,0xbf2b3f48,0x3f3fbf35,0x3de53f7a,0x3de5bf80,0x3f513ede,0xbf14bf35,
0xbedebf7a,0x3f7a3ede,0xbf60bf67,0x3f3fbde5,0xbf7ebede,0x3ea9bf20,0xbf3f3e64,0xbe2cbf72,0xbe643f48,0xbf20bf7a,0x3ec43f80,0xbf6dbf35,0x3f593f48,0xbf7ebe64,0x3f803e64,0xbf513ea9,
0x3f48bede,0xbede3f48,0x3e8ebf67,0x3d663f7e,0xbea9bf7a,0x3f083f67,0xbf51bf20,0x3f603f08,0xbf808000,0x3f808000,0xbf513f20,0x3f60bf08,0xbea93f7a,0x3f08bf67,0x3e8e3f67,0x3d66bf7e,
0x80003f48,0xbf7abf48,0x3ec43f7a,0xbf80bf08,0x3f353f7a,0xbf72be64,0x3f6d3f48,0xbf513de5,0x3f803ede,0xbf203ede,0x3f6d8000,0xbec43f35,0x3f35bede,0xbde53f67,0x3ec4bf48,0x3e2c3f7e,
0x8000bf7a,0x3ede3f7a,0xbec4bf7a,0x3f2b3f59,0xbf35bf48,0x3f593f20,0xbf6dbede,0x3f763ea9,0xbf808000,0x3f808000,0xbf6d3ede,0x3f76bea9,0xbf353f48,0x3f59bf20,0xbec43f7a,0x3f2bbf59,
0x3edebede,0x3f483f7a,0x3e8ebf20,0x3f513f72,0x3de5bf48,0x3f593f67,0xbd66bf67,0x3f603f59,0xbe64bf7a,0x3f673f48,0xbec4bf80,0x3f6d3f35,0xbf08bf7a,0x3f723f20,0xbf2bbf67,0x3f763f08,
0xbf48bf48,0x3f7a3ede,0xbf60bf20,0x3f7c3ea9,0xbf72bede,0x3f7e3e64,0xbf7cbe64,0x3f803de5,0xbf808000,0x3f808000,0xbf7c3e64,0x3f80bde5,0xbf723ede,0x3f7ebe64,0xbf603f20,0x3f7cbea9,
0xbe643f80,0x3f20bf67,0xbf673f20,0x3f80be64,0xbe8e3f80,0x3f3fbf59,0xbf7c3ede,0x3f6d3de5,0xbea93f80,0x3f59bf48,0xbf7e3e64,0x3f353ede,0xbec43f80,0x3f6dbf35,0xbf6d0000,0x3ec43f35,
0xbede3f80,0x3f7abf20,0xbf48be64,0x3f67,0xbef83f80,0x3f80bf08,0xbf14bede,0xbec43f7e,0xbf083f80,0x3f7ebede,0xbea9bf20,0xbf353f7a,0xbf143f80,0x3f76bea9,0xbd66bf48,0xbf6d3f59,
0xbf203f80,0x3f67be64,0x3e64bf67,0xbf803f20,0xbf2b3f80,0x3f51bde5,0x3ef8bf7a,0xbf6d3ea9,0xbf353f80,0x3f350000,0x3f35bf80,0xbf350000,0xbf3f3f80,0x3f143de5,0x3f60bf7a,0xbec4bea9,
0xbf483f80,0x3ede3e64,0x3f7abf67,0xbf20,0xbf513f80,0x3e8e3ea9,0x3f80bf48,0x3ec4bf59,0xbf593f80,0x3de53ede,0x3f72bf20,0x3f35bf7a,0xbf603f80,0xbd663f08,0x3f51bede,0x3f6dbf7e,
0xbf67be64,0x3f203f20,0xbe64bf67,0xbe643f80,0xbf14bf20,0x3d663f72,0x3ef8bf7a,0xbf603f35,0xbde5bf67,0xbf083f7a,0x3f72bf20,0xbf720000,0x3ec4bf80,0xbf6d3f35,0x3f6d0000,0xbec4bf35,
0x3f48bf67,0xbf7a3e64,0x3ede3f20,0x3edebf80,0x3f7cbf20,0xbf2bbea9,0xbe8e3f7a,0x3f76bf35,0x3f72be64,0xbde5bf48,0xbf593f67,0x3f590000,0x3f2b3e64,0x3ef8bf7e,0xbf7c3ede,0x3e2c3f35,
0x3e643f20,0x3f67bf67,0xbf20be64,0xbf203f80,0xbe8e3f67,0x3f7cbf08,0x3d66bf48,0xbf803f35,0xbf353f80,0x3f350000,0x3f35bf80,0xbf350000,0xbf763f67,0x3e2c3f08,0x3f80bf48,0x3d66bf35,
0xbf7a3f20,0xbede3f67,0x3f48be64,0x3f48bf80,0xbf3f3e64,0xbf603f7e,0x3e2c3ede,0x3f7cbf35,0xbea9be64,0xbf7e3f48,0xbf083f67,0x3f080000,0x3e2cbf20,0xbf3f3ea9,0xbf763f7a,0xbe8e3f35,
0x3f20bf67,0xbf673f20,0x3f80be64,0xbf67be64,0x3f80be64,0xbf51bea9,0x3ec43f48,0x3e2cbf7e,0x3f083f20,0x3de5bf7a,0xbf353f67,0x3f7ebede,0xbec43f80,0x3f6dbf35,0xbf6d0000,0x3ec43f35,
0xbf7a3f20,0x3f483e64,0xbf67,0xbf483f67,0xbf3fbe64,0xbe2c3f72,0x3f6dbf48,0xbf51bde5,0x3de5bf67,0xbf723f48,0x3f353e64,0x3ea9bf7a,0x3f60bf67,0xbf3fbde5,0xbec43f7a,0x3f80bf08,
0x3f67be64,0x3e64bf67,0xbf803f20,0x3e643f20,0x3e2c3f20,0x3f76bf59,0xbec4bede,0xbf603f72,0xbf353f80,0x3f350000,0x3f35bf80,0xbf350000,0xbf7c3f20,0xbe8e3f59,0x3f6dbede,0x3ef8bf72,
0xbedebe64,0xbf7a3f67,0x3f20,0x3f7abf20,0x3ef8bf67,0xbf2b3de5,0xbf6d3f7a,0x3d663f08,0x3f7ebf67,0x3ea9bf48,0xbf353e64,0xbf723f7a,0x3f2bbe64,0x3f7cbf72,0x3ec4bf48,0xbf143de5,
0x3f203f20,0xbe64bf67,0xbe643f80,0x3f20bf67,0xbf2b3f67,0x3f76bf08,0xbf760000,0x3f2b3f08,0xbf59be64,0x3ea93f48,0x3ea9bf80,0xbf593f48,0x3ec4bf80,0xbf6d3f35,0x3f6d0000,0xbec4bf35,
0x3f7abe64,0xbedebf20,0xbede3f80,0x3f7abf20,0xbd663f67,0x3f60bf59,0xbf600000,0x3d663f59,0xbf7e3f20,0x3f083ede,0x3f08bf80,0xbf7e3ede,0xbe8ebf20,0xbf513f72,0x3f510000,0x3e8ebf72,
0x3f67bf67,0xbf20be64,0xbf203f80,0x3f67be64,0x3f143e64,0x3f3fbf7e,0xbf3f0000,0xbf143f7e,0xbf353f80,0x3f350000,0x3f35bf80,0xbf350000,0xbf513e64,0xbf2b3f7e,0x3f2b0000,0x3f51bf7e,
0x3edebf67,0xbf483e64,0xbf483f80,0x3ede3e64,0x3f76bf20,0x3f14bf72,0xbf140000,0xbf763f72,0xbde53f20,0x3f59bede,0x3f59bf80,0xbde5bede,0xbf803f67,0xbef83f59,0x3ef80000,0x3f80bf59,
0xbf673f20,0x3f80be64,0xbf67be64,0x3f203f20,0xbe2cbf67,0xbec43f7e,0x3f51bf48,0xbf803ea9,0x3f7ebe64,0xbf35bede,0x3de53f67,0x3f08bf7a,0xbec43f80,0x3f6dbf35,0xbf6d0000,0x3ec43f35,
0xbf48be64,0x3f67,0x3f48bf67,0xbf7a3e64,0x3f51bf67,0xbf6d3de5,0x3e2c3f48,0x3f3fbf72,0x3ea93f20,0x3f35bf7a,0xbf723e64,0x3de53f48,0xbf803f20,0x3ec43f08,0x3f3fbf7a,0xbf603de5,
0x3e64bf67,0xbf803f20,0x3e643f20,0x3f67bf67,0x3f60be64,0x3ec4bf72,0xbf763ede,0xbe2c3f59,0xbf353f80,0x3f350000,0x3f35bf80,0xbf350000,0xbef8be64,0xbf6d3f72,0x3e8e3ede,0x3f7cbf59,
0x3f7abf67,0xbf20,0xbf7a3f20,0xbede3f67,0xbd663f20,0x3f6dbf08,0x3f2bbf7a,0xbef8bde5,0xbf723f20,0xbf353f7a,0x3ea93e64,0x3f7ebf48,0x3f14bf67,0xbec4bde5,0xbf7c3f48,0xbf2b3f72,
0xbe64bf67,0xbe643f80,0x3f20bf67,0xbf673f20,0x3f603e64,0xbef8bf35,0xbd663f7a,0x3f14bf72,0xbf723f20,0x3f720000,0xbf08bf20,0xbde53f7a,0x3ec4bf80,0xbf6d3f35,0x3f6d0000,0xbec4bf35,
0x3ede3f20,0x3edebf80,0xbf7a3f20,0x3f483e64,0xbf763e64,0x3e8e3f35,0x3f2bbf7a,0xbf7c3ea9,0x3f59bf67,0xbf590000,0xbde53f67,0x3f72bf48,0xbe2c3f67,0x3f7cbf35,0xbef8bede,0xbf2b3f7e,
0xbf20be64,0xbf203f80,0x3f67be64,0x3e64bf67,0x3f80bf20,0xbd66bf35,0xbf7c3f48,0x3e8e3f08,0xbf353f80,0x3f350000,0x3f35bf80,0xbf350000,0xbd66bf20,0xbf803f35,0xbe2c3f48,0x3f76bf08,
0x3f48be64,0x3f48bf80,0xbedebe64,0xbf7a3f67,0xbf7c3f67,0xbe2c3f35,0x3f60bede,0x3f3fbf7e,0x3f08bf67,0xbf080000,0xbf7e3f67,0xbea93f48,0x3e8e3e64,0x3f76bf35,0x3f3fbf7a,0xbe2cbea9,
0x3f80be64,0xbf67be64,0x3f203f20,0xbe64bf67,0xbf6d3f20,0x3f7cbde5,0xbf3fbede,0x3e8e3f59,0x3f35bf67,0xbf7e3ede,0x3f593e64,0xbea9bf48,0xbec43f80,0x3f6dbf35,0xbf6d0000,0x3ec43f35,
0xbf67,0xbf483f67,0x3f7abe64,0xbedebf20,0x3ec43f20,0x3f14bf7e,0xbf803ede,0x3ef83f08,0xbf35be64,0xbea93f7a,0x3f7ebf20,0xbf08bede,0x3f6dbe64,0x3d66bf59,0xbf763f48,0x3f143ea9,
0xbf803f20,0x3e643f20,0x3f67bf67,0xbf20be64,0x3f6dbf67,0xbef8bea9,0xbf513f7a,0x3f2b3de5,0xbf353f80,0x3f350000,0x3f35bf80,0xbf350000,0x3ec4bf67,0xbf603ea9,0xbf143f7a,0x3f3fbde5,
0x3f20,0x3f7abf20,0x3edebf67,0xbf483e64,0xbec4be64,0xbf803f59,0xbe8e3f48,0x3f51bea9,0x3f35be64,0x3f72bf7a,0x3de5bf20,0xbf593ede,0xbf6d3f20,0xbf513f7e,0x3d663ede,0x3f60bf08,
0xbe643f80,0x3f20bf67,0xbf673f20,0x3f80be64,0x3e8ebf80,0xbf3f3f59,0x3f7cbede,0xbf6dbde5,0xbea93f80,0x3f59bf48,0xbf7e3e64,0x3f353ede,0x3ec4bf80,0xbf6d3f35,0x3f6d0000,0xbec4bf35,
0xbede3f80,0x3f7abf20,0xbf48be64,0x3f67,0x3ef8bf80,0xbf803f08,0x3f143ede,0x3ec4bf7e,0xbf083f80,0x3f7ebede,0xbea9bf20,0xbf353f7a,0x3f14bf80,0xbf763ea9,0x3d663f48,0x3f6dbf59,
0xbf203f80,0x3f67be64,0x3e64bf67,0xbf803f20,0x3f2bbf80,0xbf513de5,0xbef83f7a,0x3f6dbea9,0xbf353f80,0x3f350000,0x3f35bf80,0xbf350000,0x3f3fbf80,0xbf14bde5,0xbf603f7a,0x3ec43ea9,
0xbf483f80,0x3ede3e64,0x3f7abf67,0xbf20,0x3f51bf80,0xbe8ebea9,0xbf803f48,0xbec43f59,0xbf593f80,0x3de53ede,0x3f72bf20,0x3f35bf7a,0x3f60bf80,0x3d66bf08,0xbf513ede,0xbf6d3f7e,
0xbf67be64,0x3f203f20,0xbe64bf67,0xbe643f80,0x3f143f20,0xbd66bf72,0xbef83f7a,0x3f60bf35,0xbde5bf67,0xbf083f7a,0x3f72bf20,0xbf720000,0xbec43f80,0x3f6dbf35,0xbf6d0000,0x3ec43f35,
0x3f48bf67,0xbf7a3e64,0x3ede3f20,0x3edebf80,0xbf7c3f20,0x3f2b3ea9,0x3e8ebf7a,0xbf763f35,0x3f72be64,0xbde5bf48,0xbf593f67,0x3f590000,0xbf2bbe64,0xbef83f7e,0x3f7cbede,0xbe2cbf35,
0x3e643f20,0x3f67bf67,0xbf20be64,0xbf203f80,0x3e8ebf67,0xbf7c3f08,0xbd663f48,0x3f80bf35,0xbf353f80,0x3f350000,0x3f35bf80,0xbf350000,0x3f76bf67,0xbe2cbf08,0xbf803f48,0xbd663f35,
0xbf7a3f20,0xbede3f67,0x3f48be64,0x3f48bf80,0x3f3fbe64,0x3f60bf7e,0xbe2cbede,0xbf7c3f35,0xbea9be64,0xbf7e3f48,0xbf083f67,0x3f080000,0xbe2c3f20,0x3f3fbea9,0x3f76bf7a,0x3e8ebf35,
0x3f20bf67,0xbf673f20,0x3f80be64,0xbf67be64,0xbf803e64,0x3f513ea9,0xbec4bf48,0xbe2c3f7e,0x3f083f20,0x3de5bf7a,0xbf353f67,0x3f7ebede,0x3ec4bf80,0xbf6d3f35,0x3f6d0000,0xbec4bf35,
0xbf7a3f20,0x3f483e64,0xbf67,0xbf483f67,0x3f3f3e64,0x3e2cbf72,0xbf6d3f48,0x3f513de5,0x3de5bf67,0xbf723f48,0x3f353e64,0x3ea9bf7a,0xbf603f67,0x3f3f3de5,0x3ec4bf7a,0xbf803f08,
0x3f67be64,0x3e64bf67,0xbf803f20,0x3e643f20,0xbe2cbf20,0xbf763f59,0x3ec43ede,0x3f60bf72,0xbf353f80,0x3f350000,0x3f35bf80,0xbf350000,0x3f7cbf20,0x3e8ebf59,0xbf6d3ede,0xbef83f72,
0xbedebe64,0xbf7a3f67,0x3f20,0x3f7abf20,0xbef83f67,0x3f2bbde5,0x3f6dbf7a,0xbd66bf08,0x3f7ebf67,0x3ea9bf48,0xbf353e64,0xbf723f7a,0xbf2b3e64,0xbf7c3f72,0xbec43f48,0x3f14bde5,
0x3f203f20,0xbe64bf67,0xbe643f80,0x3f20bf67,0x3f2bbf67,0xbf763f08,0x3f760000,0xbf2bbf08,0xbf59be64,0x3ea93f48,0x3ea9bf80,0xbf593f48,0xbec43f80,0x3f6dbf35,0xbf6d0000,0x3ec43f35,
0x3f7abe64,0xbedebf20,0xbede3f80,0x3f7abf20,0x3d66bf67,0xbf603f59,0x3f600000,0xbd66bf59,0xbf7e3f20,0x3f083ede,0x3f08bf80,0xbf7e3ede,0x3e8e3f20,0x3f51bf72,0xbf510000,0xbe8e3f72,
0x3f67bf67,0xbf20be64,0xbf203f80,0x3f67be64,0xbf14be64,0xbf3f3f7e,0x3f3f0000,0x3f14bf7e,0xbf353f80,0x3f350000,0x3f35bf80,0xbf350000,0x3f51be64,0x3f2bbf7e,0xbf2b0000,0xbf513f7e,
0x3edebf67,0xbf483e64,0xbf483f80,0x3ede3e64,0xbf763f20,0xbf143f72,0x3f140000,0x3f76bf72,0xbde53f20,0x3f59bede,0x3f59bf80,0xbde5bede,0x3f80bf67,0x3ef8bf59,0xbef80000,0xbf803f59,
0xbf673f20,0x3f80be64,0xbf67be64,0x3f203f20,0x3e2c3f67,0x3ec4bf7e,0xbf513f48,0x3f80bea9,0x3f7ebe64,0xbf35bede,0x3de53f67,0x3f08bf7a,0x3ec4bf80,0xbf6d3f35,0x3f6d0000,0xbec4bf35,
0xbf48be64,0x3f67,0x3f48bf67,0xbf7a3e64,0xbf513f67,0x3f6dbde5,0xbe2cbf48,0xbf3f3f72,0x3ea93f20,0x3f35bf7a,0xbf723e64,0x3de53f48,0x3f80bf20,0xbec4bf08,0xbf3f3f7a,0x3f60bde5,
0x3e64bf67,0xbf803f20,0x3e643f20,0x3f67bf67,0xbf603e64,0xbec43f72,0x3f76bede,0x3e2cbf59,0xbf353f80,0x3f350000,0x3f35bf80,0xbf350000,0x3ef83e64,0x3f6dbf72,0xbe8ebede,0xbf7c3f59,
0x3f7abf67,0xbf20,0xbf7a3f20,0xbede3f67,0x3d66bf20,0xbf6d3f08,0xbf2b3f7a,0x3ef83de5,0xbf723f20,0xbf353f7a,0x3ea93e64,0x3f7ebf48,0xbf143f67,0x3ec43de5,0x3f7cbf48,0x3f2bbf72,
0xbe64bf67,0xbe643f80,0xbf60be64,0x3ef83f35,0xbf723f20,0x3f720000,0xbec43f80,0x3f6dbf35,0x3ede3f20,0x3edebf80,0x3f76be64,0xbe8ebf35,0x3f59bf67,0xbf590000,0x3e2cbf67,0xbf7c3f35,
0xbf20be64,0xbf203f80,0xbf803f20,0x3d663f35,0xbf353f80,0x3f350000,0x3d663f20,0x3f80bf35,0x3f48be64,0x3f48bf80,0x3f7cbf67,0x3e2cbf35,0x3f08bf67,0xbf080000,0xbe8ebe64,0xbf763f35,
0x3f20bf67,0xbf673f20,0x3d66bf7a,0xbf143f72,0xbf08bf20,0xbde53f7a,0xbf6d0000,0x3ec43f35,0xbf7a3f20,0x3f483e64,0xbf2b3f7a,0x3f7cbea9,0xbde53f67,0x3f72bf48,0x3ef83ede,0x3f2bbf7e,
0x3f67be64,0x3e64bf67,0x3f7cbf48,0xbe8ebf08,0x3f35bf80,0xbf350000,0x3e2cbf48,0xbf763f08,0xbedebe64,0xbf7a3f67,0xbf603ede,0xbf3f3f7e,0xbf7e3f67,0xbea93f48,0xbf3f3f7a,0x3e2c3ea9,
0x3f80be64,0xbf67be64,0x3f6dbf20,0xbf7c3de5,0x3f35bf67,0xbf7e3ede,0x3ec4bf80,0xbf6d3f35,0xbf67,0xbf483f67,0xbec4bf20,0xbf143f7e,0xbf35be64,0xbea93f7a,0xbf6d3e64,0xbd663f59,
0xbf803f20,0x3e643f20,0xbf6d3f67,0x3ef83ea9,0xbf353f80,0x3f350000,0xbec43f67,0x3f60bea9,0x3f20,0x3f7abf20,0x3ec43e64,0x3f80bf59,0x3f35be64,0x3f72bf7a,0x3f6dbf20,0x3f51bf7e,
0x3f203f20,0xbe64bf67,0x3f3f3ede,0xbe8ebf59,0x3f593e64,0xbea9bf48,0x3f6d0000,0xbec4bf35,0x3f7abe64,0xbedebf20,0x3f80bede,0xbef8bf08,0x3f7ebf20,0xbf08bede,0x3f76bf48,0xbf14bea9,
0x3f67bf67,0xbf20be64,0x3f51bf7a,0xbf2bbde5,0x3f35bf80,0xbf350000,0x3f14bf7a,0xbf3f3de5,0x3edebf67,0xbf483e64,0x3e8ebf48,0xbf513ea9,0x3de5bf20,0xbf593ede,0xbd66bede,0xbf603f08,
0xbf7a8000,0x3f483ede,0xbedebf48,0x80003f7a,0xbf768000,0x3f2b3f08,0xbe2cbf67,0xbec43f7e,0xbf728000,0x3f083f20,0x3de5bf7a,0xbf353f67,0xbf6d8000,0x3ec43f35,0x3ec4bf80,0xbf6d3f35,
0xbf678000,0x3e643f48,0x3f20bf7a,0xbf803ede,0xbf608000,0x3d663f59,0x3f51bf67,0xbf6d3de5,0xbf598000,0xbde53f67,0x3f72bf48,0xbf35be64,0xbf518000,0xbe8e3f72,0x3f80bf20,0xbec4bf08,
0xbf488000,0xbede3f7a,0x3f7abede,0x8000bf48,0xbf3f8000,0xbf143f7e,0x3f60be64,0x3ec4bf72,0xbf358000,0xbf353f80,0x3f358000,0x3f35bf80,0xbf2b8000,0xbf513f7e,0x3ef83e64,0x3f6dbf72,
0xbf208000,0xbf673f7a,0x3e643ede,0x3f80bf48,0xbf148000,0xbf763f72,0xbd663f20,0x3f6dbf08,0xbf088000,0xbf7e3f67,0xbea93f48,0x3f35be64,0xbef88000,0xbf803f59,0xbf143f67,0x3ec43de5,
0x3edebf7a,0xbf483f48,0x3f7abede,0xbf7a8000,0x3f51bf48,0xbf803ea9,0x3f603e64,0xbef8bf35,0x3f7ebede,0xbf59be64,0x3ea93f48,0x3ea9bf80,0x3f6d8000,0xbec4bf35,0xbec43f80,0x3f6dbf35,
0x3f203ede,0x3e64bf7a,0xbf673f48,0x3f678000,0x3e2c3f48,0x3f3fbf72,0xbf763e64,0x3e8e3f35,0xbea93f7a,0x3f7ebf20,0xbf08bede,0xbf083f80,0xbf3f3f7a,0x3f60bde5,0x3e2cbf67,0xbf7c3f35,
0xbf7a3f48,0x3ede3ede,0x3f48bf7a,0xbf488000,0xbf763ede,0xbe2c3f59,0x3f80bf20,0xbd66bf35,0xbf358000,0xbf353f80,0x3f358000,0x3f35bf80,0xbe8ebede,0xbf7c3f59,0x3d663f20,0x3f80bf35,
0x3e64bf48,0xbf673ede,0xbf203f7a,0x3f208000,0x3f2bbf7a,0xbef8bde5,0xbf7c3f67,0xbe2c3f35,0x3f72bf7a,0x3de5bf20,0xbf593ede,0xbf593f80,0x3f7cbf48,0x3f2bbf72,0xbe8ebe64,0xbf763f35,
0x3f483ede,0xbedebf48,0x80003f7a,0x3edebf7a,0xbd663f7a,0x3f14bf72,0xbf6d3f20,0x3f7cbde5,0xbf593f48,0x3f7ebe64,0xbf35bede,0x3de53f67,0xbf6d8000,0x3ec43f35,0x3ec4bf80,0xbf6d3f35,
0xbe64bf48,0xbf203f7a,0x3f80bede,0xbf20bede,0x3f2bbf7a,0xbf7c3ea9,0x3ec43f20,0x3f14bf7e,0x3f7ebede,0xbea9bf20,0xbf353f7a,0x3f72be64,0x3ef83ede,0x3f2bbf7e,0xbf6d3e64,0xbd663f59,
0xbede3f7a,0x3f7abede,0x8000bf48,0xbf7a3f48,0xbf7c3f48,0x3e8e3f08,0x3f6dbf67,0xbef8bea9,0xbf358000,0xbf353f80,0x3f358000,0x3f35bf80,0x3e2cbf48,0xbf763f08,0xbec43f67,0x3f60bea9,
0x3f67bf7a,0xbe64bede,0xbf803f48,0xbe643f48,0x3f60bede,0x3f3fbf7e,0xbec4be64,0xbf803f59,0x3de53ede,0x3f72bf20,0x3f35bf7a,0xbea9be64,0xbf3f3f7a,0x3e2c3ea9,0x3f6dbf20,0x3f51bf7e,
0xbf483f48,0x3f7abede,0xbf7a8000,0x3f483ede,0xbf3fbede,0x3e8e3f59,0x3e8ebf80,0xbf3f3f59,0x3f08bf7a,0xbf723f20,0x3f728000,0xbf08bf20,0x3f6d8000,0xbec4bf35,0xbec43f80,0x3f6dbf35,
0xbe643f7a,0x3f67bf48,0xbf678000,0x3e643f48,0xbf803ede,0x3ef83f08,0x3ef8bf80,0xbf803f08,0xbde5bf48,0xbf593f67,0x3f598000,0x3de5bf67,0x3f76bf48,0xbf14bea9,0xbf143f80,0x3f76bea9,
0x3ede3ede,0x3f48bf7a,0xbf488000,0xbede3f7a,0xbf513f7a,0x3f2b3de5,0x3f2bbf80,0xbf513de5,0xbf358000,0xbf353f80,0x3f358000,0x3f35bf80,0x3f14bf7a,0xbf3f3de5,0xbf3f3f80,0x3f143de5,
0x3f67bede,0x3f20bf7a,0xbf208000,0xbf673f7a,0xbe8e3f48,0x3f51bea9,0x3f51bf80,0xbe8ebea9,0xbf7e3f48,0xbf083f67,0x3f088000,0x3f7ebf67,0xbd66bede,0xbf603f08,0xbf603f80,0xbd663f08,
0xbedebf48,0x80003f7a,0x3edebf7a,0xbf483f48,0x3f7cbede,0xbf6dbde5,0x3f143f20,0xbd66bf72,0xbde53f7a,0x3f35bf67,0xbf7e3ede,0x3f593e64,0xbf6d8000,0x3ec43f35,0x3ec4bf80,0xbf6d3f35,
0x3f20bf7a,0xbf803ede,0x3f203ede,0x3e64bf7a,0x3f143ede,0x3ec4bf7e,0xbf7c3f20,0x3f2b3ea9,0xbf723f48,0x3f353e64,0x3ea9bf7a,0xbf7e3f20,0xbd66bf48,0xbf6d3f59,0x3f2b3e64,0x3ef8bf7e,
0x3f7abede,0x8000bf48,0xbf7a3f48,0x3ede3ede,0xbef83f7a,0x3f6dbea9,0x3e8ebf67,0xbf7c3f08,0xbf358000,0xbf353f80,0x3f358000,0x3f35bf80,0x3f60bf7a,0xbec4bea9,0xbf763f67,0x3e2c3f08,
0x3e643ede,0x3f80bf48,0x3e64bf48,0xbf673ede,0xbf803f48,0xbec43f59,0x3f3fbe64,0x3f60bf7e,0x3ea9bf48,0xbf353e64,0xbf723f7a,0xbde53f20,0x3f51bede,0x3f6dbf7e,0x3e2cbf20,0xbf3f3ea9,
0x3f7abede,0xbf7a8000,0x3f483ede,0xbedebf48,0xbef83f7a,0x3f60bf35,0xbf803e64,0x3f513ea9,0xbea9bf48,0xbea93f80,0x3f59bf48,0xbf7e3e64,0x3f6d8000,0xbec4bf35,0xbec43f80,0x3f6dbf35,
0xbf673f48,0x3f678000,0xbe64bf48,0xbf203f7a,0x3e8ebf7a,0xbf763f35,0x3f3f3e64,0x3e2cbf72,0x3f083ede,0x3f08bf80,0xbf7e3ede,0x3ea93f20,0xbf7c3ede,0x3e2c3f35,0x3f60bf67,0xbf3fbde5,
0x3f48bf7a,0xbf488000,0xbede3f7a,0x3f7abede,0xbd663f48,0x3f80bf35,0xbe2cbf20,0xbf763f59,0xbf358000,0xbf353f80,0x3f358000,0x3f35bf80,0x3f80bf48,0x3d66bf35,0xbf7c3f20,0xbe8e3f59,
0xbf203f7a,0x3f208000,0x3f67bf7a,0xbe64bede,0xbe2cbede,0xbf7c3f35,0xbef83f67,0x3f2bbde5,0x3f59bede,0x3f59bf80,0xbde5bede,0xbf723f20,0xbf763f7a,0xbe8e3f35,0x3f2bbe64,0x3f7cbf72,
0x80003f7a,0x3edebf7a,0xbf483f48,0x3f7abede,0xbec4bf48,0xbe2c3f7e,0x3f2bbf67,0xbf763f08,0x3f353ede,0xbde5bf67,0xbf083f7a,0x3f72bf20,0xbf6d8000,0x3ec43f35,0x3ec4bf80,0xbf6d3f35,
0x3f80bede,0xbf20bede,0xbe643f7a,0x3f67bf48,0xbf6d3f48,0x3f513de5,0x3d66bf67,0xbf603f59,0x3f35bf7a,0xbf723e64,0x3de53f48,0x3f59bf67,0xbec43f7a,0x3f80bf08,0xbe8ebf20,0xbf513f72,
0x8000bf48,0xbf7a3f48,0x3ede3ede,0x3f48bf7a,0x3ec43ede,0x3f60bf72,0xbf14be64,0xbf3f3f7e,0xbf358000,0xbf353f80,0x3f358000,0x3f35bf80,0x3f6dbede,0x3ef8bf72,0xbf513e64,0xbf2b3f7e,
0xbf803f48,0xbe643f48,0x3f67bede,0x3f20bf7a,0x3f6dbf7a,0xbd66bf08,0xbf763f20,0xbf143f72,0xbf353f7a,0x3ea93e64,0x3f7ebf48,0x3f08bf67,0x3ec4bf48,0xbf143de5,0xbf803f67,0xbef83f59,
0xbf7a8000,0x3f483ede,0xbedebf48,0x80003f7a,0x3f768000,0xbf2bbf08,0x3e2c3f67,0x3ec4bf7e,0xbf728000,0x3f083f20,0x3de5bf7a,0xbf353f67,0x3f6d8000,0xbec4bf35,0xbec43f80,0x3f6dbf35,
0xbf678000,0x3e643f48,0x3f20bf7a,0xbf803ede,0x3f608000,0xbd66bf59,0xbf513f67,0x3f6dbde5,0xbf598000,0xbde53f67,0x3f72bf48,0xbf35be64,0x3f518000,0x3e8ebf72,0xbf803f20,0x3ec43f08,
0xbf488000,0xbede3f7a,0x3f7abede,0x8000bf48,0x3f3f8000,0x3f14bf7e,0xbf603e64,0xbec43f72,0xbf358000,0xbf353f80,0x3f358000,0x3f35bf80,0x3f2b8000,0x3f51bf7e,0xbef8be64,0xbf6d3f72,
0xbf208000,0xbf673f7a,0x3e643ede,0x3f80bf48,0x3f148000,0x3f76bf72,0x3d66bf20,0xbf6d3f08,0xbf088000,0xbf7e3f67,0xbea93f48,0x3f35be64,0x3ef88000,0x3f80bf59,0x3f14bf67,0xbec4bde5,
0x3edebf7a,0xbf483f48,0x3f7abede,0xbf7a8000,0xbf513f48,0x3f80bea9,0xbf60be64,0x3ef83f35,0x3f7ebede,0xbf59be64,0x3ea93f48,0x3ea9bf80,0xbf6d8000,0x3ec43f35,0x3ec4bf80,0xbf6d3f35,
0x3f203ede,0x3e64bf7a,0xbf673f48,0x3f678000,0xbe2cbf48,0xbf3f3f72,0x3f76be64,0xbe8ebf35,0xbea93f7a,0x3f7ebf20,0xbf08bede,0xbf083f80,0x3f3fbf7a,0xbf603de5,0xbe2c3f67,0x3f7cbf35,
0xbf7a3f48,0x3ede3ede,0x3f48bf7a,0xbf488000,0x3f76bede,0x3e2cbf59,0xbf803f20,0x3d663f35,0xbf358000,0xbf353f80,0x3f358000,0x3f35bf80,0x3e8e3ede,0x3f7cbf59,0xbd66bf20,0xbf803f35,
0x3e64bf48,0xbf673ede,0xbf203f7a,0x3f208000,0xbf2b3f7a,0x3ef83de5,0x3f7cbf67,0x3e2cbf35,0x3f72bf7a,0x3de5bf20,0xbf593ede,0xbf593f80,0xbf7c3f48,0xbf2b3f72,0x3e8e3e64,0x3f76bf35,
0x3f483ede,0xbedebf48,0x80003f7a,0x3edebf7a,0x3d66bf7a,0xbf143f72,0x3f6dbf20,0xbf7c3de5,0xbf593f48,0x3f7ebe64,0xbf35bede,0x3de53f67,0x3f6d8000,0xbec4bf35,0xbec43f80,0x3f6dbf35,
0xbe64bf48,0xbf203f7a,0x3f80bede,0xbf20bede,0xbf2b3f7a,0x3f7cbea9,0xbec4bf20,0xbf143f7e,0x3f7ebede,0xbea9bf20,0xbf353f7a,0x3f72be64,0xbef8bede,0xbf2b3f7e,0x3f6dbe64,0x3d66bf59,
0xbede3f7a,0x3f7abede,0x8000bf48,0xbf7a3f48,0x3f7cbf48,0xbe8ebf08,0xbf6d3f67,0x3ef83ea9,0xbf358000,0xbf353f80,0x3f358000,0x3f35bf80,0xbe2c3f48,0x3f76bf08,0x3ec4bf67,0xbf603ea9,
0x3f67bf7a,0xbe64bede,0xbf803f48,0xbe643f48,0xbf603ede,0xbf3f3f7e,0x3ec43e64,0x3f80bf59,0x3de53ede,0x3f72bf20,0x3f35bf7a,0xbea9be64,0x3f3fbf7a,0xbe2cbea9,0xbf6d3f20,0xbf513f7e,
0xbf483f48,0x3f7abede,0xbf7a8000,0x3f483ede,0x3f3f3ede,0xbe8ebf59,0xbe8e3f80,0x3f3fbf59,0x3f08bf7a,0xbf723f20,0x3f728000,0xbf08bf20,0xbf6d8000,0x3ec43f35,0x3ec4bf80,0xbf6d3f35,
0xbe643f7a,0x3f67bf48,0xbf678000,0x3e643f48,0x3f80bede,0xbef8bf08,0xbef83f80,0x3f80bf08,0xbde5bf48,0xbf593f67,0x3f598000,0x3de5bf67,0xbf763f48,0x3f143ea9,0x3f14bf80,0xbf763ea9,
0x3ede3ede,0x3f48bf7a,0xbf488000,0xbede3f7a,0x3f51bf7a,0xbf2bbde5,0xbf2b3f80,0x3f51bde5,0xbf358000,0xbf353f80,0x3f358000,0x3f35bf80,0xbf143f7a,0x3f3fbde5,0x3f3fbf80,0xbf14bde5,
0x3f67bede,0x3f20bf7a,0xbf208000,0xbf673f7a,0x3e8ebf48,0xbf513ea9,0xbf513f80,0x3e8e3ea9,0xbf7e3f48,0xbf083f67,0x3f088000,0x3f7ebf67,0x3d663ede,0x3f60bf08,0x3f60bf80,0x3d66bf08,
0xbedebf48,0x80003f7a,0x3edebf7a,0xbf483f48,0xbf7c3ede,0x3f6d3de5,0xbf14bf20,0x3d663f72,0xbde53f7a,0x3f35bf67,0xbf7e3ede,0x3f593e64,0x3f6d8000,0xbec4bf35,0xbec43f80,0x3f6dbf35,
0x3f20bf7a,0xbf803ede,0x3f203ede,0x3e64bf7a,0xbf14bede,0xbec43f7e,0x3f7cbf20,0xbf2bbea9,0xbf723f48,0x3f353e64,0x3ea9bf7a,0xbf7e3f20,0x3d663f48,0x3f6dbf59,0xbf2bbe64,0xbef83f7e,
0x3f7abede,0x8000bf48,0xbf7a3f48,0x3ede3ede,0x3ef8bf7a,0xbf6d3ea9,0xbe8e3f67,0x3f7cbf08,0xbf358000,0xbf353f80,0x3f358000,0x3f35bf80,0xbf603f7a,0x3ec43ea9,0x3f76bf67,0xbe2cbf08,
0x3e643ede,0x3f80bf48,0x3e64bf48,0xbf673ede,0x3f80bf48,0x3ec4bf59,0xbf3f3e64,0xbf603f7e,0x3ea9bf48,0xbf353e64,0xbf723f7a,0xbde53f20,0xbf513ede,0xbf6d3f7e,0xbe2c3f20,0x3f3fbea9,
0x3f7abede,0xbf7a8000,0x3ef8bf7a,0xbf603f35,0xbea9bf48,0xbea93f80,0xbf6d8000,0x3ec43f35,0xbf673f48,0x3f678000,0xbe8e3f7a,0x3f76bf35,0x3f083ede,0x3f08bf80,0x3f7cbede,0xbe2cbf35,
0x3f48bf7a,0xbf488000,0x3d66bf48,0xbf803f35,0xbf358000,0xbf353f80,0xbf803f48,0xbd663f35,0xbf203f7a,0x3f208000,0x3e2c3ede,0x3f7cbf35,0x3f59bede,0x3f59bf80,0x3f76bf7a,0x3e8ebf35,
0x3f483ede,0xbedebf48,0x3f80be64,0xbf51bea9,0x3f59bf48,0xbf7e3e64,0x3ec4bf80,0xbf6d3f35,0xbe64bf48,0xbf203f7a,0xbf3fbe64,0xbe2c3f72,0xbf7e3ede,0x3ea93f20,0xbf603f67,0x3f3f3de5,
0xbede3f7a,0x3f7abede,0x3e2c3f20,0x3f76bf59,0x3f358000,0x3f35bf80,0x3f7cbf20,0x3e8ebf59,0x3f67bf7a,0xbe64bede,0x3ef8bf67,0xbf2b3de5,0xbde5bede,0xbf723f20,0xbf2b3e64,0xbf7c3f72,
0x80003f7a,0x3edebf7a,0x3ec43f48,0x3e2cbf7e,0x3f353ede,0xbde5bf67,0x3f6d8000,0xbec4bf35,0x3f80bede,0xbf20bede,0x3f6dbf48,0xbf51bde5,0x3f35bf7a,0xbf723e64,0x3ec4bf7a,0xbf803f08,
0x8000bf48,0xbf7a3f48,0xbec4bede,0xbf603f72,0xbf358000,0xbf353f80,0xbf6d3ede,0xbef83f72,0xbf803f48,0xbe643f48,0xbf6d3f7a,0x3d663f08,0xbf353f7a,0x3ea93e64,0xbec43f48,0x3f14bde5,
0xbf483f48,0x3f7abede,0xbf2b3f67,0x3f76bf08,0xbf083f7a,0x3f72bf20,0xbec43f80,0x3f6dbf35,0xbe643f7a,0x3f67bf48,0xbd663f67,0x3f60bf59,0x3de53f48,0x3f59bf67,0x3e8e3f20,0x3f51bf72,
0x3ede3ede,0x3f48bf7a,0x3f143e64,0x3f3fbf7e,0x3f358000,0x3f35bf80,0x3f51be64,0x3f2bbf7e,0x3f67bede,0x3f20bf7a,0x3f76bf20,0x3f14bf72,0x3f7ebf48,0x3f08bf67,0x3f80bf67,0x3ef8bf59,
0xbf673f80,0xbe643f20,0x3f20be64,0x3f80bf67,0xbf6d3f80,0xbec43f35,0x3ec40000,0x3f6dbf35,0xbf723f80,0xbf083f48,0x3de53e64,0x3f35bede,0xbf763f80,0xbf2b3f59,0xbe2c3ede,0x3ec4bde5,
0xbf7a3f80,0xbf483f67,0xbede3f20,0x3e64,0xbf7c3f80,0xbf603f72,0xbf2b3f48,0xbec43f08,0xbf7e3f80,0xbf723f7a,0xbf593f67,0xbf353f48,0xbf803f80,0xbf7c3f7e,0xbf763f7a,0xbf6d3f72,
0xbf803f80,0xbf803f80,0xbf803f80,0xbf803f80,0,0,0,0,0,0,0,0,0,0,0,0,
0,0,0,0,0,0,0,0,0,0,0,0,0,0,0,0,
0x3f20bf67,0xbe64be64,0xbf673f20,0xbf673f80,0x3f6dbf80,0x3ec4bf35,0xbec40000,0xbf6d3f35,0x3f7ebf67,0x3f59bf7a,0x3ea9bf20,0xbea90000,0x3f51bf20,0x3f80bf72,0x3f60bf7a,0x3ef8bf35,
0x3edebe64,0x3f48bf20,0x3f7abf67,0x3f7abf80,0xbd663e64,0x3e8ebde5,0x3f14bede,0x3f51bf35,0xbf083f20,0xbea93ede,0xbde53e64,0x3de50000,0xbf603f67,0xbf513f59,0xbf3f3f48,0xbf2b3f35,
0xbf803f80,0xbf803f80,0xbf803f80,0xbf803f80,0,0,0,0,0,0,0,0,0,0,0,0,
0,0,0,0,0,0,0,0,0,0,0,0,0,0,0,0,
0xbe643f20,0x3f20be64,0x3f80bf67,0x3f20bf67,0xbf6d3f80,0xbec43f35,0x3ec40000,0x3f6dbf35,0xbf593f20,0xbf7e3f7a,0xbf353f67,0xbde53ede,0xbd66be64,0xbf143ea9,0xbf6d3f48,0xbf7c3f7e,
0x3f48bf67,0x3edebf20,0xbe64,0xbede3e64,0x3f76bf67,0x3f80bf7e,0x3f6dbf7a,0x3f3fbf59,0x3ea9be64,0x3f08bede,0x3f35bf20,0x3f59bf48,0xbf143f20,0xbef83f08,0xbec43ede,0xbe8e3ea9,
0xbf803f80,0xbf803f80,0xbf803f80,0xbf803f80,0,0,0,0,0,0,0,0,0,0,0,0,
0,0,0,0,0,0,0,0,0,0,0,0,0,0,0,0,
0xbe64be64,0xbf673f20,0xbf673f80,0xbe643f20,0x3f6dbf80,0x3ec4bf35,0xbec40000,0xbf6d3f35,0x3f08be64,0x3f72bf48,0x3f72bf80,0x3f08bf48,0xbf3f3f67,0xbe8e3f08,0x3e8e0000,0x3f3fbf08,
0xbf483f20,0xbf7a3f67,0xbf7a3f80,0xbf483f67,0x3ef8bf20,0x3e2cbea9,0xbe2c0000,0xbef83ea9,0x3f72bf67,0x3f7ebf7a,0x3f7ebf80,0x3f72bf7a,0xbe2c3e64,0xbd663de5,0x3d660000,0x3e2cbde5,
0xbf803f80,0xbf803f80,0xbf803f80,0xbf803f80,0,0,0,0,0,0,0,0,0,0,0,0,
0,0,0,0,0,0,0,0,0,0,0,0,0,0,0,0,
0x3f20be64,0x3f80bf67,0x3f20bf67,0xbe64be64,0xbf6d3f80,0xbec43f35,0x3ec40000,0x3f6dbf35,0xbde5be64,0xbf353ede,0xbf7e3f67,0xbf593f7a,0x3f7cbf67,0x3f6dbf7e,0x3f14bf48,0x3d66bea9,
0xbede3f20,0x3e64,0x3edebe64,0x3f48bf20,0xbf3f3f20,0xbf6d3f59,0xbf803f7a,0xbf763f7e,0x3f59bf67,0x3f35bf48,0x3f08bf20,0x3ea9bede,0x3e8ebe64,0x3ec4bea9,0x3ef8bede,0x3f14bf08,
0xbf803f80,0xbf803f80,0xbf803f80,0xbf803f80,0,0,0,0,0,0,0,0,0,0,0,0,
0,0,0,0,0,0,0,0,0,0,0,0,0,0,0,0,
0xbf673f20,0xbf673f80,0xbe643f20,0x3f20be64,0x3f6dbf80,0x3ec4bf35,0xbec40000,0xbf6d3f35,0xbea93f20,0x3ea90000,0x3f59bf20,0x3f7ebf7a,0xbef83e64,0xbf603f35,0xbf803f7a,0xbf513f72,
0x3f7abf67,0x3f7abf80,0x3f48bf67,0x3edebf20,0xbf513f67,0xbf143f35,0xbe8e3ede,0x3d663de5,0x3de5be64,0xbde50000,0xbea93e64,0xbf083ede,0x3f2bbf20,0x3f3fbf35,0x3f51bf48,0x3f60bf59,
0xbf803f80,0xbf803f80,0xbf803f80,0xbf803f80,0,0,0,0,0,0,0,0,0,0,0,0,
0,0,0,0,0,0,0,0,0,0,0,0,0,0,0,0,
0x3f80bf67,0x3f20bf67,0xbe64be64,0xbf673f20,0xbf6d3f80,0xbec43f35,0x3ec40000,0x3f6dbf35,0x3f35bf67,0x3de5bede,0xbf083e64,0xbf723f48,0xbec43f20,0x3e2c3de5,0x3f2bbede,0x3f76bf59,
0xbe64,0xbede3e64,0xbf483f20,0xbf7a3f67,0x3ec4be64,0x3f2bbf08,0x3f60bf48,0x3f7cbf72,0xbf353f20,0xbf593f48,0xbf723f67,0xbf7e3f7a,0x3f6dbf67,0x3f76bf72,0x3f7cbf7a,0x3f80bf7e,
0xbf803f80,0xbf803f80,0xbf803f80,0xbf803f80,0,0,0,0,0,0,0,0,0,0,0,0,
0,0,0,0,0,0,0,0,0,0,0,0,0,0,0,0,
0xbf673f80,0xbe643f20,0x3f20be64,0x3f80bf67,0x3f6dbf80,0x3ec4bf35,0xbec40000,0xbf6d3f35,0xbf723f80,0xbf083f48,0x3de53e64,0x3f35bede,0x3f76bf80,0x3f2bbf59,0x3e2cbede,0xbec43de5,
0xbf7a3f80,0xbf483f67,0xbede3f20,0x3e64,0x3f7cbf80,0x3f60bf72,0x3f2bbf48,0x3ec4bf08,0xbf7e3f80,0xbf723f7a,0xbf593f67,0xbf353f48,0x3f80bf80,0x3f7cbf7e,0x3f76bf7a,0x3f6dbf72,
0xbf803f80,0xbf803f80,0xbf803f80,0xbf803f80,0,0,0,0,0,0,0,0,0,0,0,0,
0,0,0,0,0,0,0,0,0,0,0,0,0,0,0,0,
0x3f20bf67,0xbe64be64,0xbf673f20,0xbf673f80,0xbf6d3f80,0xbec43f35,0x3ec40000,0x3f6dbf35,0x3f7ebf67,0x3f59bf7a,0x3ea9bf20,0xbea90000,0xbf513f20,0xbf803f72,0xbf603f7a,0xbef83f35,
0x3edebe64,0x3f48bf20,0x3f7abf67,0x3f7abf80,0x3d66be64,0xbe8e3de5,0xbf143ede,0xbf513f35,0xbf083f20,0xbea93ede,0xbde53e64,0x3de50000,0x3f60bf67,0x3f51bf59,0x3f3fbf48,0x3f2bbf35,
0xbf803f80,0xbf803f80,0xbf803f80,0xbf803f80,0,0,0,0,0,0,0,0,0,0,0,0,
0,0,0,0,0,0,0,0,0,0,0,0,0,0,0,0,
0xbe643f20,0x3f20be64,0x3f80bf67,0x3f20bf67,0x3f6dbf80,0x3ec4bf35,0xbec40000,0xbf6d3f35,0xbf593f20,0xbf7e3f7a,0xbf353f67,0xbde53ede,0x3d663e64,0x3f14bea9,0x3f6dbf48,0x3f7cbf7e,
0x3f48bf67,0x3edebf20,0xbe64,0xbede3e64,0xbf763f67,0xbf803f7e,0xbf6d3f7a,0xbf3f3f59,0x3ea9be64,0x3f08bede,0x3f35bf20,0x3f59bf48,0x3f14bf20,0x3ef8bf08,0x3ec4bede,0x3e8ebea9,
0xbf803f80,0xbf803f80,0xbf803f80,0xbf803f80,0,0,0,0,0,0,0,0,0,0,0,0,
0,0,0,0,0,0,0,0,0,0,0,0,0,0,0,0,
0xbe64be64,0xbf673f20,0xbf673f80,0xbe643f20,0xbf6d3f80,0xbec43f35,0x3ec40000,0x3f6dbf35,0x3f08be64,0x3f72bf48,0x3f72bf80,0x3f08bf48,0x3f3fbf67,0x3e8ebf08,0xbe8e0000,0xbf3f3f08,
0xbf483f20,0xbf7a3f67,0xbf7a3f80,0xbf483f67,0xbef83f20,0xbe2c3ea9,0x3e2c0000,0x3ef8bea9,0x3f72bf67,0x3f7ebf7a,0x3f7ebf80,0x3f72bf7a,0x3e2cbe64,0x3d66bde5,0xbd660000,0xbe2c3de5,
0xbf803f80,0xbf803f80,0xbf803f80,0xbf803f80,0,0,0,0,0,0,0,0,0,0,0,0,
0,0,0,0,0,0,0,0,0,0,0,0,0,0,0,0,
0x3f20be64,0x3f80bf67,0x3f20bf67,0xbe64be64,0x3f6dbf80,0x3ec4bf35,0xbec40000,0xbf6d3f35,0xbde5be64,0xbf353ede,0xbf7e3f67,0xbf593f7a,0xbf7c3f67,0xbf6d3f7e,0xbf143f48,0xbd663ea9,
0xbede3f20,0x3e64,0x3edebe64,0x3f48bf20,0x3f3fbf20,0x3f6dbf59,0x3f80bf7a,0x3f76bf7e,0x3f59bf67,0x3f35bf48,0x3f08bf20,0x3ea9bede,0xbe8e3e64,0xbec43ea9,0xbef83ede,0xbf143f08,
0xbf803f80,0xbf803f80,0xbf803f80,0xbf803f80,0,0,0,0,0,0,0,0,0,0,0,0,
0,0,0,0,0,0,0,0,0,0,0,0,0,0,0,0,
0xbf673f20,0xbf673f80,0xbf6d3f80,0xbec43f35,0xbea93f20,0x3ea90000,0x3ef8be64,0x3f60bf35,0x3f7abf67,0x3f7abf80,0x3f51bf67,0x3f14bf35,0x3de5be64,0xbde50000,0xbf2b3f20,0xbf3f3f35,
0xbf803f80,0xbf803f80,0,0,0,0,0,0,0,0,0,0,0,0,0,0,
0xbe643f20,0x3f20be64,0x3ec40000,0x3f6dbf35,0x3f59bf20,0x3f7ebf7a,0x3f80bf7a,0x3f51bf72,0x3f48bf67,0x3edebf20,0x3e8ebede,0xbd66bde5,0xbea93e64,0xbf083ede,0xbf513f48,0xbf603f59,
0xbf803f80,0xbf803f80,0,0,0,0,0,0,0,0,0,0,0,0,0,0,
0x3f80bf67,0x3f20bf67,0x3f6dbf80,0x3ec4bf35,0x3f35bf67,0x3de5bede,0x3ec4bf20,0xbe2cbde5,0xbe64,0xbede3e64,0xbec43e64,0xbf2b3f08,0xbf353f20,0xbf593f48,0xbf6d3f67,0xbf763f72,
0xbf803f80,0xbf803f80,0,0,0,0,0,0,0,0,0,0,0,0,0,0,
0xbe64be64,0xbf673f20,0xbec40000,0xbf6d3f35,0xbf083e64,0xbf723f48,0xbf2b3ede,0xbf763f59,0xbf483f20,0xbf7a3f67,0xbf603f48,0xbf7c3f72,0xbf723f67,0xbf7e3f7a,0xbf7c3f7a,0xbf803f7e,
0xbf803f80,0xbf803f80,0,0,0,0,0,0,0,0,0,0,0,0,0,0,
0xbede8000,0xbf7a3f48,0xbf483f7a,0x80003ede,0xbec48000,0xbf6d3f35,0xbf6d3f80,0xbec43f35,0xbea98000,0xbf593f20,0xbf7e3f7a,0xbf353f67,0xbe8e8000,0xbf3f3f08,0xbf7c3f67,0xbf6d3f7e,
0xbe648000,0xbf203ede,0xbf673f48,0xbf803f7a,0xbe2c8000,0xbef83ea9,0xbf3f3f20,0xbf6d3f59,0xbde58000,0xbea93e64,0xbf083ede,0xbf353f20,0xbd668000,0xbe2c3de5,0xbe8e3e64,0xbec43ea9,
0x80008000,0x80008000,0x80008000,0x80008000,0,0,0,0,0,0,0,0,0,0,0,0,
0,0,0,0,0,0,0,0,0,0,0,0,0,0,0,0,
0x3f48bede,0x3f7abf7a,0x3edebf48,0xbede8000,0x3ec48000,0x3f6dbf35,0x3f6dbf80,0x3ec4bf35,0xbde53ede,0x3f08be64,0x3f72bf48,0x3f72bf80,0xbf143f48,0xbd663ea9,0x3ef8be64,0x3f60bf35,
0xbf673f7a,0xbf203f48,0xbe643ede,0x3e648000,0xbf803f7a,0xbf763f7e,0xbf513f67,0xbf143f35,0xbf593f48,0xbf723f67,0xbf7e3f7a,0xbf7e3f80,0xbef83ede,0xbf143f08,0xbf2b3f20,0xbf3f3f35,
0x80008000,0x80008000,0x80008000,0x80008000,0,0,0,0,0,0,0,0,0,0,0,0,
0,0,0,0,0,0,0,0,0,0,0,0,0,0,0,0,
0xbf7a3f48,0xbf483f7a,0x80003ede,0x3f48bede,0xbec48000,0xbf6d3f35,0xbf6d3f80,0xbec43f35,0x3f08bf48,0xbde5be64,0xbf353ede,0xbf7e3f67,0x3f80bf7a,0x3f51bf72,0x3ec4bf20,0xbe2cbde5,
0x3f20bede,0x3f67bf48,0x3f80bf7a,0x3f67bf7a,0xbe8e3ede,0x3d663de5,0x3ec4be64,0x3f2bbf08,0xbf723f7a,0xbf593f67,0xbf353f48,0xbf083f20,0xbf513f48,0xbf603f59,0xbf6d3f67,0xbf763f72,
0x80008000,0x80008000,0x80008000,0x80008000,0,0,0,0,0,0,0,0,0,0,0,0,
0,0,0,0,0,0,0,0,0,0,0,0,0,0,0,0,
0x3f7abf7a,0x3edebf48,0xbede8000,0xbf7a3f48,0x3ec48000,0x3f6dbf35,0x3f6dbf80,0x3ec4bf35,0xbf593f7a,0xbea93f20,0x3ea98000,0x3f59bf20,0xbf2b3ede,0xbf763f59,0xbf763f80,0xbf2b3f59,
0x3f20bf48,0x3e64bede,0xbe648000,0xbf203ede,0x3f60bf48,0x3f7cbf72,0x3f7cbf80,0x3f60bf72,0xbea93ede,0xbde53e64,0x3de58000,0x3ea9be64,0xbf7c3f7a,0xbf803f7e,0xbf803f80,0xbf7c3f7e,
0x80008000,0x80008000,0x80008000,0x80008000,0,0,0,0,0,0,0,0,0,0,0,0,
0,0,0,0,0,0,0,0,0,0,0,0,0,0,0,0,
0xbf483f7a,0x80003ede,0x3f48bede,0x3f7abf7a,0xbec48000,0xbf6d3f35,0xbf6d3f80,0xbec43f35,0x3f7ebf7a,0x3f35bf67,0x3de5bede,0xbf083e64,0xbe2c3ede,0x3ec4bde5,0x3f51bf20,0x3f80bf72,
0xbf673f48,0xbf803f7a,0xbf673f7a,0xbf203f48,0x3f2bbf48,0x3ec4bf08,0x3d66be64,0xbe8e3de5,0x3f08bede,0x3f35bf20,0x3f59bf48,0x3f72bf67,0xbf763f7a,0xbf6d3f72,0xbf603f67,0xbf513f59,
0x80008000,0x80008000,0x80008000,0x80008000,0,0,0,0,0,0,0,0,0,0,0,0,
0,0,0,0,0,0,0,0,0,0,0,0,0,0,0,0,
0x3edebf48,0xbede8000,0xbf7a3f48,0xbf483f7a,0x3ec48000,0x3f6dbf35,0x3f6dbf80,0x3ec4bf35,0xbf723f48,0xbf723f80,0xbf083f48,0x3de53e64,0x3f60bf7a,0x3ef8bf35,0xbd66be64,0xbf143ea9,
0xbe643ede,0x3e648000,0x3f20bede,0x3f67bf48,0xbf143ede,0xbf513f35,0xbf763f67,0xbf803f7e,0x3f7ebf7a,0x3f7ebf80,0x3f72bf7a,0x3f59bf67,0xbf3f3f48,0xbf2b3f35,0xbf143f20,0xbef83f08,
0x80008000,0x80008000,0x80008000,0x80008000,0,0,0,0,0,0,0,0,0,0,0,0,
0,0,0,0,0,0,0,0,0,0,0,0,0,0,0,0,
0x80003ede,0x3f48bede,0x3f7abf7a,0x3edebf48,0xbec48000,0xbf6d3f35,0xbf6d3f80,0xbec43f35,0x3f35bede,0x3f7ebf67,0x3f59bf7a,0x3ea9bf20,0xbf6d3f48,0xbf7c3f7e,0xbf3f3f67,0xbe8e3f08,
0x3f80bf7a,0x3f67bf7a,0x3f20bf48,0x3e64bede,0xbf6d3f7a,0xbf3f3f59,0xbef83f20,0xbe2c3ea9,0x3f35bf48,0x3f08bf20,0x3ea9bede,0x3de5be64,0xbec43ede,0xbe8e3ea9,0xbe2c3e64,0xbd663de5,
0x80008000,0x80008000,0x80008000,0x80008000,0,0,0,0,0,0,0,0,0,0,0,0,
0,0,0,0,0,0,0,0,0,0,0,0,0,0,0,0,
0xbede8000,0xbf7a3f48,0xbf483f7a,0x80003ede,0x3ec48000,0x3f6dbf35,0x3f6dbf80,0x3ec4bf35,0xbea98000,0xbf593f20,0xbf7e3f7a,0xbf353f67,0x3e8e8000,0x3f3fbf08,0x3f7cbf67,0x3f6dbf7e,
0xbe648000,0xbf203ede,0xbf673f48,0xbf803f7a,0x3e2c8000,0x3ef8bea9,0x3f3fbf20,0x3f6dbf59,0xbde58000,0xbea93e64,0xbf083ede,0xbf353f20,0x3d668000,0x3e2cbde5,0x3e8ebe64,0x3ec4bea9,
0x80008000,0x80008000,0x80008000,0x80008000,0,0,0,0,0,0,0,0,0,0,0,0,
0,0,0,0,0,0,0,0,0,0,0,0,0,0,0,0,
0x3f48bede,0x3f7abf7a,0x3edebf48,0xbede8000,0xbec48000,0xbf6d3f35,0xbf6d3f80,0xbec43f35,0xbde53ede,0x3f08be64,0x3f72bf48,0x3f72bf80,0x3f14bf48,0x3d66bea9,0xbef83e64,0xbf603f35,
0xbf673f7a,0xbf203f48,0xbe643ede,0x3e648000,0x3f80bf7a,0x3f76bf7e,0x3f51bf67,0x3f14bf35,0xbf593f48,0xbf723f67,0xbf7e3f7a,0xbf7e3f80,0x3ef8bede,0x3f14bf08,0x3f2bbf20,0x3f3fbf35,
0x80008000,0x80008000,0x80008000,0x80008000,0,0,0,0,0,0,0,0,0,0,0,0,
0,0,0,0,0,0,0,0,0,0,0,0,0,0,0,0,
0xbf7a3f48,0xbf483f7a,0x80003ede,0x3f48bede,0x3ec48000,0x3f6dbf35,0x3f6dbf80,0x3ec4bf35,0x3f08bf48,0xbde5be64,0xbf353ede,0xbf7e3f67,0xbf803f7a,0xbf513f72,0xbec43f20,0x3e2c3de5,
0x3f20bede,0x3f67bf48,0x3f80bf7a,0x3f67bf7a,0x3e8ebede,0xbd66bde5,0xbec43e64,0xbf2b3f08,0xbf723f7a,0xbf593f67,0xbf353f48,0xbf083f20,0x3f51bf48,0x3f60bf59,0x3f6dbf67,0x3f76bf72,
0x80008000,0x80008000,0x80008000,0x80008000,0,0,0,0,0,0,0,0,0,0,0,0,
0,0,0,0,0,0,0,0,0,0,0,0,0,0,0,0,
0x3f7abf7a,0x3edebf48,0xbede8000,0xbf7a3f48,0xbec48000,0xbf6d3f35,0xbf6d3f80,0xbec43f35,0xbf593f7a,0xbea93f20,0x3ea98000,0x3f59bf20,0x3f2bbede,0x3f76bf59,0x3f76bf80,0x3f2bbf59,
0x3f20bf48,0x3e64bede,0xbe648000,0xbf203ede,0xbf603f48,0xbf7c3f72,0xbf7c3f80,0xbf603f72,0xbea93ede,0xbde53e64,0x3de58000,0x3ea9be64,0x3f7cbf7a,0x3f80bf7e,0x3f80bf80,0x3f7cbf7e,
0x80008000,0x80008000,0x80008000,0x80008000,0,0,0,0,0,0,0,0,0,0,0,0,
0,0,0,0,0,0,0,0,0,0,0,0,0,0,0,0,
0xbf483f7a,0x80003ede,0x3f48bede,0x3f7abf7a,0x3ec48000,0x3f6dbf35,0x3f6dbf80,0x3ec4bf35,0x3f7ebf7a,0x3f35bf67,0x3de5bede,0xbf083e64,0x3e2cbede,0xbec43de5,0xbf513f20,0xbf803f72,
0xbf673f48,0xbf803f7a,0xbf673f7a,0xbf203f48,0xbf2b3f48,0xbec43f08,0xbd663e64,0x3e8ebde5,0x3f08bede,0x3f35bf20,0x3f59bf48,0x3f72bf67,0x3f76bf7a,0x3f6dbf72,0x3f60bf67,0x3f51bf59,
0x80008000,0x80008000,0x80008000,0x80008000,0,0,0,0,0,0,0,0,0,0,0,0,
0,0,0,0,0,0,0,0,0,0,0,0,0,0,0,0,
0x3edebf48,0xbede8000,0xbec48000,0xbf6d3f35,0xbf723f48,0xbf723f80,0xbf603f7a,0xbef83f35,0xbe643ede,0x3e648000,0x3f14bede,0x3f51bf35,0x3f7ebf7a,0x3f7ebf80,0x3f3fbf48,0x3f2bbf35,
0x80008000,0x80008000,0,0,0,0,0,0,0,0,0,0,0,0,0,0,
0xbf7a3f48,0xbf483f7a,0xbf6d3f80,0xbec43f35,0xbf083f48,0x3de53e64,0x3d663e64,0x3f14bea9,0x3f20bede,0x3f67bf48,0x3f76bf67,0x3f80bf7e,0x3f72bf7a,0x3f59bf67,0x3f14bf20,0x3ef8bf08,
0x80008000,0x80008000,0,0,0,0,0,0,0,0,0,0,0,0,0,0,
0x80003ede,0x3f48bede,0x3ec48000,0x3f6dbf35,0x3f35bede,0x3f7ebf67,0x3f6dbf48,0x3f7cbf7e,0x3f80bf7a,0x3f67bf7a,0x3f6dbf7a,0x3f3fbf59,0x3f35bf48,0x3f08bf20,0x3ec4bede,0x3e8ebea9,
0x80008000,0x80008000,0,0,0,0,0,0,0,0,0,0,0,0,0,0,
0x3f7abf7a,0x3edebf48,0x3f6dbf80,0x3ec4bf35,0x3f59bf7a,0x3ea9bf20,0x3f3fbf67,0x3e8ebf08,0x3f20bf48,0x3e64bede,0x3ef8bf20,0x3e2cbea9,0x3ea9bede,0x3de5be64,0x3e2cbe64,0x3d66bde5,
0x80008000,0x80008000,0,0,0,0,0,0,0,0,0,0,0,0,0,0,
0x3f80,0x80004000,0x80004000,0x80004000,0x3f80,0xbde64000,0xbe653ffe,0xbeac3ffc,0x3f80,0xbe653ffe,0xbee43ffa,0xbf293ff2,0x3f80,0xbeac3ffc,0xbf293ff2,0xbf783fe0,
0x3f80,0xbee43ffa,0xbf5e3fe7,0xbfa03fc8,0x3f80,0xbf0e3ff6,0xbf883fd9,0xbfbf3fab,0x3f80,0xbf293ff2,0xbfa03fc8,0xbfd93f88,0x3f80,0xbf443fed,0xbfb53fb5,0xbfed3f44,
0x3f80,0xbf5e3fe7,0xbfc83fa0,0xbffa3ee4,0x3f80,0xbf783fe0,0xbfd93f88,0xc0003de6,0x3f80,0xbf883fd9,0xbfe73f5e,0xbffebe65,0x3f80,0xbf943fd1,0xbff23f29,0xbff6bf0e,
0x3f80,0xbfa03fc8,0xbffa3ee4,0xbfe7bf5e,0x3f80,0xbfab3fbf,0xbffe3e65,0xbfd1bf94,0x3f80,0xbfb53fb5,0xc0000000,0xbfb5bfb5,0x3f80,0xbfbf3fab,0xbffebe65,0xbf94bfd1,
0x80004000,0x80004000,0x80004000,0x80004000,0xbee43ffa,0xbf0e3ff6,0xbf293ff2,0xbf443fed,0xbf5e3fe7,0xbf883fd9,0xbfa03fc8,0xbfb53fb5,0xbfa03fc8,0xbfbf3fab,0xbfd93f88,0xbfed3f44,
0xbfc83fa0,0xbfe73f5e,0xbffa3ee4,0xc0000000,0xbfe73f5e,0xbffc3eac,0xbffebe65,0xbfedbf44,0xbffa3ee4,0xbffebe65,0xbfe7bf5e,0xbfb5bfb5,0xc0000000,0xbfedbf44,0xbfb5bfb5,0xbf44bfed,
0xbffabee4,0xbfc8bfa0,0xbf5ebfe7,0x8000c000,0xbfe7bf5e,0xbf94bfd1,0xbe65bffe,0x3f44bfed,0xbfc8bfa0,0xbf29bff2,0x3ee4bffa,0x3fb5bfb5,0xbfa0bfc8,0xbde6c000,0x3f88bfd9,0x3fedbf44,
0xbf5ebfe7,0x3ee4bffa,0x3fc8bfa0,0x40000000,0xbee4bffa,0x3f78bfe0,0x3ff2bf29,0x3fed3f44,0x8000c000,0x3fb5bfb5,0x40000000,0x3fb53fb5,0x3ee4bffa,0x3fe0bf78,0x3ff23f29,0x3f443fed,
0x80004000,0x80004000,0x80004000,0x80004000,0xbf5e3fe7,0xbf783fe0,0xbf883fd9,0xbf943fd1,0xbfc83fa0,0xbfd93f88,0xbfe73f5e,0xbff23f29,0xbffa3ee4,0xc0003de6,0xbffebe65,0xbff6bf0e,
0xbffabee4,0xbfe7bf5e,0xbfc8bfa0,0xbfa0bfc8,0xbfc8bfa0,0xbf94bfd1,0xbf29bff2,0xbde6c000,0xbf5ebfe7,0xbe65bffe,0x3ee4bffa,0x3f88bfd9,0x8000c000,0x3f44bfed,0x3fb5bfb5,0x3fedbf44,
0x3f5ebfe7,0x3fc8bfa0,0x3ffabee4,0x3ffa3ee4,0x3fc8bfa0,0x3ffcbeac,0x3ff23f29,0x3fab3fbf,0x3ffabee4,0x3ff23f29,0x3fa03fc8,0x3e653ffe,0x3ffa3ee4,0x3fab3fbf,0x3e653ffe,0xbf783fe0,
0x3fc83fa0,0x3ee43ffa,0xbf5e3fe7,0xbfe73f5e,0x3f5e3fe7,0xbf0e3ff6,0xbfd93f88,0xbffcbeac,0x80004000,0xbfb53fb5,0xc0000000,0xbfb5bfb5,0xbf5e3fe7,0xbff63f0e,0xbfd9bf88,0xbeacbffc,
0x80004000,0x80004000,0x80004000,0x80004000,0xbfa03fc8,0xbfab3fbf,0xbfb53fb5,0xbfbf3fab,0xbffa3ee4,0xbffe3e65,0xc0000000,0xbffebe65,0xbfe7bf5e,0xbfd1bf94,0xbfb5bfb5,0xbf94bfd1,
0xbf5ebfe7,0xbee4bffa,0x8000c000,0x3ee4bffa,0x3ee4bffa,0x3f78bfe0,0x3fb5bfb5,0x3fe0bf78,0x3fc8bfa0,0x3ff2bf29,0x40000000,0x3ff23f29,0x40000000,0x3fed3f44,0x3fb53fb5,0x3f443fed,
0x3fc83fa0,0x3f5e3fe7,0x80004000,0xbf5e3fe7,0x3ee43ffa,0xbf0e3ff6,0xbfb53fb5,0xbff63f0e,0xbf5e3fe7,0xbfd93f88,0xc0000000,0xbfd9bf88,0xbfe73f5e,0xbffcbeac,0xbfb5bfb5,0xbeacbffc,
0xbffabee4,0xbfa0bfc8,0x8000c000,0x3fa0bfc8,0xbfa0bfc8,0x3de6c000,0x3fb5bfb5,0x4000bde6,0x8000c000,0x3fb5bfb5,0x40000000,0x3fb53fb5,0x3fa0bfc8,0x4000bde6,0x3fb53fb5,0xbde64000,
0x80004000,0x80004000,0x80004000,0x80004000,0xbfc83fa0,0xbfd13f94,0xbfd93f88,0xbfe03f78,0xbffabee4,0xbff2bf29,0xbfe7bf5e,0xbfd9bf88,0xbf5ebfe7,0xbf0ebff6,0xbe65bffe,0x3de6c000,
0x3f5ebfe7,0x3fa0bfc8,0x3fc8bfa0,0x3fe7bf5e,0x3ffabee4,0x40003de6,0x3ff23f29,0x3fd13f94,0x3fc83fa0,0x3f883fd9,0x3ee43ffa,0xbe653ffe,0x80004000,0xbf443fed,0xbfb53fb5,0xbfed3f44,
0xbfc83fa0,0xbffa3ee4,0xbffabee4,0xbfc8bfa0,0xbffabee4,0xbfbfbfab,0xbf29bff2,0x3eacbffc,0xbf5ebfe7,0x3e65bffe,0x3fa0bfc8,0x3ff2bf29,0x3f5ebfe7,0x3fe0bf78,0x3ffe3e65,0x3fbf3fab,
0x3ffabee4,0x3fe73f5e,0x3f5e3fe7,0xbee43ffa,0x3fc83fa0,0x3eac3ffc,0xbf883fd9,0xbff63f0e,0x80004000,0xbfb53fb5,0xc0000000,0xbfb5bfb5,0xbfc83fa0,0xbffcbeac,0xbf88bfd9,0x3f0ebff6,
0x80004000,0x80004000,0x80004000,0x80004000,0xbfe73f5e,0xbfed3f44,0xbff23f29,0xbff63f0e,0xbfc8bfa0,0xbfb5bfb5,0xbfa0bfc8,0xbf88bfd9,0x3ee4bffa,0x3f44bfed,0x3f88bfd9,0x3fabbfbf,
0x3ffabee4,0x40000000,0x3ffa3ee4,0x3fe73f5e,0x3fa03fc8,0x3f443fed,0x3e653ffe,0xbeac3ffc,0xbf5e3fe7,0xbfb53fb5,0xbfe73f5e,0xbffe3e65,0xc0000000,0xbfedbf44,0xbfb5bfb5,0xbf44bfed,
0xbf5ebfe7,0x8000c000,0x3f5ebfe7,0x3fc8bfa0,0x3fa0bfc8,0x3fedbf44,0x3ffe3e65,0x3fd13f94,0x3ffa3ee4,0x3fb53fb5,0x3ee43ffa,0xbf293ff2,0x3ee43ffa,0xbf443fed,0xbfd93f88,0xc000bde6,
0xbfc83fa0,0xc0000000,0xbfc8bfa0,0xbee4bffa,0xbfe7bf5e,0xbf44bfed,0x3f29bff2,0x3fe0bf78,0x8000c000,0x3fb5bfb5,0x40000000,0x3fb53fb5,0x3fe7bf5e,0x3fed3f44,0x3f293ff2,0xbf783fe0,
0x80004000,0x80004000,0x80004000,0x80004000,0xbffa3ee4,0xbffc3eac,0xbffe3e65,0xc0003de6,0xbf5ebfe7,0xbf29bff2,0xbee4bffa,0xbe65bffe,0x3fc8bfa0,0x3fe0bf78,0x3ff2bf29,0x3ffcbeac,
0x3fc83fa0,0x3fa03fc8,0x3f5e3fe7,0x3ee43ffa,0xbf5e3fe7,0xbfab3fbf,0xbfd93f88,0xbff63f0e,0xbffabee4,0xbfd9bf88,0xbfa0bfc8,0xbf29bff2,0x8000c000,0x3f44bfed,0x3fb5bfb5,0x3fedbf44,
0x3ffabee4,0x3ffa3ee4,0x3fc83fa0,0x3f5e3fe7,0x3f5e3fe7,0xbde64000,0xbf883fd9,0xbfe03f78,0xbfc83fa0,0xbffe3e65,0xbfe7bf5e,0xbf88bfd9,0xbfc8bfa0,0xbf0ebff6,0x3f29bff2,0x3fd1bf94,
0x3f5ebfe7,0x3fe7bf5e,0x3ffa3ee4,0x3fa03fc8,0x3ffa3ee4,0x3f943fd1,0xbe653ffe,0xbfbf3fab,0x80004000,0xbfb53fb5,0xc0000000,0xbfb5bfb5,0xbffa3ee4,0xbfd1bf94,0xbe65bffe,0x3fabbfbf,
0x80004000,0x80004000,0x80004000,0x80004000,0xc0000000,0xc000bde6,0xbffebe65,0xbffcbeac,0x8000c000,0x3e65bffe,0x3ee4bffa,0x3f29bff2,0x40000000,0x3ffc3eac,0x3ff23f29,0x3fe03f78,
0x80004000,0xbee43ffa,0xbf5e3fe7,0xbfa03fc8,0xc0000000,0xbff6bf0e,0xbfd9bf88,0xbfabbfbf,0x8000c000,0x3f29bff2,0x3fa0bfc8,0x3fd9bf88,0x40000000,0x3fed3f44,0x3fb53fb5,0x3f443fed,
0x80004000,0xbf5e3fe7,0xbfc83fa0,0xbffa3ee4,0xc0000000,0xbfe0bf78,0xbf88bfd9,0xbde6c000,0x8000c000,0x3f88bfd9,0x3fe7bf5e,0x3ffe3e65,0x40000000,0x3fd13f94,0x3f293ff2,0xbf0e3ff6,
0x80004000,0xbfa03fc8,0xbffa3ee4,0xbfe7bf5e,0xc0000000,0xbfbfbfab,0xbe65bffe,0x3f94bfd1,0x8000c000,0x3fb5bfb5,0x40000000,0x3fb53fb5,0x40000000,0x3fab3fbf,0xbe653ffe,0xbfd13f94,
0x80004000,0x80004000,0x80004000,0x80004000,0xbffabee4,0xbff6bf0e,0xbff2bf29,0xbfedbf44,0x3f5ebfe7,0x3f88bfd9,0x3fa0bfc8,0x3fb5bfb5,0x3fc83fa0,0x3fab3fbf,0x3f883fd9,0x3f443fed,
0xbfc83fa0,0xbfe73f5e,0xbffa3ee4,0xc0000000,0xbf5ebfe7,0xbeacbffc,0x3e65bffe,0x3f44bfed,0x3ffabee4,0x3ffe3e65,0x3fe73f5e,0x3fb53fb5,0x80004000,0xbf443fed,0xbfb53fb5,0xbfed3f44,
0xbffabee4,0xbfc8bfa0,0xbf5ebfe7,0x8000c000,0x3f5ebfe7,0x3fd1bf94,0x3ffebe65,0x3fed3f44,0x3fc83fa0,0x3f293ff2,0xbee43ffa,0xbfb53fb5,0xbfc83fa0,0xc0003de6,0xbfd9bf88,0xbf44bfed,
0xbf5ebfe7,0x3ee4bffa,0x3fc8bfa0,0x40000000,0x3ffabee4,0x3fe03f78,0x3f293ff2,0xbf443fed,0x80004000,0xbfb53fb5,0xc0000000,0xbfb5bfb5,0xbffabee4,0xbf78bfe0,0x3f29bff2,0x3fedbf44,
0x80004000,0x80004000,0x80004000,0x80004000,0xbfe7bf5e,0xbfe0bf78,0xbfd9bf88,0xbfd1bf94,0x3fc8bfa0,0x3fd9bf88,0x3fe7bf5e,0x3ff2bf29,0x3ee43ffa,0x3de64000,0xbe653ffe,0xbf0e3ff6,
0xbffabee4,0xbfe7bf5e,0xbfc8bfa0,0xbfa0bfc8,0x3fa0bfc8,0x3fd1bf94,0x3ff2bf29,0x4000bde6,0x3f5e3fe7,0x3e653ffe,0xbee43ffa,0xbf883fd9,0xc0000000,0xbfedbf44,0xbfb5bfb5,0xbf44bfed,
0x3f5ebfe7,0x3fc8bfa0,0x3ffabee4,0x3ffa3ee4,0x3fa03fc8,0x3eac3ffc,0xbf293ff2,0xbfbf3fab,0xbffa3ee4,0xbff2bf29,0xbfa0bfc8,0xbe65bffe,0x3ee4bffa,0x3fbfbfab,0x3ffebe65,0x3fe03f78,
0x3fc83fa0,0x3ee43ffa,0xbf5e3fe7,0xbfe73f5e,0xbfe73f5e,0xbff6bf0e,0xbf88bfd9,0x3eacbffc,0x8000c000,0x3fb5bfb5,0x40000000,0x3fb53fb5,0x3fe73f5e,0x3f0e3ff6,0xbf883fd9,0xbffc3eac,
0x80004000,0x80004000,0x80004000,0x80004000,0xbfc8bfa0,0xbfbfbfab,0xbfb5bfb5,0xbfabbfbf,0x3ffabee4,0x3ffebe65,0x40000000,0x3ffe3e65,0xbf5e3fe7,0xbf943fd1,0xbfb53fb5,0xbfd13f94,
0xbf5ebfe7,0xbee4bffa,0x8000c000,0x3ee4bffa,0x3ffa3ee4,0x3fe03f78,0x3fb53fb5,0x3f783fe0,0xbfc83fa0,0xbff23f29,0xc0000000,0xbff2bf29,0x8000c000,0x3f44bfed,0x3fb5bfb5,0x3fedbf44,
0x3fc83fa0,0x3f5e3fe7,0x80004000,0xbf5e3fe7,0xbffa3ee4,0xbff6bf0e,0xbfb5bfb5,0xbf0ebff6,0x3f5ebfe7,0x3fd9bf88,0x40000000,0x3fd93f88,0x3f5e3fe7,0xbeac3ffc,0xbfb53fb5,0xbffc3eac,
0xbffabee4,0xbfa0bfc8,0x8000c000,0x3fa0bfc8,0x3fc8bfa0,0x40003de6,0x3fb53fb5,0x3de64000,0x80004000,0xbfb53fb5,0xc0000000,0xbfb5bfb5,0xbfc8bfa0,0xbde6c000,0x3fb5bfb5,0x40003de6,
0x80004000,0x80004000,0x80004000,0x80004000,0xbfa0bfc8,0xbf94bfd1,0xbf88bfd9,0xbf78bfe0,0x3ffa3ee4,0x3ff23f29,0x3fe73f5e,0x3fd93f88,0xbfe73f5e,0xbff63f0e,0xbffe3e65,0xc000bde6,
0x3f5ebfe7,0x3fa0bfc8,0x3fc8bfa0,0x3fe7bf5e,0x3ee43ffa,0xbde64000,0xbf293ff2,0xbf943fd1,0xbfc8bfa0,0xbf88bfd9,0xbee4bffa,0x3e65bffe,0x40000000,0x3fed3f44,0x3fb53fb5,0x3f443fed,
0xbfc83fa0,0xbffa3ee4,0xbffabee4,0xbfc8bfa0,0x3ee4bffa,0x3fabbfbf,0x3ff2bf29,0x3ffc3eac,0x3f5e3fe7,0xbe653ffe,0xbfa03fc8,0xbff23f29,0xbfe7bf5e,0xbf78bfe0,0x3e65bffe,0x3fabbfbf,
0x3ffabee4,0x3fe73f5e,0x3f5e3fe7,0xbee43ffa,0xbfa03fc8,0xbffc3eac,0xbfd9bf88,0xbf0ebff6,0x8000c000,0x3fb5bfb5,0x40000000,0x3fb53fb5,0x3fa03fc8,0xbeac3ffc,0xbfd93f88,0xbff6bf0e,
0x80004000,0x80004000,0x80004000,0x80004000,0xbf5ebfe7,0xbf44bfed,0xbf29bff2,0xbf0ebff6,0x3fc83fa0,0x3fb53fb5,0x3fa03fc8,0x3f883fd9,0xbffabee4,0xbfedbf44,0xbfd9bf88,0xbfbfbfab,
0x3ffabee4,0x40000000,0x3ffa3ee4,0x3fe73f5e,0xbfc83fa0,0xbfed3f44,0xbffe3e65,0xbffcbeac,0x3f5ebfe7,0x3fb5bfb5,0x3fe7bf5e,0x3ffebe65,0x80004000,0xbf443fed,0xbfb53fb5,0xbfed3f44,
0xbf5ebfe7,0x8000c000,0x3f5ebfe7,0x3fc8bfa0,0x3fc83fa0,0x3f443fed,0xbe653ffe,0xbf943fd1,0xbffabee4,0xbfb5bfb5,0xbee4bffa,0x3f29bff2,0x3ffabee4,0x3fed3f44,0x3f883fd9,0xbde64000,
0xbfc83fa0,0xc0000000,0xbfc8bfa0,0xbee4bffa,0x3f5ebfe7,0x3fedbf44,0x3ff23f29,0x3f783fe0,0x80004000,0xbfb53fb5,0xc0000000,0xbfb5bfb5,0xbf5ebfe7,0x3f44bfed,0x3ff2bf29,0x3fe03f78,
0x80004000,0x80004000,0x80004000,0x80004000,0xbee4bffa,0xbeacbffc,0xbe65bffe,0xbde6c000,0x3f5e3fe7,0x3f293ff2,0x3ee43ffa,0x3e653ffe,0xbfa0bfc8,0xbf78bfe0,0xbf29bff2,0xbeacbffc,
0x3fc83fa0,0x3fa03fc8,0x3f5e3fe7,0x3ee43ffa,0xbfe7bf5e,0xbfbfbfab,0xbf88bfd9,0xbf0ebff6,0x3ffa3ee4,0x3fd93f88,0x3fa03fc8,0x3f293ff2,0xc0000000,0xbfedbf44,0xbfb5bfb5,0xbf44bfed,
0x3ffabee4,0x3ffa3ee4,0x3fc83fa0,0x3f5e3fe7,0xbfe73f5e,0xc000bde6,0xbfd9bf88,0xbf78bfe0,0x3fc8bfa0,0x3ffebe65,0x3fe73f5e,0x3f883fd9,0xbfa03fc8,0xbff63f0e,0xbff2bf29,0xbf94bfd1,
0x3f5ebfe7,0x3fe7bf5e,0x3ffa3ee4,0x3fa03fc8,0xbee43ffa,0xbfd13f94,0xbffebe65,0xbfabbfbf,0x8000c000,0x3fb5bfb5,0x40000000,0x3fb53fb5,0x3ee43ffa,0xbf943fd1,0xbffe3e65,0xbfbfbfab,
0x3f80,0,0,0,0xbf80,0,0,0,0x3f80,0,0,0,0xbf80,0,0,0,
0x3f80,0,0,0,0xbf80,0,0,0,0x3f80,0,0,0,0xbf80,0,0,0,
0x3f80,0,0,0,0xbf80,0,0,0,0x3f80,0,0,0,0xbf80,0,0,0,
0x3f80,0,0,0,0xbf80,0,0,0,0x3f80,0,0,0,0xbf80,0,0,0,
0,0,0,0,0,0,0,0,0,0,0,0,0,0,0,0,
0,0,0,0,0,0,0,0,0,0,0,0,0,0,0,0,
0,0,0,0,0,0,0,0,0,0,0,0,0,0,0,0,
0,0,0,0,0,0,0,0,0,0,0,0,0,0,0,0,
0x3f80,0xbfc83fa0,0xbffabee4,0xbf5ebfe7,0x3f80,0xbfd13f94,0xbff2bf29,0xbf0ebff6,0x3f80,0xbfd93f88,0xbfe7bf5e,0xbe65bffe,0x3f80,0xbfe03f78,0xbfd9bf88,0x3de6c000,
0x3f80,0xbfe73f5e,0xbfc8bfa0,0x3ee4bffa,0x3f80,0xbfed3f44,0xbfb5bfb5,0x3f44bfed,0x3f80,0xbff23f29,0xbfa0bfc8,0x3f88bfd9,0x3f80,0xbff63f0e,0xbf88bfd9,0x3fabbfbf,
0x3f80,0xbffa3ee4,0xbf5ebfe7,0x3fc8bfa0,0x3f80,0xbffc3eac,0xbf29bff2,0x3fe0bf78,0x3f80,0xbffe3e65,0xbee4bffa,0x3ff2bf29,0x3f80,0xc0003de6,0xbe65bffe,0x3ffcbeac,
0,0,0,0,0,0,0,0,0,0,0,0,0,0,0,0,
0x3f5ebfe7,0x3ffabee4,0x3fc83fa0,0x80004000,0x3fa0bfc8,0x40003de6,0x3f883fd9,0xbf443fed,0x3fc8bfa0,0x3ff23f29,0x3ee43ffa,0xbfb53fb5,0x3fe7bf5e,0x3fd13f94,0xbe653ffe,0xbfed3f44,
0x3ffabee4,0x3fa03fc8,0xbf5e3fe7,0xc0000000,0x40000000,0x3f443fed,0xbfb53fb5,0xbfedbf44,0x3ffa3ee4,0x3e653ffe,0xbfe73f5e,0xbfb5bfb5,0x3fe73f5e,0xbeac3ffc,0xbffe3e65,0xbf44bfed,
0x3fc83fa0,0xbf5e3fe7,0xbffabee4,0x8000c000,0x3fa03fc8,0xbfab3fbf,0xbfd9bf88,0x3f44bfed,0x3f5e3fe7,0xbfd93f88,0xbfa0bfc8,0x3fb5bfb5,0x3ee43ffa,0xbff63f0e,0xbf29bff2,0x3fedbf44,
0,0,0,0,0,0,0,0,0,0,0,0,0,0,0,0,
0xbfc83fa0,0xbffabee4,0xbf5ebfe7,0x3f5ebfe7,0xbffa3ee4,0xbfbfbfab,0x3e65bffe,0x3fe0bf78,0xbffabee4,0xbf29bff2,0x3fa0bfc8,0x3ffe3e65,0xbfc8bfa0,0x3eacbffc,0x3ff2bf29,0x3fbf3fab,
0xbf5ebfe7,0x3fa0bfc8,0x3ffa3ee4,0x3ee43ffa,0x8000c000,0x3fedbf44,0x3fb53fb5,0xbf443fed,0x3f5ebfe7,0x3ffe3e65,0x3ee43ffa,0xbfd93f88,0x3fc8bfa0,0x3fd13f94,0xbf293ff2,0xc000bde6,
0x3ffabee4,0x3f5e3fe7,0xbfc83fa0,0xbfc8bfa0,0x3ffa3ee4,0xbde64000,0xbffe3e65,0xbf0ebff6,0x3fc83fa0,0xbf883fd9,0xbfe7bf5e,0x3f29bff2,0x3f5e3fe7,0xbfe03f78,0xbf88bfd9,0x3fd1bf94,
0,0,0,0,0,0,0,0,0,0,0,0,0,0,0,0,
0x3ffabee4,0x3fc83fa0,0x80004000,0xbfc83fa0,0x3fe73f5e,0x3eac3ffc,0xbfb53fb5,0xbffcbeac,0x3f5e3fe7,0xbf883fd9,0xc0000000,0xbf88bfd9,0xbee43ffa,0xbff63f0e,0xbfb5bfb5,0x3f0ebff6,
0xbfc83fa0,0xbfe7bf5e,0x8000c000,0x3fe7bf5e,0xc0000000,0xbf44bfed,0x3fb5bfb5,0x3fed3f44,0xbfc8bfa0,0x3f29bff2,0x40000000,0x3f293ff2,0xbee4bffa,0x3fe0bf78,0x3fb53fb5,0xbf783fe0,
0x3f5ebfe7,0x3ffa3ee4,0x80004000,0xbffa3ee4,0x3fe7bf5e,0x3f943fd1,0xbfb53fb5,0xbfd1bf94,0x3ffa3ee4,0xbe653ffe,0xc0000000,0xbe65bffe,0x3fa03fc8,0xbfbf3fab,0xbfb5bfb5,0x3fabbfbf,
0,0,0,0,0,0,0,0,0,0,0,0,0,0,0,0,
0xbffabee4,0xbf5ebfe7,0x3f5ebfe7,0x3ffabee4,0xbf5ebfe7,0x3f78bfe0,0x3ffebe65,0x3fab3fbf,0x3f5ebfe7,0x3ffebe65,0x3fa03fc8,0xbf293ff2,0x3ffabee4,0x3fab3fbf,0xbf293ff2,0xbffc3eac,
0x3fc83fa0,0xbee43ffa,0xbffa3ee4,0xbfa0bfc8,0x80004000,0xbfed3f44,0xbfb5bfb5,0x3f44bfed,0xbfc83fa0,0xbfd9bf88,0x3ee4bffa,0x3ffebe65,0xbffabee4,0xbde6c000,0x3ff2bf29,0x3f943fd1,
0xbf5ebfe7,0x3fc8bfa0,0x3fc83fa0,0xbf5e3fe7,0x3f5ebfe7,0x3ff63f0e,0xbe653ffe,0xc0003de6,0x3ffabee4,0x3f293ff2,0xbfe73f5e,0xbf88bfd9,0x3fc83fa0,0xbf943fd1,0xbfd9bf88,0x3f78bfe0,
0,0,0,0,0,0,0,0,0,0,0,0,0,0,0,0,
0x3fc83fa0,0x80004000,0xbfc83fa0,0xbffabee4,0xbee43ffa,0xbfed3f44,0xbfd9bf88,0xbde6c000,0xbffa3ee4,0xbfb5bfb5,0x3ee4bffa,0x3ff2bf29,0xbfa0bfc8,0x3f44bfed,0x3ffebe65,0x3f943fd1,
0x3f5ebfe7,0x40000000,0x3f5e3fe7,0xbfa03fc8,0x40000000,0x3f443fed,0xbfb53fb5,0xbfedbf44,0x3f5e3fe7,0xbfb53fb5,0xbfe7bf5e,0x3e65bffe,0xbfa03fc8,0xbfedbf44,0x3e65bffe,0x3ffcbeac,
0xbffabee4,0x8000c000,0x3ffabee4,0x3f5e3fe7,0xbee4bffa,0x3fedbf44,0x3f883fd9,0xbfbf3fab,0x3fc8bfa0,0x3fb53fb5,0xbfa03fc8,0xbfd9bf88,0x3fe73f5e,0xbf443fed,0xbff2bf29,0x3f0ebff6,
0,0,0,0,0,0,0,0,0,0,0,0,0,0,0,0,
0xbf5ebfe7,0x3f5ebfe7,0x3ffabee4,0x3fc83fa0,0x3fc8bfa0,0x3ff63f0e,0x3f293ff2,0xbf943fd1,0x3fc83fa0,0xbe653ffe,0xbfe73f5e,0xbfd9bf88,0xbf5e3fe7,0xc0003de6,0xbf88bfd9,0x3f78bfe0,
0xbffabee4,0xbee4bffa,0x3fc8bfa0,0x3fe73f5e,0x8000c000,0x3fedbf44,0x3fb53fb5,0xbf443fed,0x3ffabee4,0x3f883fd9,0xbfa03fc8,0xbff2bf29,0x3f5e3fe7,0xbfbf3fab,0xbfd9bf88,0x3f0ebff6,
0xbfc83fa0,0xbfc8bfa0,0x3f5ebfe7,0x3ffa3ee4,0xbfc8bfa0,0x3f78bfe0,0x3ff23f29,0xbeac3ffc,0x3f5ebfe7,0x3ff23f29,0xbee43ffa,0xbffebe65,0x3ffa3ee4,0xbeac3ffc,0xbffebe65,0x3de6c000,
0,0,0,0,0,0,0,0,0,0,0,0,0,0,0,0,
0x80004000,0xbfc83fa0,0xbffabee4,0xbf5ebfe7,0xc0000000,0xbf94bfd1,0x3f29bff2,0x3ff6bf0e,0x8000c000,0x3fd9bf88,0x3fe73f5e,0x3e653ffe,0x40000000,0x3f783fe0,0xbf883fd9,0xc000bde6,
0x80004000,0xbfe73f5e,0xbfc8bfa0,0x3ee4bffa,0xc0000000,0xbf44bfed,0x3fb5bfb5,0x3fed3f44,0x8000c000,0x3ff2bf29,0x3fa03fc8,0xbf883fd9,0x40000000,0x3f0e3ff6,0xbfd93f88,0xbfbfbfab,
0x80004000,0xbffa3ee4,0xbf5ebfe7,0x3fc8bfa0,0xc0000000,0xbeacbffc,0x3ff2bf29,0x3f783fe0,0x8000c000,0x3ffebe65,0x3ee43ffa,0xbff23f29,0x40000000,0x3de64000,0xbffe3e65,0xbeacbffc,
0,0,0,0,0,0,0,0,0,0,0,0,0,0,0,0,
0x3f5ebfe7,0x3ffabee4,0x3fc83fa0,0x80004000,0x3fc83fa0,0xbde64000,0xbfd93f88,0xbfedbf44,0xbfc83fa0,0xbff2bf29,0xbee4bffa,0x3fb5bfb5,0xbf5ebfe7,0x3f94bfd1,0x3ffe3e65,0x3f443fed,
0x3ffabee4,0x3fa03fc8,0xbf5e3fe7,0xc0000000,0x80004000,0xbfed3f44,0xbfb5bfb5,0x3f44bfed,0xbffabee4,0xbe65bffe,0x3fe7bf5e,0x3fb53fb5,0x3f5ebfe7,0x3ffc3eac,0x3e653ffe,0xbfed3f44,
0x3fc83fa0,0xbf5e3fe7,0xbffabee4,0x8000c000,0xbfc83fa0,0xbfbfbfab,0x3f88bfd9,0x3fed3f44,0xbf5ebfe7,0x3fd9bf88,0x3fa03fc8,0xbfb53fb5,0x3ffabee4,0x3f0e3ff6,0xbff23f29,0xbf44bfed,
0,0,0,0,0,0,0,0,0,0,0,0,0,0,0,0,
0xbfc83fa0,0xbffabee4,0xbf5ebfe7,0x3f5ebfe7,0xbee4bffa,0x3fabbfbf,0x3ffe3e65,0x3f783fe0,0x3ffa3ee4,0x3f293ff2,0xbfa03fc8,0xbffebe65,0xbfa03fc8,0xbffcbeac,0xbf29bff2,0x3fabbfbf,
0xbf5ebfe7,0x3fa0bfc8,0x3ffa3ee4,0x3ee43ffa,0x40000000,0x3f443fed,0xbfb53fb5,0xbfedbf44,0xbf5e3fe7,0xbffebe65,0xbee4bffa,0x3fd9bf88,0xbfa0bfc8,0x3f94bfd1,0x3ff23f29,0xbde64000,
0x3ffabee4,0x3f5e3fe7,0xbfc83fa0,0xbfc8bfa0,0xbee43ffa,0xc000bde6,0xbe65bffe,0x3ff6bf0e,0xbfc8bfa0,0x3f88bfd9,0x3fe73f5e,0xbf293ff2,0x3fe7bf5e,0x3f783fe0,0xbfd93f88,0xbf94bfd1,
0,0,0,0,0,0,0,0,0,0,0,0,0,0,0,0,
0x3ffabee4,0x3fc83fa0,0x80004000,0xbfc83fa0,0xbf5e3fe7,0xbffc3eac,0xbfb5bfb5,0x3eacbffc,0xbf5ebfe7,0x3f88bfd9,0x40000000,0x3f883fd9,0x3ffa3ee4,0x3f0e3ff6,0xbfb53fb5,0xbff6bf0e,
0xbfc83fa0,0xbfe7bf5e,0x8000c000,0x3fe7bf5e,0x8000c000,0x3fedbf44,0x3fb53fb5,0xbf443fed,0x3fc83fa0,0xbf293ff2,0xc0000000,0xbf29bff2,0xbffa3ee4,0xbf78bfe0,0x3fb5bfb5,0x3fe03f78,
0x3f5ebfe7,0x3ffa3ee4,0x80004000,0xbffa3ee4,0x3f5e3fe7,0xbfd13f94,0xbfb5bfb5,0x3f94bfd1,0xbffabee4,0x3e65bffe,0x40000000,0x3e653ffe,0x3fc8bfa0,0x3fab3fbf,0xbfb53fb5,0xbfbfbfab,
0,0,0,0,0,0,0,0,0,0,0,0,0,0,0,0,
0xbffabee4,0xbf5ebfe7,0x3f5ebfe7,0x3ffabee4,0x3fe7bf5e,0x3fe03f78,0x3e653ffe,0xbfbf3fab,0xbf5e3fe7,0xbffe3e65,0xbfa0bfc8,0x3f29bff2,0xbee4bffa,0x3fbfbfab,0x3ff23f29,0x3eac3ffc,
0x3fc83fa0,0xbee43ffa,0xbffa3ee4,0xbfa0bfc8,0xc0000000,0xbf44bfed,0x3fb5bfb5,0x3fed3f44,0x3fc8bfa0,0x3fd93f88,0xbee43ffa,0xbffe3e65,0xbee43ffa,0xc0003de6,0xbf29bff2,0x3fd1bf94,
0xbf5ebfe7,0x3fc8bfa0,0x3fc83fa0,0xbf5e3fe7,0x3fe73f5e,0xbf0e3ff6,0xbffebe65,0xbde6c000,0xbffa3ee4,0xbf29bff2,0x3fe7bf5e,0x3f883fd9,0x3fa0bfc8,0x3fd13f94,0xbf883fd9,0xbfe0bf78,
0,0,0,0,0,0,0,0,0,0,0,0,0,0,0,0,
0x3fc83fa0,0x80004000,0xbfc83fa0,0xbffabee4,0xbffabee4,0xbf44bfed,0x3f88bfd9,0x4000bde6,0x3ffabee4,0x3fb53fb5,0xbee43ffa,0xbff23f29,0xbfc83fa0,0xbfedbf44,0xbe65bffe,0x3fd1bf94,
0x3f5ebfe7,0x40000000,0x3f5e3fe7,0xbfa03fc8,0x80004000,0xbfed3f44,0xbfb5bfb5,0x3f44bfed,0xbf5ebfe7,0x3fb5bfb5,0x3fe73f5e,0xbe653ffe,0x3fc83fa0,0xbf443fed,0xbffebe65,0xbeacbffc,
0xbffabee4,0x8000c000,0x3ffabee4,0x3f5e3fe7,0x3ffabee4,0x3f443fed,0xbfd93f88,0xbfabbfbf,0xbfc83fa0,0xbfb5bfb5,0x3fa0bfc8,0x3fd93f88,0x3f5ebfe7,0x3fed3f44,0xbf293ff2,0xbff6bf0e,
0,0,0,0,0,0,0,0,0,0,0,0,0,0,0,0,
0xbf5ebfe7,0x3f5ebfe7,0x3ffabee4,0x3fc83fa0,0x3fa03fc8,0xbf0e3ff6,0xbff23f29,0xbfd1bf94,0xbfc8bfa0,0x3e65bffe,0x3fe7bf5e,0x3fd93f88,0x3fe73f5e,0x3de64000,0xbfd93f88,0xbfe0bf78,
0xbffabee4,0xbee4bffa,0x3fc8bfa0,0x3fe73f5e,0x40000000,0x3f443fed,0xbfb53fb5,0xbfedbf44,0xbffa3ee4,0xbf88bfd9,0x3fa0bfc8,0x3ff23f29,0x3fe7bf5e,0x3fab3fbf,0xbf883fd9,0xbff6bf0e,
0xbfc83fa0,0xbfc8bfa0,0x3f5ebfe7,0x3ffa3ee4,0x3fa0bfc8,0x3fe03f78,0xbf293ff2,0xbffcbeac,0xbf5e3fe7,0xbff2bf29,0x3ee4bffa,0x3ffe3e65,0x3ee4bffa,0x3ffc3eac,0xbe653ffe,0xc000bde6,
0,0,0,0,0,0,0,0,0,0,0,0,0,0,0,0,
0x3f80,0,0,0,0xbf80,0,0,0,0x3f80,0,0,0,0xbf80,0,0,0,
0x3f80,0,0,0,0xbf80,0,0,0,0x3f80,0,0,0,0xbf80,0,0,0,
0x3f80,0,0,0,0xbf80,0,0,0,0x3f80,0,0,0,0xbf80,0,0,0,
0,0,0,0,0,0,0,0,0,0,0,0,0,0,0,0,
0,0,0,0,0,0,0,0,0,0,0,0,0,0,0,0,
0,0,0,0,0,0,0,0,0,0,0,0,0,0,0,0,
0,0,0,0,0,0,0,0,0,0,0,0,0,0,0,0,
0,0,0,0,0,0,0,0,0,0,0,0,0,0,0,0,
0x3f80,0xc0000000,0x8000c000,0x40000000,0x3f80,0xc000bde6,0x3e65bffe,0x3ffc3eac,0x3f80,0xbffebe65,0x3ee4bffa,0x3ff23f29,0x3f80,0xbffcbeac,0x3f29bff2,0x3fe03f78,
0x3f80,0xbffabee4,0x3f5ebfe7,0x3fc83fa0,0x3f80,0xbff6bf0e,0x3f88bfd9,0x3fab3fbf,0x3f80,0xbff2bf29,0x3fa0bfc8,0x3f883fd9,0x3f80,0xbfedbf44,0x3fb5bfb5,0x3f443fed,
0x3f80,0xbfe7bf5e,0x3fc8bfa0,0x3ee43ffa,0x3f80,0xbfe0bf78,0x3fd9bf88,0x3de64000,0x3f80,0xbfd9bf88,0x3fe7bf5e,0xbe653ffe,0x3f80,0xbfd1bf94,0x3ff2bf29,0xbf0e3ff6,
0x3f80,0xbfc8bfa0,0x3ffabee4,0xbf5e3fe7,0x3f80,0xbfbfbfab,0x3ffebe65,0xbf943fd1,0x3f80,0xbfb5bfb5,0x40000000,0xbfb53fb5,0x3f80,0xbfabbfbf,0x3ffe3e65,0xbfd13f94,
0x80004000,0xc0000000,0x8000c000,0x40000000,0xbee43ffa,0xbff6bf0e,0x3f29bff2,0x3fed3f44,0xbf5e3fe7,0xbfd9bf88,0x3fa0bfc8,0x3fb53fb5,0xbfa03fc8,0xbfabbfbf,0x3fd9bf88,0x3f443fed,
0xbfc83fa0,0xbf5ebfe7,0x3ffabee4,0x80004000,0xbfe73f5e,0xbeacbffc,0x3ffe3e65,0xbf443fed,0xbffa3ee4,0x3e65bffe,0x3fe73f5e,0xbfb53fb5,0xc0000000,0x3f44bfed,0x3fb53fb5,0xbfed3f44,
0xbffabee4,0x3fa0bfc8,0x3f5e3fe7,0xc0000000,0xbfe7bf5e,0x3fd1bf94,0x3e653ffe,0xbfedbf44,0xbfc8bfa0,0x3ff2bf29,0xbee43ffa,0xbfb5bfb5,0xbfa0bfc8,0x4000bde6,0xbf883fd9,0xbf44bfed,
0xbf5ebfe7,0x3ffa3ee4,0xbfc83fa0,0x8000c000,0xbee4bffa,0x3fe03f78,0xbff23f29,0x3f44bfed,0x8000c000,0x3fb53fb5,0xc0000000,0x3fb5bfb5,0x3ee4bffa,0x3f783fe0,0xbff2bf29,0x3fedbf44,
0x80004000,0xc0000000,0x8000c000,0x40000000,0xbf5e3fe7,0xbfe0bf78,0x3f88bfd9,0x3fd13f94,0xbfc83fa0,0xbf88bfd9,0x3fe7bf5e,0x3f293ff2,0xbffa3ee4,0xbde6c000,0x3ffe3e65,0xbf0e3ff6,
0xbffabee4,0x3f5ebfe7,0x3fc83fa0,0xbfc83fa0,0xbfc8bfa0,0x3fd1bf94,0x3f293ff2,0xc0003de6,0xbf5ebfe7,0x3ffebe65,0xbee43ffa,0xbfd9bf88,0x8000c000,0x3fed3f44,0xbfb53fb5,0xbf44bfed,
0x3f5ebfe7,0x3fa03fc8,0xbffa3ee4,0x3ee4bffa,0x3fc8bfa0,0x3eac3ffc,0xbff2bf29,0x3fbfbfab,0x3ffabee4,0xbf293ff2,0xbfa0bfc8,0x3ffebe65,0x3ffa3ee4,0xbfbf3fab,0xbe65bffe,0x3fe03f78,
0x3fc83fa0,0xbffa3ee4,0x3f5ebfe7,0x3f5e3fe7,0x3f5e3fe7,0xbff6bf0e,0x3fd9bf88,0xbeac3ffc,0x80004000,0xbfb5bfb5,0x40000000,0xbfb53fb5,0xbf5e3fe7,0xbf0ebff6,0x3fd93f88,0xbffc3eac,
0x80004000,0xc0000000,0x8000c000,0x40000000,0xbfa03fc8,0xbfbfbfab,0x3fb5bfb5,0x3fab3fbf,0xbffa3ee4,0xbe65bffe,0x40000000,0xbe653ffe,0xbfe7bf5e,0x3f94bfd1,0x3fb53fb5,0xbfd13f94,
0xbf5ebfe7,0x3ffabee4,0x80004000,0xbffabee4,0x3ee4bffa,0x3fe03f78,0xbfb53fb5,0xbf78bfe0,0x3fc8bfa0,0x3f293ff2,0xc0000000,0x3f29bff2,0x40000000,0xbf443fed,0xbfb5bfb5,0x3fedbf44,
0x3fc83fa0,0xbfe73f5e,0x8000c000,0x3fe73f5e,0x3ee43ffa,0xbff6bf0e,0x3fb5bfb5,0x3f0e3ff6,0xbf5e3fe7,0xbf88bfd9,0x40000000,0xbf883fd9,0xbfe73f5e,0x3eacbffc,0x3fb53fb5,0xbffc3eac,
0xbffabee4,0x3fc8bfa0,0x80004000,0xbfc8bfa0,0xbfa0bfc8,0x40003de6,0xbfb53fb5,0xbde6c000,0x8000c000,0x3fb53fb5,0xc0000000,0x3fb5bfb5,0x3fa0bfc8,0x3de64000,0xbfb5bfb5,0x40003de6,
0x80004000,0xc0000000,0x8000c000,0x40000000,0xbfc83fa0,0xbf94bfd1,0x3fd9bf88,0x3f783fe0,0xbffabee4,0x3f29bff2,0x3fe73f5e,0xbf883fd9,0xbf5ebfe7,0x3ff6bf0e,0x3e653ffe,0xc000bde6,
0x3f5ebfe7,0x3fc83fa0,0xbfc83fa0,0xbf5ebfe7,0x3ffabee4,0xbde64000,0xbff2bf29,0x3f94bfd1,0x3fc83fa0,0xbfd93f88,0xbee4bffa,0x3ffe3e65,0x80004000,0xbfedbf44,0x3fb5bfb5,0x3f443fed,
0xbfc83fa0,0xbee4bffa,0x3ffa3ee4,0xbfa03fc8,0xbffabee4,0x3fabbfbf,0x3f293ff2,0xbffcbeac,0xbf5ebfe7,0x3ffe3e65,0xbfa03fc8,0xbf29bff2,0x3f5ebfe7,0x3f783fe0,0xbffebe65,0x3fabbfbf,
0x3ffabee4,0xbf5e3fe7,0xbf5ebfe7,0x3ffa3ee4,0x3fc83fa0,0xbffc3eac,0x3f88bfd9,0x3f0e3ff6,0x80004000,0xbfb5bfb5,0x40000000,0xbfb53fb5,0xbfc83fa0,0x3eacbffc,0x3f883fd9,0xbff6bf0e,
0x80004000,0xc0000000,0x8000c000,0x40000000,0xbfe73f5e,0xbf44bfed,0x3ff2bf29,0x3f0e3ff6,0xbfc8bfa0,0x3fb5bfb5,0x3fa03fc8,0xbfd93f88,0x3ee4bffa,0x3fed3f44,0xbf883fd9,0xbfbfbfab,
0x3ffabee4,0x80004000,0xbffabee4,0x3f5ebfe7,0x3fa03fc8,0xbfed3f44,0xbe65bffe,0x3ffc3eac,0xbf5e3fe7,0xbfb5bfb5,0x3fe7bf5e,0x3e653ffe,0xc0000000,0x3f44bfed,0x3fb53fb5,0xbfed3f44,
0xbf5ebfe7,0x40000000,0xbf5e3fe7,0xbfa0bfc8,0x3fa0bfc8,0x3f443fed,0xbffebe65,0x3f94bfd1,0x3ffa3ee4,0xbfb53fb5,0xbee4bffa,0x3ff23f29,0x3ee43ffa,0xbfedbf44,0x3fd9bf88,0xbde64000,
0xbfc83fa0,0x8000c000,0x3fc83fa0,0xbffa3ee4,0xbfe7bf5e,0x3fedbf44,0xbf293ff2,0xbf78bfe0,0x8000c000,0x3fb53fb5,0xc0000000,0x3fb5bfb5,0x3fe7bf5e,0xbf443fed,0xbf29bff2,0x3fe03f78,
0x80004000,0xc0000000,0x8000c000,0x40000000,0xbffa3ee4,0xbeacbffc,0x3ffebe65,0x3de64000,0xbf5ebfe7,0x3ff2bf29,0x3ee43ffa,0xbffe3e65,0x3fc8bfa0,0x3f783fe0,0xbff23f29,0xbeacbffc,
0x3fc83fa0,0xbfc83fa0,0xbf5ebfe7,0x3ffabee4,0xbf5e3fe7,0xbfbfbfab,0x3fd9bf88,0x3f0e3ff6,0xbffabee4,0x3f88bfd9,0x3fa03fc8,0xbff23f29,0x8000c000,0x3fed3f44,0xbfb53fb5,0xbf44bfed,
0x3ffabee4,0xbee43ffa,0xbfc8bfa0,0x3fe7bf5e,0x3f5e3fe7,0xc000bde6,0x3f88bfd9,0x3f783fe0,0xbfc83fa0,0xbe65bffe,0x3fe73f5e,0xbfd93f88,0xbfc8bfa0,0x3ff6bf0e,0xbf293ff2,0xbf94bfd1,
0x3f5ebfe7,0x3f5e3fe7,0xbffabee4,0x3fc8bfa0,0x3ffa3ee4,0xbfd13f94,0x3e65bffe,0x3fab3fbf,0x80004000,0xbfb5bfb5,0x40000000,0xbfb53fb5,0xbffa3ee4,0x3f94bfd1,0x3e653ffe,0xbfbfbfab,
0x80004000,0xc0000000,0x8000c000,0x40000000,0xc0000000,0x3de6c000,0x3ffe3e65,0xbeac3ffc,0x8000c000,0x3ffe3e65,0xbee43ffa,0xbff2bf29,0x40000000,0xbeac3ffc,0xbff2bf29,0x3f78bfe0,
0x80004000,0xbffabee4,0x3f5ebfe7,0x3fc83fa0,0xc0000000,0x3f0ebff6,0x3fd93f88,0xbfbf3fab,0x8000c000,0x3ff23f29,0xbfa03fc8,0xbf88bfd9,0x40000000,0xbf443fed,0xbfb5bfb5,0x3fedbf44,
0x80004000,0xbfe7bf5e,0x3fc8bfa0,0x3ee43ffa,0xc0000000,0x3f78bfe0,0x3f883fd9,0xc0003de6,0x8000c000,0x3fd93f88,0xbfe73f5e,0x3e65bffe,0x40000000,0xbf943fd1,0xbf29bff2,0x3ff63f0e,
0x80004000,0xbfc8bfa0,0x3ffabee4,0xbf5e3fe7,0xc0000000,0x3fabbfbf,0x3e653ffe,0xbfd1bf94,0x8000c000,0x3fb53fb5,0xc0000000,0x3fb5bfb5,0x40000000,0xbfbf3fab,0x3e65bffe,0x3f943fd1,
0x80004000,0xc0000000,0x8000c000,0x40000000,0xbffabee4,0x3f0ebff6,0x3ff23f29,0xbf443fed,0x3f5ebfe7,0x3fd93f88,0xbfa03fc8,0xbfb5bfb5,0x3fc83fa0,0xbfbf3fab,0xbf88bfd9,0x3fedbf44,
0xbfc83fa0,0xbf5ebfe7,0x3ffabee4,0x80004000,0xbf5ebfe7,0x3ffcbeac,0xbe653ffe,0xbfedbf44,0x3ffabee4,0xbe653ffe,0xbfe7bf5e,0x3fb5bfb5,0x80004000,0xbfedbf44,0x3fb5bfb5,0x3f443fed,
0xbffabee4,0x3fa0bfc8,0x3f5e3fe7,0xc0000000,0x3f5ebfe7,0x3f943fd1,0xbffe3e65,0x3f44bfed,0x3fc83fa0,0xbff23f29,0x3ee4bffa,0x3fb53fb5,0xbfc83fa0,0xbde6c000,0x3fd93f88,0xbfed3f44,
0xbf5ebfe7,0x3ffa3ee4,0xbfc83fa0,0x8000c000,0x3ffabee4,0xbf783fe0,0xbf29bff2,0x3fed3f44,0x80004000,0xbfb5bfb5,0x40000000,0xbfb53fb5,0xbffabee4,0x3fe0bf78,0xbf293ff2,0xbf44bfed,
0x80004000,0xc0000000,0x8000c000,0x40000000,0xbfe7bf5e,0x3f78bfe0,0x3fd93f88,0xbf943fd1,0x3fc8bfa0,0x3f883fd9,0xbfe73f5e,0xbf29bff2,0x3ee43ffa,0xc0003de6,0x3e65bffe,0x3ff63f0e,
0xbffabee4,0x3f5ebfe7,0x3fc83fa0,0xbfc83fa0,0x3fa0bfc8,0x3f943fd1,0xbff23f29,0xbde6c000,0x3f5e3fe7,0xbffe3e65,0x3ee4bffa,0x3fd93f88,0xc0000000,0x3f44bfed,0x3fb53fb5,0xbfed3f44,
0x3f5ebfe7,0x3fa03fc8,0xbffa3ee4,0x3ee4bffa,0x3fa03fc8,0xbffc3eac,0x3f29bff2,0x3fab3fbf,0xbffa3ee4,0x3f29bff2,0x3fa03fc8,0xbffe3e65,0x3ee4bffa,0x3fab3fbf,0xbffe3e65,0x3f78bfe0,
0x3fc83fa0,0xbffa3ee4,0x3f5ebfe7,0x3f5e3fe7,0xbfe73f5e,0x3f0ebff6,0x3f883fd9,0xbffcbeac,0x8000c000,0x3fb53fb5,0xc0000000,0x3fb5bfb5,0x3fe73f5e,0xbff63f0e,0x3f88bfd9,0x3eac3ffc,
0x80004000,0xc0000000,0x8000c000,0x40000000,0xbfc8bfa0,0x3fabbfbf,0x3fb53fb5,0xbfbf3fab,0x3ffabee4,0x3e653ffe,0xc0000000,0x3e65bffe,0xbf5e3fe7,0xbfd1bf94,0x3fb5bfb5,0x3f943fd1,
0xbf5ebfe7,0x3ffabee4,0x80004000,0xbffabee4,0x3ffa3ee4,0xbf783fe0,0xbfb5bfb5,0x3fe0bf78,0xbfc83fa0,0xbf29bff2,0x40000000,0xbf293ff2,0x8000c000,0x3fed3f44,0xbfb53fb5,0xbf44bfed,
0x3fc83fa0,0xbfe73f5e,0x8000c000,0x3fe73f5e,0xbffa3ee4,0x3f0ebff6,0x3fb53fb5,0xbff63f0e,0x3f5ebfe7,0x3f883fd9,0xc0000000,0x3f88bfd9,0x3f5e3fe7,0xbffcbeac,0x3fb5bfb5,0x3eac3ffc,
0xbffabee4,0x3fc8bfa0,0x80004000,0xbfc8bfa0,0x3fc8bfa0,0xbde64000,0xbfb5bfb5,0x4000bde6,0x80004000,0xbfb5bfb5,0x40000000,0xbfb53fb5,0xbfc8bfa0,0x4000bde6,0xbfb53fb5,0x3de6c000,
0x80004000,0xc0000000,0x8000c000,0x40000000,0xbfa0bfc8,0x3fd1bf94,0x3f883fd9,0xbfe03f78,0x3ffa3ee4,0xbf293ff2,0xbfe7bf5e,0x3f88bfd9,0xbfe73f5e,0xbf0ebff6,0x3ffebe65,0xbde64000,
0x3f5ebfe7,0x3fc83fa0,0xbfc83fa0,0xbf5ebfe7,0x3ee43ffa,0xc000bde6,0x3f29bff2,0x3fd13f94,0xbfc8bfa0,0x3fd9bf88,0x3ee43ffa,0xbffebe65,0x40000000,0xbf443fed,0xbfb5bfb5,0x3fedbf44,
0xbfc83fa0,0xbee4bffa,0x3ffa3ee4,0xbfa03fc8,0x3ee4bffa,0x3fbf3fab,0xbff23f29,0x3eacbffc,0x3f5e3fe7,0xbffebe65,0x3fa0bfc8,0x3f293ff2,0xbfe7bf5e,0x3fe0bf78,0xbe653ffe,0xbfbfbfab,
0x3ffabee4,0xbf5e3fe7,0xbf5ebfe7,0x3ffa3ee4,0xbfa03fc8,0xbeacbffc,0x3fd93f88,0xbff63f0e,0x8000c000,0x3fb53fb5,0xc0000000,0x3fb5bfb5,0x3fa03fc8,0xbffcbeac,0x3fd9bf88,0xbf0e3ff6,
0x80004000,0xc0000000,0x8000c000,0x40000000,0xbf5ebfe7,0x3fedbf44,0x3f293ff2,0xbff63f0e,0x3fc83fa0,0xbfb53fb5,0xbfa0bfc8,0x3fd9bf88,0xbffabee4,0x3f44bfed,0x3fd93f88,0xbfab3fbf,
0x3ffabee4,0x80004000,0xbffabee4,0x3f5ebfe7,0xbfc83fa0,0xbf44bfed,0x3ffebe65,0xbeac3ffc,0x3f5ebfe7,0x3fb53fb5,0xbfe73f5e,0xbe65bffe,0x80004000,0xbfedbf44,0x3fb5bfb5,0x3f443fed,
0xbf5ebfe7,0x40000000,0xbf5e3fe7,0xbfa0bfc8,0x3fc83fa0,0xbfed3f44,0x3e65bffe,0x3fd13f94,0xbffabee4,0x3fb5bfb5,0x3ee43ffa,0xbff2bf29,0x3ffabee4,0xbf443fed,0xbf88bfd9,0x40003de6,
0xbfc83fa0,0x8000c000,0x3fc83fa0,0xbffa3ee4,0x3f5ebfe7,0x3f443fed,0xbff2bf29,0x3fe0bf78,0x80004000,0xbfb5bfb5,0x40000000,0xbfb53fb5,0xbf5ebfe7,0x3fed3f44,0xbff23f29,0x3f78bfe0,
0x80004000,0xc0000000,0x8000c000,0x40000000,0xbee4bffa,0x3ffcbeac,0x3e653ffe,0xc0003de6,0x3f5e3fe7,0xbff23f29,0xbee4bffa,0x3ffebe65,0xbfa0bfc8,0x3fe0bf78,0x3f293ff2,0xbffc3eac,
0x3fc83fa0,0xbfc83fa0,0xbf5ebfe7,0x3ffabee4,0xbfe7bf5e,0x3fabbfbf,0x3f883fd9,0xbff63f0e,0x3ffa3ee4,0xbf883fd9,0xbfa0bfc8,0x3ff2bf29,0xc0000000,0x3f44bfed,0x3fb53fb5,0xbfed3f44,
0x3ffabee4,0xbee43ffa,0xbfc8bfa0,0x3fe7bf5e,0xbfe73f5e,0x3de6c000,0x3fd93f88,0xbfe03f78,0x3fc8bfa0,0x3e653ffe,0xbfe7bf5e,0x3fd9bf88,0xbfa03fc8,0xbf0ebff6,0x3ff23f29,0xbfd13f94,
0x3f5ebfe7,0x3f5e3fe7,0xbffabee4,0x3fc8bfa0,0xbee43ffa,0xbf94bfd1,0x3ffe3e65,0xbfbf3fab,0x8000c000,0x3fb53fb5,0xc0000000,0x3fb5bfb5,0x3ee43ffa,0xbfd1bf94,0x3ffebe65,0xbfab3fbf,
0x3f80,0,0,0,0xbf80,0,0,0,0x3f80,0,0,0,0xbf80,0,0,0,
0x3f80,0,0,0,0xbf80,0,0,0,0x3f80,0,0,0,0xbf80,0,0,0,
0x3f80,0,0,0,0xbf80,0,0,0,0x3f80,0,0,0,0xbf80,0,0,0,
0x3f80,0,0,0,0xbf80,0,0,0,0x3f80,0,0,0,0xbf80,0,0,0,
0,0,0,0,0,0,0,0,0,0,0,0,0,0,0,0,
0,0,0,0,0,0,0,0,0,0,0,0,0,0,0,0,
0,0,0,0,0,0,0,0,0,0,0,0,0,0,0,0,
0,0,0,0,0,0,0,0,0,0,0,0,0,0,0,0,
0x3f80,0xbfa0bfc8,0x3ffa3ee4,0xbfe73f5e,0x3f80,0xbf94bfd1,0x3ff23f29,0xbff63f0e,0x3f80,0xbf88bfd9,0x3fe73f5e,0xbffe3e65,0x3f80,0xbf78bfe0,0x3fd93f88,0xc000bde6,
0x3f80,0xbf5ebfe7,0x3fc83fa0,0xbffabee4,0x3f80,0xbf44bfed,0x3fb53fb5,0xbfedbf44,0x3f80,0xbf29bff2,0x3fa03fc8,0xbfd9bf88,0x3f80,0xbf0ebff6,0x3f883fd9,0xbfbfbfab,
0x3f80,0xbee4bffa,0x3f5e3fe7,0xbfa0bfc8,0x3f80,0xbeacbffc,0x3f293ff2,0xbf78bfe0,0x3f80,0xbe65bffe,0x3ee43ffa,0xbf29bff2,0x3f80,0xbde6c000,0x3e653ffe,0xbeacbffc,
0,0,0,0,0,0,0,0,0,0,0,0,0,0,0,0,
0x3f5ebfe7,0x3ee43ffa,0xbfc8bfa0,0x40000000,0x3fa0bfc8,0xbde64000,0xbf88bfd9,0x3fed3f44,0x3fc8bfa0,0xbf293ff2,0xbee4bffa,0x3fb53fb5,0x3fe7bf5e,0xbf943fd1,0x3e65bffe,0x3f443fed,
0x3ffabee4,0xbfc83fa0,0x3f5ebfe7,0x80004000,0x40000000,0xbfed3f44,0x3fb5bfb5,0xbf443fed,0x3ffa3ee4,0xbffe3e65,0x3fe7bf5e,0xbfb53fb5,0x3fe73f5e,0xbffcbeac,0x3ffebe65,0xbfed3f44,
0x3fc83fa0,0xbfe7bf5e,0x3ffa3ee4,0xc0000000,0x3fa03fc8,0xbfbfbfab,0x3fd93f88,0xbfedbf44,0x3f5e3fe7,0xbf88bfd9,0x3fa03fc8,0xbfb5bfb5,0x3ee43ffa,0xbf0ebff6,0x3f293ff2,0xbf44bfed,
0,0,0,0,0,0,0,0,0,0,0,0,0,0,0,0,
0xbfc83fa0,0x3ee4bffa,0x3f5e3fe7,0xbfe7bf5e,0xbffa3ee4,0x3fabbfbf,0xbe653ffe,0xbf78bfe0,0xbffabee4,0x3ff2bf29,0xbfa03fc8,0x3e65bffe,0xbfc8bfa0,0x3ffc3eac,0xbff23f29,0x3fabbfbf,
0xbf5ebfe7,0x3fc83fa0,0xbffabee4,0x3ffabee4,0x8000c000,0x3f443fed,0xbfb5bfb5,0x3fed3f44,0x3f5ebfe7,0xbe653ffe,0xbee4bffa,0x3f883fd9,0x3fc8bfa0,0xbf943fd1,0x3f29bff2,0xbde64000,
0x3ffabee4,0xbfe73f5e,0x3fc8bfa0,0xbfa03fc8,0x3ffa3ee4,0xc000bde6,0x3ffebe65,0xbff63f0e,0x3fc83fa0,0xbfd9bf88,0x3fe73f5e,0xbff2bf29,0x3f5e3fe7,0xbf78bfe0,0x3f883fd9,0xbf94bfd1,
0,0,0,0,0,0,0,0,0,0,0,0,0,0,0,0,
0x3ffabee4,0xbfa03fc8,0x8000c000,0x3fa03fc8,0x3fe73f5e,0xbffc3eac,0x3fb5bfb5,0xbeac3ffc,0x3f5e3fe7,0xbfd9bf88,0x40000000,0xbfd93f88,0xbee43ffa,0xbf0ebff6,0x3fb53fb5,0xbff6bf0e,
0xbfc83fa0,0x3f5ebfe7,0x80004000,0xbf5ebfe7,0xc0000000,0x3fedbf44,0xbfb53fb5,0x3f44bfed,0xbfc8bfa0,0x3ff23f29,0xc0000000,0x3ff2bf29,0xbee4bffa,0x3f783fe0,0xbfb5bfb5,0x3fe03f78,
0x3f5ebfe7,0xbee43ffa,0x8000c000,0x3ee43ffa,0x3fe7bf5e,0xbfd13f94,0x3fb5bfb5,0xbf943fd1,0x3ffa3ee4,0xbffebe65,0x40000000,0xbffe3e65,0x3fa03fc8,0xbfabbfbf,0x3fb53fb5,0xbfbfbfab,
0,0,0,0,0,0,0,0,0,0,0,0,0,0,0,0,
0xbffabee4,0x3fe7bf5e,0xbf5e3fe7,0xbee4bffa,0xbf5ebfe7,0x3fe03f78,0xbffe3e65,0x3fbfbfab,0x3f5ebfe7,0x3e653ffe,0xbfa0bfc8,0x3ff23f29,0x3ffabee4,0xbfbf3fab,0x3f29bff2,0x3eac3ffc,
0x3fc83fa0,0xbffabee4,0x3ffabee4,0xbfc83fa0,0x80004000,0xbf44bfed,0x3fb53fb5,0xbfedbf44,0xbfc83fa0,0x3f88bfd9,0xbee43ffa,0xbe65bffe,0xbffabee4,0x4000bde6,0xbff23f29,0x3fd1bf94,
0xbf5ebfe7,0x3fa03fc8,0xbfc8bfa0,0x3fe73f5e,0x3f5ebfe7,0xbf0e3ff6,0x3e65bffe,0x3de64000,0x3ffabee4,0xbff23f29,0x3fe7bf5e,0xbfd93f88,0x3fc83fa0,0xbfd1bf94,0x3fd93f88,0xbfe0bf78,
0,0,0,0,0,0,0,0,0,0,0,0,0,0,0,0,
0x3fc83fa0,0xc0000000,0x3fc8bfa0,0xbee43ffa,0xbee43ffa,0xbf44bfed,0x3fd93f88,0xc0003de6,0xbffa3ee4,0x3fb5bfb5,0xbee43ffa,0xbf29bff2,0xbfa0bfc8,0x3fed3f44,0xbffe3e65,0x3fd1bf94,
0x3f5ebfe7,0x80004000,0xbf5ebfe7,0x3fc83fa0,0x40000000,0xbfed3f44,0x3fb5bfb5,0xbf443fed,0x3f5e3fe7,0xbfb5bfb5,0x3fe73f5e,0xbffebe65,0xbfa03fc8,0x3f44bfed,0xbe653ffe,0xbeacbffc,
0xbffabee4,0x40000000,0xbffa3ee4,0x3fe7bf5e,0xbee4bffa,0x3f443fed,0xbf88bfd9,0x3fab3fbf,0x3fc8bfa0,0xbfb53fb5,0x3fa0bfc8,0xbf883fd9,0x3fe73f5e,0xbfedbf44,0x3ff23f29,0xbff6bf0e,
0,0,0,0,0,0,0,0,0,0,0,0,0,0,0,0,
0xbf5ebfe7,0x3fe73f5e,0xbffa3ee4,0x3fa0bfc8,0x3fc8bfa0,0xbf0e3ff6,0xbf29bff2,0x3fd13f94,0x3fc83fa0,0xbffebe65,0x3fe7bf5e,0xbf883fd9,0xbf5e3fe7,0xbde6c000,0x3f883fd9,0xbfe0bf78,
0xbffabee4,0x3ffabee4,0xbfc83fa0,0x3f5ebfe7,0x8000c000,0x3f443fed,0xbfb5bfb5,0x3fed3f44,0x3ffabee4,0xbfd93f88,0x3fa0bfc8,0xbf293ff2,0x3f5e3fe7,0xbfabbfbf,0x3fd93f88,0xbff6bf0e,
0xbfc83fa0,0x3fa0bfc8,0xbf5e3fe7,0x3ee4bffa,0xbfc8bfa0,0x3fe03f78,0xbff2bf29,0x3ffc3eac,0x3f5ebfe7,0xbf293ff2,0x3ee4bffa,0xbe653ffe,0x3ffa3ee4,0xbffcbeac,0x3ffe3e65,0xc000bde6,
0,0,0,0,0,0,0,0,0,0,0,0,0,0,0,0,
0x80004000,0xbfa0bfc8,0x3ffa3ee4,0xbfe73f5e,0xc0000000,0x3fd1bf94,0xbf293ff2,0xbf0ebff6,0x8000c000,0x3f883fd9,0xbfe7bf5e,0x3ffebe65,0x40000000,0xbfe03f78,0x3f88bfd9,0xbde64000,
0x80004000,0xbf5ebfe7,0x3fc83fa0,0xbffabee4,0xc0000000,0x3fedbf44,0xbfb53fb5,0x3f44bfed,0x8000c000,0x3f293ff2,0xbfa0bfc8,0x3fd93f88,0x40000000,0xbff63f0e,0x3fd9bf88,0xbfab3fbf,
0x80004000,0xbee4bffa,0x3f5e3fe7,0xbfa0bfc8,0xc0000000,0x3ffcbeac,0xbff23f29,0x3fe0bf78,0x8000c000,0x3e653ffe,0xbee4bffa,0x3f293ff2,0x40000000,0xc0003de6,0x3ffebe65,0xbffc3eac,
0,0,0,0,0,0,0,0,0,0,0,0,0,0,0,0,
0x3f5ebfe7,0x3ee43ffa,0xbfc8bfa0,0x40000000,0x3fc83fa0,0xc000bde6,0x3fd9bf88,0xbf443fed,0xbfc83fa0,0x3f29bff2,0x3ee43ffa,0xbfb5bfb5,0xbf5ebfe7,0x3fd13f94,0xbffebe65,0x3fedbf44,
0x3ffabee4,0xbfc83fa0,0x3f5ebfe7,0x80004000,0x80004000,0xbf44bfed,0x3fb53fb5,0xbfedbf44,0xbffabee4,0x3ffebe65,0xbfe73f5e,0x3fb5bfb5,0x3f5ebfe7,0xbeac3ffc,0xbe65bffe,0x3f443fed,
0x3fc83fa0,0xbfe7bf5e,0x3ffa3ee4,0xc0000000,0xbfc83fa0,0x3fabbfbf,0xbf883fd9,0x3f44bfed,0xbf5ebfe7,0x3f883fd9,0xbfa0bfc8,0x3fb53fb5,0x3ffabee4,0xbff63f0e,0x3ff2bf29,0xbfed3f44,
0,0,0,0,0,0,0,0,0,0,0,0,0,0,0,0,
0xbfc83fa0,0x3ee4bffa,0x3f5e3fe7,0xbfe7bf5e,0xbee4bffa,0x3fbf3fab,0xbffebe65,0x3fe0bf78,0x3ffa3ee4,0xbff23f29,0x3fa0bfc8,0xbe653ffe,0xbfa03fc8,0x3eacbffc,0x3f293ff2,0xbfbfbfab,
0xbf5ebfe7,0x3fc83fa0,0xbffabee4,0x3ffabee4,0x40000000,0xbfed3f44,0x3fb5bfb5,0xbf443fed,0xbf5e3fe7,0x3e65bffe,0x3ee43ffa,0xbf88bfd9,0xbfa0bfc8,0x3fd13f94,0xbff2bf29,0x40003de6,
0x3ffabee4,0xbfe73f5e,0x3fc8bfa0,0xbfa03fc8,0xbee43ffa,0x3de6c000,0x3e653ffe,0xbf0ebff6,0xbfc8bfa0,0x3fd93f88,0xbfe7bf5e,0x3ff23f29,0x3fe7bf5e,0xbfe03f78,0x3fd9bf88,0xbfd13f94,
0,0,0,0,0,0,0,0,0,0,0,0,0,0,0,0,
0x3ffabee4,0xbfa03fc8,0x8000c000,0x3fa03fc8,0xbf5e3fe7,0xbeacbffc,0x3fb53fb5,0xbffcbeac,0xbf5ebfe7,0x3fd93f88,0xc0000000,0x3fd9bf88,0x3ffa3ee4,0xbff63f0e,0x3fb5bfb5,0xbf0e3ff6,
0xbfc83fa0,0x3f5ebfe7,0x80004000,0xbf5ebfe7,0x8000c000,0x3f443fed,0xbfb5bfb5,0x3fed3f44,0x3fc83fa0,0xbff2bf29,0x40000000,0xbff23f29,0xbffa3ee4,0x3fe0bf78,0xbfb53fb5,0x3f78bfe0,
0x3f5ebfe7,0xbee43ffa,0x8000c000,0x3ee43ffa,0x3f5e3fe7,0xbf94bfd1,0x3fb53fb5,0xbfd1bf94,0xbffabee4,0x3ffe3e65,0xc0000000,0x3ffebe65,0x3fc8bfa0,0xbfbf3fab,0x3fb5bfb5,0xbfab3fbf,
0,0,0,0,0,0,0,0,0,0,0,0,0,0,0,0,
0xbffabee4,0x3fe7bf5e,0xbf5e3fe7,0xbee4bffa,0x3fe7bf5e,0xbf783fe0,0xbe65bffe,0x3fab3fbf,0xbf5e3fe7,0xbe65bffe,0x3fa03fc8,0xbff2bf29,0xbee4bffa,0x3fab3fbf,0xbff2bf29,0x3ffcbeac,
0x3fc83fa0,0xbffabee4,0x3ffabee4,0xbfc83fa0,0xc0000000,0x3fedbf44,0xbfb53fb5,0x3f44bfed,0x3fc8bfa0,0xbf883fd9,0x3ee4bffa,0x3e653ffe,0xbee43ffa,0xbde6c000,0x3f293ff2,0xbf94bfd1,
0xbf5ebfe7,0x3fa03fc8,0xbfc8bfa0,0x3fe73f5e,0x3fe73f5e,0xbff6bf0e,0x3ffe3e65,0xc0003de6,0xbffa3ee4,0x3ff2bf29,0xbfe73f5e,0x3fd9bf88,0x3fa0bfc8,0xbf943fd1,0x3f88bfd9,0xbf783fe0,
0,0,0,0,0,0,0,0,0,0,0,0,0,0,0,0,
0x3fc83fa0,0xc0000000,0x3fc8bfa0,0xbee43ffa,0xbffabee4,0x3fedbf44,0xbf883fd9,0xbde6c000,0x3ffabee4,0xbfb53fb5,0x3ee4bffa,0x3f293ff2,0xbfc83fa0,0x3f44bfed,0x3e653ffe,0xbf94bfd1,
0x3f5ebfe7,0x80004000,0xbf5ebfe7,0x3fc83fa0,0x80004000,0xbf44bfed,0x3fb53fb5,0xbfedbf44,0xbf5ebfe7,0x3fb53fb5,0xbfe7bf5e,0x3ffe3e65,0x3fc83fa0,0xbfedbf44,0x3ffe3e65,0xbffc3eac,
0xbffabee4,0x40000000,0xbffa3ee4,0x3fe7bf5e,0x3ffabee4,0xbfed3f44,0x3fd9bf88,0xbfbf3fab,0xbfc83fa0,0x3fb5bfb5,0xbfa03fc8,0x3f88bfd9,0x3f5ebfe7,0xbf443fed,0x3f29bff2,0xbf0e3ff6,
0,0,0,0,0,0,0,0,0,0,0,0,0,0,0,0,
0xbf5ebfe7,0x3fe73f5e,0xbffa3ee4,0x3fa0bfc8,0x3fa03fc8,0xbff6bf0e,0x3ff2bf29,0xbf943fd1,0xbfc8bfa0,0x3ffe3e65,0xbfe73f5e,0x3f88bfd9,0x3fe73f5e,0xc0003de6,0x3fd9bf88,0xbf783fe0,
0xbffabee4,0x3ffabee4,0xbfc83fa0,0x3f5ebfe7,0x40000000,0xbfed3f44,0x3fb5bfb5,0xbf443fed,0xbffa3ee4,0x3fd9bf88,0xbfa03fc8,0x3f29bff2,0x3fe7bf5e,0xbfbf3fab,0x3f88bfd9,0xbf0e3ff6,
0xbfc83fa0,0x3fa0bfc8,0xbf5e3fe7,0x3ee4bffa,0x3fa0bfc8,0xbf783fe0,0x3f29bff2,0xbeac3ffc,0xbf5e3fe7,0x3f29bff2,0xbee43ffa,0x3e65bffe,0x3ee4bffa,0xbeac3ffc,0x3e65bffe,0xbde64000,
0,0,0,0,0,0,0,0,0,0,0,0,0,0,0,0,
0x3f80,0,0,0,0xbf80,0,0,0,0x3f80,0,0,0,0xbf80,0,0,0,
0x3f80,0,0,0,0xbf80,0,0,0,0x3f80,0,0,0,0xbf80,0,0,0,
0x3f80,0,0,0,0xbf80,0,0,0,0x3f80,0,0,0,0xbf80,0,0,0,
0,0,0,0,0,0,0,0,0,0,0,0,0,0,0,0,
0,0,0,0,0,0,0,0,0,0,0,0,0,0,0,0,
0,0,0,0,0,0,0,0,0,0,0,0,0,0,0,0,
0,0,0,0,0,0,0,0,0,0,0,0,0,0,0,0,
0,0,0,0,0,0,0,0,0,0,0,0,0,0,0,0,
0x3f80,0x8000c000,0x80004000,0x8000c000,0x3f80,0x3de6c000,0xbe653ffe,0x3eacbffc,0x3f80,0x3e65bffe,0xbee43ffa,0x3f29bff2,0x3f80,0x3eacbffc,0xbf293ff2,0x3f78bfe0,
0x3f80,0x3ee4bffa,0xbf5e3fe7,0x3fa0bfc8,0x3f80,0x3f0ebff6,0xbf883fd9,0x3fbfbfab,0x3f80,0x3f29bff2,0xbfa03fc8,0x3fd9bf88,0x3f80,0x3f44bfed,0xbfb53fb5,0x3fedbf44,
0x3f80,0x3f5ebfe7,0xbfc83fa0,0x3ffabee4,0x3f80,0x3f78bfe0,0xbfd93f88,0x4000bde6,0x3f80,0x3f88bfd9,0xbfe73f5e,0x3ffe3e65,0x3f80,0x3f94bfd1,0xbff23f29,0x3ff63f0e,
0x3f80,0x3fa0bfc8,0xbffa3ee4,0x3fe73f5e,0x3f80,0x3fabbfbf,0xbffe3e65,0x3fd13f94,0x3f80,0x3fb5bfb5,0xc0000000,0x3fb53fb5,0x3f80,0x3fbfbfab,0xbffebe65,0x3f943fd1,
0x80004000,0x8000c000,0x80004000,0x8000c000,0xbee43ffa,0x3f0ebff6,0xbf293ff2,0x3f44bfed,0xbf5e3fe7,0x3f88bfd9,0xbfa03fc8,0x3fb5bfb5,0xbfa03fc8,0x3fbfbfab,0xbfd93f88,0x3fedbf44,
0xbfc83fa0,0x3fe7bf5e,0xbffa3ee4,0x40000000,0xbfe73f5e,0x3ffcbeac,0xbffebe65,0x3fed3f44,0xbffa3ee4,0x3ffe3e65,0xbfe7bf5e,0x3fb53fb5,0xc0000000,0x3fed3f44,0xbfb5bfb5,0x3f443fed,
0xbffabee4,0x3fc83fa0,0xbf5ebfe7,0x80004000,0xbfe7bf5e,0x3f943fd1,0xbe65bffe,0xbf443fed,0xbfc8bfa0,0x3f293ff2,0x3ee4bffa,0xbfb53fb5,0xbfa0bfc8,0x3de64000,0x3f88bfd9,0xbfed3f44,
0xbf5ebfe7,0xbee43ffa,0x3fc8bfa0,0xc0000000,0xbee4bffa,0xbf783fe0,0x3ff2bf29,0xbfedbf44,0x8000c000,0xbfb53fb5,0x40000000,0xbfb5bfb5,0x3ee4bffa,0xbfe03f78,0x3ff23f29,0xbf44bfed,
0x80004000,0x8000c000,0x80004000,0x8000c000,0xbf5e3fe7,0x3f78bfe0,0xbf883fd9,0x3f94bfd1,0xbfc83fa0,0x3fd9bf88,0xbfe73f5e,0x3ff2bf29,0xbffa3ee4,0x4000bde6,0xbffebe65,0x3ff63f0e,
0xbffabee4,0x3fe73f5e,0xbfc8bfa0,0x3fa03fc8,0xbfc8bfa0,0x3f943fd1,0xbf29bff2,0x3de64000,0xbf5ebfe7,0x3e653ffe,0x3ee4bffa,0xbf883fd9,0x8000c000,0xbf443fed,0x3fb5bfb5,0xbfed3f44,
0x3f5ebfe7,0xbfc83fa0,0x3ffabee4,0xbffabee4,0x3fc8bfa0,0xbffc3eac,0x3ff23f29,0xbfabbfbf,0x3ffabee4,0xbff2bf29,0x3fa03fc8,0xbe65bffe,0x3ffa3ee4,0xbfabbfbf,0x3e653ffe,0x3f78bfe0,
0x3fc83fa0,0xbee4bffa,0xbf5e3fe7,0x3fe7bf5e,0x3f5e3fe7,0x3f0ebff6,0xbfd93f88,0x3ffc3eac,0x80004000,0x3fb5bfb5,0xc0000000,0x3fb53fb5,0xbf5e3fe7,0x3ff6bf0e,0xbfd9bf88,0x3eac3ffc,
0x80004000,0x8000c000,0x80004000,0x8000c000,0xbfa03fc8,0x3fabbfbf,0xbfb53fb5,0x3fbfbfab,0xbffa3ee4,0x3ffebe65,0xc0000000,0x3ffe3e65,0xbfe7bf5e,0x3fd13f94,0xbfb5bfb5,0x3f943fd1,
0xbf5ebfe7,0x3ee43ffa,0x8000c000,0xbee43ffa,0x3ee4bffa,0xbf783fe0,0x3fb5bfb5,0xbfe03f78,0x3fc8bfa0,0xbff23f29,0x40000000,0xbff2bf29,0x40000000,0xbfedbf44,0x3fb53fb5,0xbf44bfed,
0x3fc83fa0,0xbf5ebfe7,0x80004000,0x3f5ebfe7,0x3ee43ffa,0x3f0ebff6,0xbfb53fb5,0x3ff6bf0e,0xbf5e3fe7,0x3fd9bf88,0xc0000000,0x3fd93f88,0xbfe73f5e,0x3ffc3eac,0xbfb5bfb5,0x3eac3ffc,
0xbffabee4,0x3fa03fc8,0x8000c000,0xbfa03fc8,0xbfa0bfc8,0xbde64000,0x3fb5bfb5,0xc0003de6,0x8000c000,0xbfb53fb5,0x40000000,0xbfb5bfb5,0x3fa0bfc8,0xc0003de6,0x3fb53fb5,0x3de6c000,
0x80004000,0x8000c000,0x80004000,0x8000c000,0xbfc83fa0,0x3fd1bf94,0xbfd93f88,0x3fe0bf78,0xbffabee4,0x3ff23f29,0xbfe7bf5e,0x3fd93f88,0xbf5ebfe7,0x3f0e3ff6,0xbe65bffe,0xbde64000,
0x3f5ebfe7,0xbfa03fc8,0x3fc8bfa0,0xbfe73f5e,0x3ffabee4,0xc000bde6,0x3ff23f29,0xbfd1bf94,0x3fc83fa0,0xbf88bfd9,0x3ee43ffa,0x3e65bffe,0x80004000,0x3f44bfed,0xbfb53fb5,0x3fedbf44,
0xbfc83fa0,0x3ffabee4,0xbffabee4,0x3fc83fa0,0xbffabee4,0x3fbf3fab,0xbf29bff2,0xbeac3ffc,0xbf5ebfe7,0xbe653ffe,0x3fa0bfc8,0xbff23f29,0x3f5ebfe7,0xbfe03f78,0x3ffe3e65,0xbfbfbfab,
0x3ffabee4,0xbfe7bf5e,0x3f5e3fe7,0x3ee4bffa,0x3fc83fa0,0xbeacbffc,0xbf883fd9,0x3ff6bf0e,0x80004000,0x3fb5bfb5,0xc0000000,0x3fb53fb5,0xbfc83fa0,0x3ffc3eac,0xbf88bfd9,0xbf0e3ff6,
0x80004000,0x8000c000,0x80004000,0x8000c000,0xbfe73f5e,0x3fedbf44,0xbff23f29,0x3ff6bf0e,0xbfc8bfa0,0x3fb53fb5,0xbfa0bfc8,0x3f883fd9,0x3ee4bffa,0xbf443fed,0x3f88bfd9,0xbfab3fbf,
0x3ffabee4,0xc0000000,0x3ffa3ee4,0xbfe7bf5e,0x3fa03fc8,0xbf44bfed,0x3e653ffe,0x3eacbffc,0xbf5e3fe7,0x3fb5bfb5,0xbfe73f5e,0x3ffebe65,0xc0000000,0x3fed3f44,0xbfb5bfb5,0x3f443fed,
0xbf5ebfe7,0x80004000,0x3f5ebfe7,0xbfc83fa0,0x3fa0bfc8,0xbfed3f44,0x3ffe3e65,0xbfd1bf94,0x3ffa3ee4,0xbfb5bfb5,0x3ee43ffa,0x3f29bff2,0x3ee43ffa,0x3f44bfed,0xbfd93f88,0x40003de6,
0xbfc83fa0,0x40000000,0xbfc8bfa0,0x3ee43ffa,0xbfe7bf5e,0x3f443fed,0x3f29bff2,0xbfe03f78,0x8000c000,0xbfb53fb5,0x40000000,0xbfb5bfb5,0x3fe7bf5e,0xbfedbf44,0x3f293ff2,0x3f78bfe0,
0x80004000,0x8000c000,0x80004000,0x8000c000,0xbffa3ee4,0x3ffcbeac,0xbffe3e65,0x4000bde6,0xbf5ebfe7,0x3f293ff2,0xbee4bffa,0x3e653ffe,0x3fc8bfa0,0xbfe03f78,0x3ff2bf29,0xbffc3eac,
0x3fc83fa0,0xbfa0bfc8,0x3f5e3fe7,0xbee4bffa,0xbf5e3fe7,0x3fabbfbf,0xbfd93f88,0x3ff6bf0e,0xbffabee4,0x3fd93f88,0xbfa0bfc8,0x3f293ff2,0x8000c000,0xbf443fed,0x3fb5bfb5,0xbfed3f44,
0x3ffabee4,0xbffabee4,0x3fc83fa0,0xbf5ebfe7,0x3f5e3fe7,0x3de6c000,0xbf883fd9,0x3fe0bf78,0xbfc83fa0,0x3ffebe65,0xbfe7bf5e,0x3f883fd9,0xbfc8bfa0,0x3f0e3ff6,0x3f29bff2,0xbfd13f94,
0x3f5ebfe7,0xbfe73f5e,0x3ffa3ee4,0xbfa0bfc8,0x3ffa3ee4,0xbf94bfd1,0xbe653ffe,0x3fbfbfab,0x80004000,0x3fb5bfb5,0xc0000000,0x3fb53fb5,0xbffa3ee4,0x3fd13f94,0xbe65bffe,0xbfab3fbf,
0x80004000,0x8000c000,0x80004000,0x8000c000,0xc0000000,0x40003de6,0xbffebe65,0x3ffc3eac,0x8000c000,0xbe653ffe,0x3ee4bffa,0xbf293ff2,0x40000000,0xbffcbeac,0x3ff23f29,0xbfe0bf78,
0x80004000,0x3ee4bffa,0xbf5e3fe7,0x3fa0bfc8,0xc0000000,0x3ff63f0e,0xbfd9bf88,0x3fab3fbf,0x8000c000,0xbf293ff2,0x3fa0bfc8,0xbfd93f88,0x40000000,0xbfedbf44,0x3fb53fb5,0xbf44bfed,
0x80004000,0x3f5ebfe7,0xbfc83fa0,0x3ffabee4,0xc0000000,0x3fe03f78,0xbf88bfd9,0x3de64000,0x8000c000,0xbf883fd9,0x3fe7bf5e,0xbffebe65,0x40000000,0xbfd1bf94,0x3f293ff2,0x3f0ebff6,
0x80004000,0x3fa0bfc8,0xbffa3ee4,0x3fe73f5e,0xc0000000,0x3fbf3fab,0xbe65bffe,0xbf943fd1,0x8000c000,0xbfb53fb5,0x40000000,0xbfb5bfb5,0x40000000,0xbfabbfbf,0xbe653ffe,0x3fd1bf94,
0x80004000,0x8000c000,0x80004000,0x8000c000,0xbffabee4,0x3ff63f0e,0xbff2bf29,0x3fed3f44,0x3f5ebfe7,0xbf883fd9,0x3fa0bfc8,0xbfb53fb5,0x3fc83fa0,0xbfabbfbf,0x3f883fd9,0xbf44bfed,
0xbfc83fa0,0x3fe7bf5e,0xbffa3ee4,0x40000000,0xbf5ebfe7,0x3eac3ffc,0x3e65bffe,0xbf443fed,0x3ffabee4,0xbffebe65,0x3fe73f5e,0xbfb5bfb5,0x80004000,0x3f44bfed,0xbfb53fb5,0x3fedbf44,
0xbffabee4,0x3fc83fa0,0xbf5ebfe7,0x80004000,0x3f5ebfe7,0xbfd13f94,0x3ffebe65,0xbfedbf44,0x3fc83fa0,0xbf29bff2,0xbee43ffa,0x3fb5bfb5,0xbfc83fa0,0x4000bde6,0xbfd9bf88,0x3f443fed,
0xbf5ebfe7,0xbee43ffa,0x3fc8bfa0,0xc0000000,0x3ffabee4,0xbfe0bf78,0x3f293ff2,0x3f44bfed,0x80004000,0x3fb5bfb5,0xc0000000,0x3fb53fb5,0xbffabee4,0x3f783fe0,0x3f29bff2,0xbfed3f44,
0x80004000,0x8000c000,0x80004000,0x8000c000,0xbfe7bf5e,0x3fe03f78,0xbfd9bf88,0x3fd13f94,0x3fc8bfa0,0xbfd93f88,0x3fe7bf5e,0xbff23f29,0x3ee43ffa,0xbde6c000,0xbe653ffe,0x3f0ebff6,
0xbffabee4,0x3fe73f5e,0xbfc8bfa0,0x3fa03fc8,0x3fa0bfc8,0xbfd13f94,0x3ff2bf29,0xc0003de6,0x3f5e3fe7,0xbe65bffe,0xbee43ffa,0x3f88bfd9,0xc0000000,0x3fed3f44,0xbfb5bfb5,0x3f443fed,
0x3f5ebfe7,0xbfc83fa0,0x3ffabee4,0xbffabee4,0x3fa03fc8,0xbeacbffc,0xbf293ff2,0x3fbfbfab,0xbffa3ee4,0x3ff23f29,0xbfa0bfc8,0x3e653ffe,0x3ee4bffa,0xbfbf3fab,0x3ffebe65,0xbfe0bf78,
0x3fc83fa0,0xbee4bffa,0xbf5e3fe7,0x3fe7bf5e,0xbfe73f5e,0x3ff63f0e,0xbf88bfd9,0xbeac3ffc,0x8000c000,0xbfb53fb5,0x40000000,0xbfb5bfb5,0x3fe73f5e,0xbf0ebff6,0xbf883fd9,0x3ffcbeac,
0x80004000,0x8000c000,0x80004000,0x8000c000,0xbfc8bfa0,0x3fbf3fab,0xbfb5bfb5,0x3fab3fbf,0x3ffabee4,0xbffe3e65,0x40000000,0xbffebe65,0xbf5e3fe7,0x3f94bfd1,0xbfb53fb5,0x3fd1bf94,
0xbf5ebfe7,0x3ee43ffa,0x8000c000,0xbee43ffa,0x3ffa3ee4,0xbfe0bf78,0x3fb53fb5,0xbf78bfe0,0xbfc83fa0,0x3ff2bf29,0xc0000000,0x3ff23f29,0x8000c000,0xbf443fed,0x3fb5bfb5,0xbfed3f44,
0x3fc83fa0,0xbf5ebfe7,0x80004000,0x3f5ebfe7,0xbffa3ee4,0x3ff63f0e,0xbfb5bfb5,0x3f0e3ff6,0x3f5ebfe7,0xbfd93f88,0x40000000,0xbfd9bf88,0x3f5e3fe7,0x3eacbffc,0xbfb53fb5,0x3ffcbeac,
0xbffabee4,0x3fa03fc8,0x8000c000,0xbfa03fc8,0x3fc8bfa0,0xc000bde6,0x3fb53fb5,0xbde6c000,0x80004000,0x3fb5bfb5,0xc0000000,0x3fb53fb5,0xbfc8bfa0,0x3de64000,0x3fb5bfb5,0xc000bde6,
0x80004000,0x8000c000,0x80004000,0x8000c000,0xbfa0bfc8,0x3f943fd1,0xbf88bfd9,0x3f783fe0,0x3ffa3ee4,0xbff2bf29,0x3fe73f5e,0xbfd9bf88,0xbfe73f5e,0x3ff6bf0e,0xbffe3e65,0x40003de6,
0x3f5ebfe7,0xbfa03fc8,0x3fc8bfa0,0xbfe73f5e,0x3ee43ffa,0x3de6c000,0xbf293ff2,0x3f94bfd1,0xbfc8bfa0,0x3f883fd9,0xbee4bffa,0xbe653ffe,0x40000000,0xbfedbf44,0x3fb53fb5,0xbf44bfed,
0xbfc83fa0,0x3ffabee4,0xbffabee4,0x3fc83fa0,0x3ee4bffa,0xbfab3fbf,0x3ff2bf29,0xbffcbeac,0x3f5e3fe7,0x3e65bffe,0xbfa03fc8,0x3ff2bf29,0xbfe7bf5e,0x3f783fe0,0x3e65bffe,0xbfab3fbf,
0x3ffabee4,0xbfe7bf5e,0x3f5e3fe7,0x3ee4bffa,0xbfa03fc8,0x3ffcbeac,0xbfd9bf88,0x3f0e3ff6,0x8000c000,0xbfb53fb5,0x40000000,0xbfb5bfb5,0x3fa03fc8,0x3eacbffc,0xbfd93f88,0x3ff63f0e,
0x80004000,0x8000c000,0x80004000,0x8000c000,0xbf5ebfe7,0x3f443fed,0xbf29bff2,0x3f0e3ff6,0x3fc83fa0,0xbfb5bfb5,0x3fa03fc8,0xbf88bfd9,0xbffabee4,0x3fed3f44,0xbfd9bf88,0x3fbf3fab,
0x3ffabee4,0xc0000000,0x3ffa3ee4,0xbfe7bf5e,0xbfc83fa0,0x3fedbf44,0xbffe3e65,0x3ffc3eac,0x3f5ebfe7,0xbfb53fb5,0x3fe7bf5e,0xbffe3e65,0x80004000,0x3f44bfed,0xbfb53fb5,0x3fedbf44,
0xbf5ebfe7,0x80004000,0x3f5ebfe7,0xbfc83fa0,0x3fc83fa0,0xbf44bfed,0xbe653ffe,0x3f94bfd1,0xbffabee4,0x3fb53fb5,0xbee4bffa,0xbf293ff2,0x3ffabee4,0xbfedbf44,0x3f883fd9,0x3de6c000,
0xbfc83fa0,0x40000000,0xbfc8bfa0,0x3ee43ffa,0x3f5ebfe7,0xbfed3f44,0x3ff23f29,0xbf78bfe0,0x80004000,0x3fb5bfb5,0xc0000000,0x3fb53fb5,0xbf5ebfe7,0xbf443fed,0x3ff2bf29,0xbfe0bf78,
0x80004000,0x8000c000,0x80004000,0x8000c000,0xbee4bffa,0x3eac3ffc,0xbe65bffe,0x3de64000,0x3f5e3fe7,0xbf29bff2,0x3ee43ffa,0xbe65bffe,0xbfa0bfc8,0x3f783fe0,0xbf29bff2,0x3eac3ffc,
0x3fc83fa0,0xbfa0bfc8,0x3f5e3fe7,0xbee4bffa,0xbfe7bf5e,0x3fbf3fab,0xbf88bfd9,0x3f0e3ff6,0x3ffa3ee4,0xbfd9bf88,0x3fa03fc8,0xbf29bff2,0xc0000000,0x3fed3f44,0xbfb5bfb5,0x3f443fed,
0x3ffabee4,0xbffabee4,0x3fc83fa0,0xbf5ebfe7,0xbfe73f5e,0x40003de6,0xbfd9bf88,0x3f783fe0,0x3fc8bfa0,0xbffe3e65,0x3fe73f5e,0xbf88bfd9,0xbfa03fc8,0x3ff6bf0e,0xbff2bf29,0x3f943fd1,
0x3f5ebfe7,0xbfe73f5e,0x3ffa3ee4,0xbfa0bfc8,0xbee43ffa,0x3fd1bf94,0xbffebe65,0x3fab3fbf,0x8000c000,0xbfb53fb5,0x40000000,0xbfb5bfb5,0x3ee43ffa,0x3f94bfd1,0xbffe3e65,0x3fbf3fab,
0x3f80,0,0,0,0xbf80,0,0,0,0x3f80,0,0,0,0xbf80,0,0,0,
0x3f80,0,0,0,0xbf80,0,0,0,0x3f80,0,0,0,0xbf80,0,0,0,
0x3f80,0,0,0,0xbf80,0,0,0,0x3f80,0,0,0,0xbf80,0,0,0,
0x3f80,0,0,0,0xbf80,0,0,0,0x3f80,0,0,0,0xbf80,0,0,0,
0,0,0,0,0,0,0,0,0,0,0,0,0,0,0,0,
0,0,0,0,0,0,0,0,0,0,0,0,0,0,0,0,
0,0,0,0,0,0,0,0,0,0,0,0,0,0,0,0,
0,0,0,0,0,0,0,0,0,0,0,0,0,0,0,0,
0x3f80,0x3fc8bfa0,0xbffabee4,0x3f5e3fe7,0x3f80,0x3fd1bf94,0xbff2bf29,0x3f0e3ff6,0x3f80,0x3fd9bf88,0xbfe7bf5e,0x3e653ffe,0x3f80,0x3fe0bf78,0xbfd9bf88,0xbde64000,
0x3f80,0x3fe7bf5e,0xbfc8bfa0,0xbee43ffa,0x3f80,0x3fedbf44,0xbfb5bfb5,0xbf443fed,0x3f80,0x3ff2bf29,0xbfa0bfc8,0xbf883fd9,0x3f80,0x3ff6bf0e,0xbf88bfd9,0xbfab3fbf,
0x3f80,0x3ffabee4,0xbf5ebfe7,0xbfc83fa0,0x3f80,0x3ffcbeac,0xbf29bff2,0xbfe03f78,0x3f80,0x3ffebe65,0xbee4bffa,0xbff23f29,0x3f80,0x4000bde6,0xbe65bffe,0xbffc3eac,
0,0,0,0,0,0,0,0,0,0,0,0,0,0,0,0,
0x3f5ebfe7,0xbffa3ee4,0x3fc83fa0,0x8000c000,0x3fa0bfc8,0xc000bde6,0x3f883fd9,0x3f44bfed,0x3fc8bfa0,0xbff2bf29,0x3ee43ffa,0x3fb5bfb5,0x3fe7bf5e,0xbfd1bf94,0xbe653ffe,0x3fedbf44,
0x3ffabee4,0xbfa0bfc8,0xbf5e3fe7,0x40000000,0x40000000,0xbf44bfed,0xbfb53fb5,0x3fed3f44,0x3ffa3ee4,0xbe65bffe,0xbfe73f5e,0x3fb53fb5,0x3fe73f5e,0x3eacbffc,0xbffe3e65,0x3f443fed,
0x3fc83fa0,0x3f5ebfe7,0xbffabee4,0x80004000,0x3fa03fc8,0x3fabbfbf,0xbfd9bf88,0xbf443fed,0x3f5e3fe7,0x3fd9bf88,0xbfa0bfc8,0xbfb53fb5,0x3ee43ffa,0x3ff6bf0e,0xbf29bff2,0xbfed3f44,
0,0,0,0,0,0,0,0,0,0,0,0,0,0,0,0,
0xbfc83fa0,0x3ffa3ee4,0xbf5ebfe7,0xbf5e3fe7,0xbffa3ee4,0x3fbf3fab,0x3e65bffe,0xbfe03f78,0xbffabee4,0x3f293ff2,0x3fa0bfc8,0xbffebe65,0xbfc8bfa0,0xbeac3ffc,0x3ff2bf29,0xbfbfbfab,
0xbf5ebfe7,0xbfa03fc8,0x3ffa3ee4,0xbee4bffa,0x8000c000,0xbfed3f44,0x3fb53fb5,0x3f44bfed,0x3f5ebfe7,0xbffebe65,0x3ee43ffa,0x3fd9bf88,0x3fc8bfa0,0xbfd1bf94,0xbf293ff2,0x40003de6,
0x3ffabee4,0xbf5ebfe7,0xbfc83fa0,0x3fc83fa0,0x3ffa3ee4,0x3de6c000,0xbffe3e65,0x3f0e3ff6,0x3fc83fa0,0x3f88bfd9,0xbfe7bf5e,0xbf293ff2,0x3f5e3fe7,0x3fe0bf78,0xbf88bfd9,0xbfd13f94,
0,0,0,0,0,0,0,0,0,0,0,0,0,0,0,0,
0x3ffabee4,0xbfc8bfa0,0x80004000,0x3fc8bfa0,0x3fe73f5e,0xbeacbffc,0xbfb53fb5,0x3ffc3eac,0x3f5e3fe7,0x3f88bfd9,0xc0000000,0x3f883fd9,0xbee43ffa,0x3ff6bf0e,0xbfb5bfb5,0xbf0e3ff6,
0xbfc83fa0,0x3fe73f5e,0x8000c000,0xbfe73f5e,0xc0000000,0x3f443fed,0x3fb5bfb5,0xbfedbf44,0xbfc8bfa0,0xbf293ff2,0x40000000,0xbf29bff2,0xbee4bffa,0xbfe03f78,0x3fb53fb5,0x3f78bfe0,
0x3f5ebfe7,0xbffabee4,0x80004000,0x3ffabee4,0x3fe7bf5e,0xbf94bfd1,0xbfb53fb5,0x3fd13f94,0x3ffa3ee4,0x3e65bffe,0xc0000000,0x3e653ffe,0x3fa03fc8,0x3fbfbfab,0xbfb5bfb5,0xbfab3fbf,
0,0,0,0,0,0,0,0,0,0,0,0,0,0,0,0,
0xbffabee4,0x3f5e3fe7,0x3f5ebfe7,0xbffa3ee4,0xbf5ebfe7,0xbf783fe0,0x3ffebe65,0xbfabbfbf,0x3f5ebfe7,0xbffe3e65,0x3fa03fc8,0x3f29bff2,0x3ffabee4,0xbfabbfbf,0xbf293ff2,0x3ffcbeac,
0x3fc83fa0,0x3ee4bffa,0xbffa3ee4,0x3fa03fc8,0x80004000,0x3fedbf44,0xbfb5bfb5,0xbf443fed,0xbfc83fa0,0x3fd93f88,0x3ee4bffa,0xbffe3e65,0xbffabee4,0x3de64000,0x3ff2bf29,0xbf94bfd1,
0xbf5ebfe7,0xbfc83fa0,0x3fc83fa0,0x3f5ebfe7,0x3f5ebfe7,0xbff6bf0e,0xbe653ffe,0x4000bde6,0x3ffabee4,0xbf29bff2,0xbfe73f5e,0x3f883fd9,0x3fc83fa0,0x3f94bfd1,0xbfd9bf88,0xbf783fe0,
0,0,0,0,0,0,0,0,0,0,0,0,0,0,0,0,
0x3fc83fa0,0x8000c000,0xbfc83fa0,0x3ffa3ee4,0xbee43ffa,0x3fedbf44,0xbfd9bf88,0x3de64000,0xbffa3ee4,0x3fb53fb5,0x3ee4bffa,0xbff23f29,0xbfa0bfc8,0xbf443fed,0x3ffebe65,0xbf94bfd1,
0x3f5ebfe7,0xc0000000,0x3f5e3fe7,0x3fa0bfc8,0x40000000,0xbf44bfed,0xbfb53fb5,0x3fed3f44,0x3f5e3fe7,0x3fb5bfb5,0xbfe7bf5e,0xbe653ffe,0xbfa03fc8,0x3fed3f44,0x3e65bffe,0xbffc3eac,
0xbffabee4,0x80004000,0x3ffabee4,0xbf5ebfe7,0xbee4bffa,0xbfed3f44,0x3f883fd9,0x3fbfbfab,0x3fc8bfa0,0xbfb5bfb5,0xbfa03fc8,0x3fd93f88,0x3fe73f5e,0x3f44bfed,0xbff2bf29,0xbf0e3ff6,
0,0,0,0,0,0,0,0,0,0,0,0,0,0,0,0,
0xbf5ebfe7,0xbf5e3fe7,0x3ffabee4,0xbfc8bfa0,0x3fc8bfa0,0xbff6bf0e,0x3f293ff2,0x3f94bfd1,0x3fc83fa0,0x3e65bffe,0xbfe73f5e,0x3fd93f88,0xbf5e3fe7,0x4000bde6,0xbf88bfd9,0xbf783fe0,
0xbffabee4,0x3ee43ffa,0x3fc8bfa0,0xbfe7bf5e,0x8000c000,0xbfed3f44,0x3fb53fb5,0x3f44bfed,0x3ffabee4,0xbf88bfd9,0xbfa03fc8,0x3ff23f29,0x3f5e3fe7,0x3fbfbfab,0xbfd9bf88,0xbf0e3ff6,
0xbfc83fa0,0x3fc83fa0,0x3f5ebfe7,0xbffabee4,0xbfc8bfa0,0xbf783fe0,0x3ff23f29,0x3eacbffc,0x3f5ebfe7,0xbff2bf29,0xbee43ffa,0x3ffe3e65,0x3ffa3ee4,0x3eacbffc,0xbffebe65,0xbde64000,
0,0,0,0,0,0,0,0,0,0,0,0,0,0,0,0,
0x80004000,0x3fc8bfa0,0xbffabee4,0x3f5e3fe7,0xc0000000,0x3f943fd1,0x3f29bff2,0xbff63f0e,0x8000c000,0xbfd93f88,0x3fe73f5e,0xbe65bffe,0x40000000,0xbf78bfe0,0xbf883fd9,0x40003de6,
0x80004000,0x3fe7bf5e,0xbfc8bfa0,0xbee43ffa,0xc0000000,0x3f443fed,0x3fb5bfb5,0xbfedbf44,0x8000c000,0xbff23f29,0x3fa03fc8,0x3f88bfd9,0x40000000,0xbf0ebff6,0xbfd93f88,0x3fbf3fab,
0x80004000,0x3ffabee4,0xbf5ebfe7,0xbfc83fa0,0xc0000000,0x3eac3ffc,0x3ff2bf29,0xbf78bfe0,0x8000c000,0xbffe3e65,0x3ee43ffa,0x3ff2bf29,0x40000000,0xbde6c000,0xbffe3e65,0x3eac3ffc,
0,0,0,0,0,0,0,0,0,0,0,0,0,0,0,0,
0x3f5ebfe7,0xbffa3ee4,0x3fc83fa0,0x8000c000,0x3fc83fa0,0x3de6c000,0xbfd93f88,0x3fed3f44,0xbfc83fa0,0x3ff23f29,0xbee4bffa,0xbfb53fb5,0xbf5ebfe7,0xbf943fd1,0x3ffe3e65,0xbf44bfed,
0x3ffabee4,0xbfa0bfc8,0xbf5e3fe7,0x40000000,0x80004000,0x3fedbf44,0xbfb5bfb5,0xbf443fed,0xbffabee4,0x3e653ffe,0x3fe7bf5e,0xbfb5bfb5,0x3f5ebfe7,0xbffcbeac,0x3e653ffe,0x3fedbf44,
0x3fc83fa0,0x3f5ebfe7,0xbffabee4,0x80004000,0xbfc83fa0,0x3fbf3fab,0x3f88bfd9,0xbfedbf44,0xbf5ebfe7,0xbfd93f88,0x3fa03fc8,0x3fb5bfb5,0x3ffabee4,0xbf0ebff6,0xbff23f29,0x3f443fed,
0,0,0,0,0,0,0,0,0,0,0,0,0,0,0,0,
0xbfc83fa0,0x3ffa3ee4,0xbf5ebfe7,0xbf5e3fe7,0xbee4bffa,0xbfab3fbf,0x3ffe3e65,0xbf78bfe0,0x3ffa3ee4,0xbf29bff2,0xbfa03fc8,0x3ffe3e65,0xbfa03fc8,0x3ffc3eac,0xbf29bff2,0xbfab3fbf,
0xbf5ebfe7,0xbfa03fc8,0x3ffa3ee4,0xbee4bffa,0x40000000,0xbf44bfed,0xbfb53fb5,0x3fed3f44,0xbf5e3fe7,0x3ffe3e65,0xbee4bffa,0xbfd93f88,0xbfa0bfc8,0xbf943fd1,0x3ff23f29,0x3de6c000,
0x3ffabee4,0xbf5ebfe7,0xbfc83fa0,0x3fc83fa0,0xbee43ffa,0x40003de6,0xbe65bffe,0xbff63f0e,0xbfc8bfa0,0xbf883fd9,0x3fe73f5e,0x3f29bff2,0x3fe7bf5e,0xbf78bfe0,0xbfd93f88,0x3f943fd1,
0,0,0,0,0,0,0,0,0,0,0,0,0,0,0,0,
0x3ffabee4,0xbfc8bfa0,0x80004000,0x3fc8bfa0,0xbf5e3fe7,0x3ffcbeac,0xbfb5bfb5,0xbeac3ffc,0xbf5ebfe7,0xbf883fd9,0x40000000,0xbf88bfd9,0x3ffa3ee4,0xbf0ebff6,0xbfb53fb5,0x3ff63f0e,
0xbfc83fa0,0x3fe73f5e,0x8000c000,0xbfe73f5e,0x8000c000,0xbfed3f44,0x3fb53fb5,0x3f44bfed,0x3fc83fa0,0x3f29bff2,0xc0000000,0x3f293ff2,0xbffa3ee4,0x3f783fe0,0x3fb5bfb5,0xbfe0bf78,
0x3f5ebfe7,0xbffabee4,0x80004000,0x3ffabee4,0x3f5e3fe7,0x3fd1bf94,0xbfb5bfb5,0xbf943fd1,0xbffabee4,0xbe653ffe,0x40000000,0xbe65bffe,0x3fc8bfa0,0xbfabbfbf,0xbfb53fb5,0x3fbf3fab,
0,0,0,0,0,0,0,0,0,0,0,0,0,0,0,0,
0xbffabee4,0x3f5e3fe7,0x3f5ebfe7,0xbffa3ee4,0x3fe7bf5e,0xbfe0bf78,0x3e653ffe,0x3fbfbfab,0xbf5e3fe7,0x3ffebe65,0xbfa0bfc8,0xbf293ff2,0xbee4bffa,0xbfbf3fab,0x3ff23f29,0xbeacbffc,
0x3fc83fa0,0x3ee4bffa,0xbffa3ee4,0x3fa03fc8,0xc0000000,0x3f443fed,0x3fb5bfb5,0xbfedbf44,0x3fc8bfa0,0xbfd9bf88,0xbee43ffa,0x3ffebe65,0xbee43ffa,0x4000bde6,0xbf29bff2,0xbfd13f94,
0xbf5ebfe7,0xbfc83fa0,0x3fc83fa0,0x3f5ebfe7,0x3fe73f5e,0x3f0ebff6,0xbffebe65,0x3de64000,0xbffa3ee4,0x3f293ff2,0x3fe7bf5e,0xbf88bfd9,0x3fa0bfc8,0xbfd1bf94,0xbf883fd9,0x3fe03f78,
0,0,0,0,0,0,0,0,0,0,0,0,0,0,0,0,
0x3fc83fa0,0x8000c000,0xbfc83fa0,0x3ffa3ee4,0xbffabee4,0x3f443fed,0x3f88bfd9,0xc0003de6,0x3ffabee4,0xbfb5bfb5,0xbee43ffa,0x3ff2bf29,0xbfc83fa0,0x3fed3f44,0xbe65bffe,0xbfd13f94,
0x3f5ebfe7,0xc0000000,0x3f5e3fe7,0x3fa0bfc8,0x80004000,0x3fedbf44,0xbfb5bfb5,0xbf443fed,0xbf5ebfe7,0xbfb53fb5,0x3fe73f5e,0x3e65bffe,0x3fc83fa0,0x3f44bfed,0xbffebe65,0x3eac3ffc,
0xbffabee4,0x80004000,0x3ffabee4,0xbf5ebfe7,0x3ffabee4,0xbf44bfed,0xbfd93f88,0x3fab3fbf,0xbfc83fa0,0x3fb53fb5,0x3fa0bfc8,0xbfd9bf88,0x3f5ebfe7,0xbfedbf44,0xbf293ff2,0x3ff63f0e,
0,0,0,0,0,0,0,0,0,0,0,0,0,0,0,0,
0xbf5ebfe7,0xbf5e3fe7,0x3ffabee4,0xbfc8bfa0,0x3fa03fc8,0x3f0ebff6,0xbff23f29,0x3fd13f94,0xbfc8bfa0,0xbe653ffe,0x3fe7bf5e,0xbfd9bf88,0x3fe73f5e,0xbde6c000,0xbfd93f88,0x3fe03f78,
0xbffabee4,0x3ee43ffa,0x3fc8bfa0,0xbfe7bf5e,0x40000000,0xbf44bfed,0xbfb53fb5,0x3fed3f44,0xbffa3ee4,0x3f883fd9,0x3fa0bfc8,0xbff2bf29,0x3fe7bf5e,0xbfabbfbf,0xbf883fd9,0x3ff63f0e,
0xbfc83fa0,0x3fc83fa0,0x3f5ebfe7,0xbffabee4,0x3fa0bfc8,0xbfe0bf78,0xbf293ff2,0x3ffc3eac,0xbf5e3fe7,0x3ff23f29,0x3ee4bffa,0xbffebe65,0x3ee4bffa,0xbffcbeac,0xbe653ffe,0x40003de6,
0,0,0,0,0,0,0,0,0,0,0,0,0,0,0,0,
0x3f80,0,0,0,0xbf80,0,0,0,0x3f80,0,0,0,0xbf80,0,0,0,
0x3f80,0,0,0,0xbf80,0,0,0,0x3f80,0,0,0,0xbf80,0,0,0,
0x3f80,0,0,0,0xbf80,0,0,0,0x3f80,0,0,0,0xbf80,0,0,0,
0,0,0,0,0,0,0,0,0,0,0,0,0,0,0,0,
0,0,0,0,0,0,0,0,0,0,0,0,0,0,0,0,
0,0,0,0,0,0,0,0,0,0,0,0,0,0,0,0,
0,0,0,0,0,0,0,0,0,0,0,0,0,0,0,0,
0,0,0,0,0,0,0,0,0,0,0,0,0,0,0,0,
0x3f80,0x40000000,0x8000c000,0xc0000000,0x3f80,0x40003de6,0x3e65bffe,0xbffcbeac,0x3f80,0x3ffe3e65,0x3ee4bffa,0xbff2bf29,0x3f80,0x3ffc3eac,0x3f29bff2,0xbfe0bf78,
0x3f80,0x3ffa3ee4,0x3f5ebfe7,0xbfc8bfa0,0x3f80,0x3ff63f0e,0x3f88bfd9,0xbfabbfbf,0x3f80,0x3ff23f29,0x3fa0bfc8,0xbf88bfd9,0x3f80,0x3fed3f44,0x3fb5bfb5,0xbf44bfed,
0x3f80,0x3fe73f5e,0x3fc8bfa0,0xbee4bffa,0x3f80,0x3fe03f78,0x3fd9bf88,0xbde6c000,0x3f80,0x3fd93f88,0x3fe7bf5e,0x3e65bffe,0x3f80,0x3fd13f94,0x3ff2bf29,0x3f0ebff6,
0x3f80,0x3fc83fa0,0x3ffabee4,0x3f5ebfe7,0x3f80,0x3fbf3fab,0x3ffebe65,0x3f94bfd1,0x3f80,0x3fb53fb5,0x40000000,0x3fb5bfb5,0x3f80,0x3fab3fbf,0x3ffe3e65,0x3fd1bf94,
0x80004000,0x40000000,0x8000c000,0xc0000000,0xbee43ffa,0x3ff63f0e,0x3f29bff2,0xbfedbf44,0xbf5e3fe7,0x3fd93f88,0x3fa0bfc8,0xbfb5bfb5,0xbfa03fc8,0x3fab3fbf,0x3fd9bf88,0xbf44bfed,
0xbfc83fa0,0x3f5e3fe7,0x3ffabee4,0x8000c000,0xbfe73f5e,0x3eac3ffc,0x3ffe3e65,0x3f44bfed,0xbffa3ee4,0xbe653ffe,0x3fe73f5e,0x3fb5bfb5,0xc0000000,0xbf443fed,0x3fb53fb5,0x3fedbf44,
0xbffabee4,0xbfa03fc8,0x3f5e3fe7,0x40000000,0xbfe7bf5e,0xbfd13f94,0x3e653ffe,0x3fed3f44,0xbfc8bfa0,0xbff23f29,0xbee43ffa,0x3fb53fb5,0xbfa0bfc8,0xc0003de6,0xbf883fd9,0x3f443fed,
0xbf5ebfe7,0xbffabee4,0xbfc83fa0,0x80004000,0xbee4bffa,0xbfe0bf78,0xbff23f29,0xbf443fed,0x8000c000,0xbfb5bfb5,0xc0000000,0xbfb53fb5,0x3ee4bffa,0xbf78bfe0,0xbff2bf29,0xbfed3f44,
0x80004000,0x40000000,0x8000c000,0xc0000000,0xbf5e3fe7,0x3fe03f78,0x3f88bfd9,0xbfd1bf94,0xbfc83fa0,0x3f883fd9,0x3fe7bf5e,0xbf29bff2,0xbffa3ee4,0x3de64000,0x3ffe3e65,0x3f0ebff6,
0xbffabee4,0xbf5e3fe7,0x3fc83fa0,0x3fc8bfa0,0xbfc8bfa0,0xbfd13f94,0x3f293ff2,0x4000bde6,0xbf5ebfe7,0xbffe3e65,0xbee43ffa,0x3fd93f88,0x8000c000,0xbfedbf44,0xbfb53fb5,0x3f443fed,
0x3f5ebfe7,0xbfa0bfc8,0xbffa3ee4,0xbee43ffa,0x3fc8bfa0,0xbeacbffc,0xbff2bf29,0xbfbf3fab,0x3ffabee4,0x3f29bff2,0xbfa0bfc8,0xbffe3e65,0x3ffa3ee4,0x3fbfbfab,0xbe65bffe,0xbfe0bf78,
0x3fc83fa0,0x3ffabee4,0x3f5ebfe7,0xbf5ebfe7,0x3f5e3fe7,0x3ff63f0e,0x3fd9bf88,0x3eacbffc,0x80004000,0x3fb53fb5,0x40000000,0x3fb5bfb5,0xbf5e3fe7,0x3f0e3ff6,0x3fd93f88,0x3ffcbeac,
0x80004000,0x40000000,0x8000c000,0xc0000000,0xbfa03fc8,0x3fbf3fab,0x3fb5bfb5,0xbfabbfbf,0xbffa3ee4,0x3e653ffe,0x40000000,0x3e65bffe,0xbfe7bf5e,0xbf943fd1,0x3fb53fb5,0x3fd1bf94,
0xbf5ebfe7,0xbffa3ee4,0x80004000,0x3ffa3ee4,0x3ee4bffa,0xbfe0bf78,0xbfb53fb5,0x3f783fe0,0x3fc8bfa0,0xbf29bff2,0xc0000000,0xbf293ff2,0x40000000,0x3f44bfed,0xbfb5bfb5,0xbfed3f44,
0x3fc83fa0,0x3fe7bf5e,0x8000c000,0xbfe7bf5e,0x3ee43ffa,0x3ff63f0e,0x3fb5bfb5,0xbf0ebff6,0xbf5e3fe7,0x3f883fd9,0x40000000,0x3f88bfd9,0xbfe73f5e,0xbeac3ffc,0x3fb53fb5,0x3ffcbeac,
0xbffabee4,0xbfc83fa0,0x80004000,0x3fc83fa0,0xbfa0bfc8,0xc000bde6,0xbfb53fb5,0x3de64000,0x8000c000,0xbfb5bfb5,0xc0000000,0xbfb53fb5,0x3fa0bfc8,0xbde6c000,0xbfb5bfb5,0xc000bde6,
0x80004000,0x40000000,0x8000c000,0xc0000000,0xbfc83fa0,0x3f943fd1,0x3fd9bf88,0xbf78bfe0,0xbffabee4,0xbf293ff2,0x3fe73f5e,0x3f88bfd9,0xbf5ebfe7,0xbff63f0e,0x3e653ffe,0x40003de6,
0x3f5ebfe7,0xbfc8bfa0,0xbfc83fa0,0x3f5e3fe7,0x3ffabee4,0x3de6c000,0xbff2bf29,0xbf943fd1,0x3fc83fa0,0x3fd9bf88,0xbee4bffa,0xbffebe65,0x80004000,0x3fed3f44,0x3fb5bfb5,0xbf44bfed,
0xbfc83fa0,0x3ee43ffa,0x3ffa3ee4,0x3fa0bfc8,0xbffabee4,0xbfab3fbf,0x3f293ff2,0x3ffc3eac,0xbf5ebfe7,0xbffebe65,0xbfa03fc8,0x3f293ff2,0x3f5ebfe7,0xbf78bfe0,0xbffebe65,0xbfab3fbf,
0x3ffabee4,0x3f5ebfe7,0xbf5ebfe7,0xbffabee4,0x3fc83fa0,0x3ffcbeac,0x3f88bfd9,0xbf0ebff6,0x80004000,0x3fb53fb5,0x40000000,0x3fb5bfb5,0xbfc83fa0,0xbeac3ffc,0x3f883fd9,0x3ff63f0e,
0x80004000,0x40000000,0x8000c000,0xc0000000,0xbfe73f5e,0x3f443fed,0x3ff2bf29,0xbf0ebff6,0xbfc8bfa0,0xbfb53fb5,0x3fa03fc8,0x3fd9bf88,0x3ee4bffa,0xbfedbf44,0xbf883fd9,0x3fbf3fab,
0x3ffabee4,0x8000c000,0xbffabee4,0xbf5e3fe7,0x3fa03fc8,0x3fedbf44,0xbe65bffe,0xbffcbeac,0xbf5e3fe7,0x3fb53fb5,0x3fe7bf5e,0xbe65bffe,0xc0000000,0xbf443fed,0x3fb53fb5,0x3fedbf44,
0xbf5ebfe7,0xc0000000,0xbf5e3fe7,0x3fa03fc8,0x3fa0bfc8,0xbf44bfed,0xbffebe65,0xbf943fd1,0x3ffa3ee4,0x3fb5bfb5,0xbee4bffa,0xbff2bf29,0x3ee43ffa,0x3fed3f44,0x3fd9bf88,0x3de6c000,
0xbfc83fa0,0x80004000,0x3fc83fa0,0x3ffabee4,0xbfe7bf5e,0xbfed3f44,0xbf293ff2,0x3f783fe0,0x8000c000,0xbfb5bfb5,0xc0000000,0xbfb53fb5,0x3fe7bf5e,0x3f44bfed,0xbf29bff2,0xbfe0bf78,
0x80004000,0x40000000,0x8000c000,0xc0000000,0xbffa3ee4,0x3eac3ffc,0x3ffebe65,0xbde6c000,0xbf5ebfe7,0xbff23f29,0x3ee43ffa,0x3ffebe65,0x3fc8bfa0,0xbf78bfe0,0xbff23f29,0x3eac3ffc,
0x3fc83fa0,0x3fc8bfa0,0xbf5ebfe7,0xbffa3ee4,0xbf5e3fe7,0x3fbf3fab,0x3fd9bf88,0xbf0ebff6,0xbffabee4,0xbf883fd9,0x3fa03fc8,0x3ff2bf29,0x8000c000,0xbfedbf44,0xbfb53fb5,0x3f443fed,
0x3ffabee4,0x3ee4bffa,0xbfc8bfa0,0xbfe73f5e,0x3f5e3fe7,0x40003de6,0x3f88bfd9,0xbf78bfe0,0xbfc83fa0,0x3e653ffe,0x3fe73f5e,0x3fd9bf88,0xbfc8bfa0,0xbff63f0e,0xbf293ff2,0x3f943fd1,
0x3f5ebfe7,0xbf5ebfe7,0xbffabee4,0xbfc83fa0,0x3ffa3ee4,0x3fd1bf94,0x3e65bffe,0xbfabbfbf,0x80004000,0x3fb53fb5,0x40000000,0x3fb5bfb5,0xbffa3ee4,0xbf943fd1,0x3e653ffe,0x3fbf3fab,
0x80004000,0x40000000,0x8000c000,0xc0000000,0xc0000000,0xbde64000,0x3ffe3e65,0x3eacbffc,0x8000c000,0xbffebe65,0xbee43ffa,0x3ff23f29,0x40000000,0x3eacbffc,0xbff2bf29,0xbf783fe0,
0x80004000,0x3ffa3ee4,0x3f5ebfe7,0xbfc8bfa0,0xc0000000,0xbf0e3ff6,0x3fd93f88,0x3fbfbfab,0x8000c000,0xbff2bf29,0xbfa03fc8,0x3f883fd9,0x40000000,0x3f44bfed,0xbfb5bfb5,0xbfed3f44,
0x80004000,0x3fe73f5e,0x3fc8bfa0,0xbee4bffa,0xc0000000,0xbf783fe0,0x3f883fd9,0x4000bde6,0x8000c000,0xbfd9bf88,0xbfe73f5e,0xbe653ffe,0x40000000,0x3f94bfd1,0xbf29bff2,0xbff6bf0e,
0x80004000,0x3fc83fa0,0x3ffabee4,0x3f5ebfe7,0xc0000000,0xbfab3fbf,0x3e653ffe,0x3fd13f94,0x8000c000,0xbfb5bfb5,0xc0000000,0xbfb53fb5,0x40000000,0x3fbfbfab,0x3e65bffe,0xbf94bfd1,
0x80004000,0x40000000,0x8000c000,0xc0000000,0xbffabee4,0xbf0e3ff6,0x3ff23f29,0x3f44bfed,0x3f5ebfe7,0xbfd9bf88,0xbfa03fc8,0x3fb53fb5,0x3fc83fa0,0x3fbfbfab,0xbf88bfd9,0xbfed3f44,
0xbfc83fa0,0x3f5e3fe7,0x3ffabee4,0x8000c000,0xbf5ebfe7,0xbffc3eac,0xbe653ffe,0x3fed3f44,0x3ffabee4,0x3e65bffe,0xbfe7bf5e,0xbfb53fb5,0x80004000,0x3fed3f44,0x3fb5bfb5,0xbf44bfed,
0xbffabee4,0xbfa03fc8,0x3f5e3fe7,0x40000000,0x3f5ebfe7,0xbf94bfd1,0xbffe3e65,0xbf443fed,0x3fc83fa0,0x3ff2bf29,0x3ee4bffa,0xbfb5bfb5,0xbfc83fa0,0x3de64000,0x3fd93f88,0x3fedbf44,
0xbf5ebfe7,0xbffabee4,0xbfc83fa0,0x80004000,0x3ffabee4,0x3f78bfe0,0xbf29bff2,0xbfedbf44,0x80004000,0x3fb53fb5,0x40000000,0x3fb5bfb5,0xbffabee4,0xbfe03f78,0xbf293ff2,0x3f443fed,
0x80004000,0x40000000,0x8000c000,0xc0000000,0xbfe7bf5e,0xbf783fe0,0x3fd93f88,0x3f94bfd1,0x3fc8bfa0,0xbf88bfd9,0xbfe73f5e,0x3f293ff2,0x3ee43ffa,0x4000bde6,0x3e65bffe,0xbff6bf0e,
0xbffabee4,0xbf5e3fe7,0x3fc83fa0,0x3fc8bfa0,0x3fa0bfc8,0xbf94bfd1,0xbff23f29,0x3de64000,0x3f5e3fe7,0x3ffebe65,0x3ee4bffa,0xbfd9bf88,0xc0000000,0xbf443fed,0x3fb53fb5,0x3fedbf44,
0x3f5ebfe7,0xbfa0bfc8,0xbffa3ee4,0xbee43ffa,0x3fa03fc8,0x3ffcbeac,0x3f29bff2,0xbfabbfbf,0xbffa3ee4,0xbf293ff2,0x3fa03fc8,0x3ffebe65,0x3ee4bffa,0xbfabbfbf,0xbffe3e65,0xbf783fe0,
0x3fc83fa0,0x3ffabee4,0x3f5ebfe7,0xbf5ebfe7,0xbfe73f5e,0xbf0e3ff6,0x3f883fd9,0x3ffc3eac,0x8000c000,0xbfb5bfb5,0xc0000000,0xbfb53fb5,0x3fe73f5e,0x3ff6bf0e,0x3f88bfd9,0xbeacbffc,
0x80004000,0x40000000,0x8000c000,0xc0000000,0xbfc8bfa0,0xbfab3fbf,0x3fb53fb5,0x3fbfbfab,0x3ffabee4,0xbe65bffe,0xc0000000,0xbe653ffe,0xbf5e3fe7,0x3fd13f94,0x3fb5bfb5,0xbf94bfd1,
0xbf5ebfe7,0xbffa3ee4,0x80004000,0x3ffa3ee4,0x3ffa3ee4,0x3f78bfe0,0xbfb5bfb5,0xbfe03f78,0xbfc83fa0,0x3f293ff2,0x40000000,0x3f29bff2,0x8000c000,0xbfedbf44,0xbfb53fb5,0x3f443fed,
0x3fc83fa0,0x3fe7bf5e,0x8000c000,0xbfe7bf5e,0xbffa3ee4,0xbf0e3ff6,0x3fb53fb5,0x3ff6bf0e,0x3f5ebfe7,0xbf88bfd9,0xc0000000,0xbf883fd9,0x3f5e3fe7,0x3ffc3eac,0x3fb5bfb5,0xbeacbffc,
0xbffabee4,0xbfc83fa0,0x80004000,0x3fc83fa0,0x3fc8bfa0,0x3de6c000,0xbfb5bfb5,0xc0003de6,0x80004000,0x3fb53fb5,0x40000000,0x3fb5bfb5,0xbfc8bfa0,0xc0003de6,0xbfb53fb5,0xbde64000,
0x80004000,0x40000000,0x8000c000,0xc0000000,0xbfa0bfc8,0xbfd13f94,0x3f883fd9,0x3fe0bf78,0x3ffa3ee4,0x3f29bff2,0xbfe7bf5e,0xbf883fd9,0xbfe73f5e,0x3f0e3ff6,0x3ffebe65,0x3de6c000,
0x3f5ebfe7,0xbfc8bfa0,0xbfc83fa0,0x3f5e3fe7,0x3ee43ffa,0x40003de6,0x3f29bff2,0xbfd1bf94,0xbfc8bfa0,0xbfd93f88,0x3ee43ffa,0x3ffe3e65,0x40000000,0x3f44bfed,0xbfb5bfb5,0xbfed3f44,
0xbfc83fa0,0x3ee43ffa,0x3ffa3ee4,0x3fa0bfc8,0x3ee4bffa,0xbfbfbfab,0xbff23f29,0xbeac3ffc,0x3f5e3fe7,0x3ffe3e65,0x3fa0bfc8,0xbf29bff2,0xbfe7bf5e,0xbfe03f78,0xbe653ffe,0x3fbf3fab,
0x3ffabee4,0x3f5ebfe7,0xbf5ebfe7,0xbffabee4,0xbfa03fc8,0x3eac3ffc,0x3fd93f88,0x3ff6bf0e,0x8000c000,0xbfb5bfb5,0xc0000000,0xbfb53fb5,0x3fa03fc8,0x3ffc3eac,0x3fd9bf88,0x3f0ebff6,
0x80004000,0x40000000,0x8000c000,0xc0000000,0xbf5ebfe7,0xbfed3f44,0x3f293ff2,0x3ff6bf0e,0x3fc83fa0,0x3fb5bfb5,0xbfa0bfc8,0xbfd93f88,0xbffabee4,0xbf443fed,0x3fd93f88,0x3fabbfbf,
0x3ffabee4,0x8000c000,0xbffabee4,0xbf5e3fe7,0xbfc83fa0,0x3f443fed,0x3ffebe65,0x3eacbffc,0x3f5ebfe7,0xbfb5bfb5,0xbfe73f5e,0x3e653ffe,0x80004000,0x3fed3f44,0x3fb5bfb5,0xbf44bfed,
0xbf5ebfe7,0xc0000000,0xbf5e3fe7,0x3fa03fc8,0x3fc83fa0,0x3fedbf44,0x3e65bffe,0xbfd1bf94,0xbffabee4,0xbfb53fb5,0x3ee43ffa,0x3ff23f29,0x3ffabee4,0x3f44bfed,0xbf88bfd9,0xc000bde6,
0xbfc83fa0,0x80004000,0x3fc83fa0,0x3ffabee4,0x3f5ebfe7,0xbf44bfed,0xbff2bf29,0xbfe03f78,0x80004000,0x3fb53fb5,0x40000000,0x3fb5bfb5,0xbf5ebfe7,0xbfedbf44,0xbff23f29,0xbf783fe0,
0x80004000,0x40000000,0x8000c000,0xc0000000,0xbee4bffa,0xbffc3eac,0x3e653ffe,0x4000bde6,0x3f5e3fe7,0x3ff2bf29,0xbee4bffa,0xbffe3e65,0xbfa0bfc8,0xbfe03f78,0x3f293ff2,0x3ffcbeac,
0x3fc83fa0,0x3fc8bfa0,0xbf5ebfe7,0xbffa3ee4,0xbfe7bf5e,0xbfab3fbf,0x3f883fd9,0x3ff6bf0e,0x3ffa3ee4,0x3f88bfd9,0xbfa0bfc8,0xbff23f29,0xc0000000,0xbf443fed,0x3fb53fb5,0x3fedbf44,
0x3ffabee4,0x3ee4bffa,0xbfc8bfa0,0xbfe73f5e,0xbfe73f5e,0xbde64000,0x3fd93f88,0x3fe0bf78,0x3fc8bfa0,0xbe65bffe,0xbfe7bf5e,0xbfd93f88,0xbfa03fc8,0x3f0e3ff6,0x3ff23f29,0x3fd1bf94,
0x3f5ebfe7,0xbf5ebfe7,0xbffabee4,0xbfc83fa0,0xbee43ffa,0x3f943fd1,0x3ffe3e65,0x3fbfbfab,0x8000c000,0xbfb5bfb5,0xc0000000,0xbfb53fb5,0x3ee43ffa,0x3fd13f94,0x3ffebe65,0x3fabbfbf,
0x3f80,0,0,0,0xbf80,0,0,0,0x3f80,0,0,0,0xbf80,0,0,0,
0x3f80,0,0,0,0xbf80,0,0,0,0x3f80,0,0,0,0xbf80,0,0,0,
0x3f80,0,0,0,0xbf80,0,0,0,0x3f80,0,0,0,0xbf80,0,0,0,
0x3f80,0,0,0,0xbf80,0,0,0,0x3f80,0,0,0,0xbf80,0,0,0,
0,0,0,0,0,0,0,0,0,0,0,0,0,0,0,0,
0,0,0,0,0,0,0,0,0,0,0,0,0,0,0,0,
0,0,0,0,0,0,0,0,0,0,0,0,0,0,0,0,
0,0,0,0,0,0,0,0,0,0,0,0,0,0,0,0,
0x3f80,0x3fa03fc8,0x3ffa3ee4,0x3fe7bf5e,0x3f80,0x3f943fd1,0x3ff23f29,0x3ff6bf0e,0x3f80,0x3f883fd9,0x3fe73f5e,0x3ffebe65,0x3f80,0x3f783fe0,0x3fd93f88,0x40003de6,
0x3f80,0x3f5e3fe7,0x3fc83fa0,0x3ffa3ee4,0x3f80,0x3f443fed,0x3fb53fb5,0x3fed3f44,0x3f80,0x3f293ff2,0x3fa03fc8,0x3fd93f88,0x3f80,0x3f0e3ff6,0x3f883fd9,0x3fbf3fab,
0x3f80,0x3ee43ffa,0x3f5e3fe7,0x3fa03fc8,0x3f80,0x3eac3ffc,0x3f293ff2,0x3f783fe0,0x3f80,0x3e653ffe,0x3ee43ffa,0x3f293ff2,0x3f80,0x3de64000,0x3e653ffe,0x3eac3ffc,
0,0,0,0,0,0,0,0,0,0,0,0,0,0,0,0,
0x3f5ebfe7,0xbee4bffa,0xbfc8bfa0,0xc0000000,0x3fa0bfc8,0x3de6c000,0xbf88bfd9,0xbfedbf44,0x3fc8bfa0,0x3f29bff2,0xbee4bffa,0xbfb5bfb5,0x3fe7bf5e,0x3f94bfd1,0x3e65bffe,0xbf44bfed,
0x3ffabee4,0x3fc8bfa0,0x3f5ebfe7,0x8000c000,0x40000000,0x3fedbf44,0x3fb5bfb5,0x3f44bfed,0x3ffa3ee4,0x3ffebe65,0x3fe7bf5e,0x3fb5bfb5,0x3fe73f5e,0x3ffc3eac,0x3ffebe65,0x3fedbf44,
0x3fc83fa0,0x3fe73f5e,0x3ffa3ee4,0x40000000,0x3fa03fc8,0x3fbf3fab,0x3fd93f88,0x3fed3f44,0x3f5e3fe7,0x3f883fd9,0x3fa03fc8,0x3fb53fb5,0x3ee43ffa,0x3f0e3ff6,0x3f293ff2,0x3f443fed,
0,0,0,0,0,0,0,0,0,0,0,0,0,0,0,0,
0xbfc83fa0,0xbee43ffa,0x3f5e3fe7,0x3fe73f5e,0xbffa3ee4,0xbfab3fbf,0xbe653ffe,0x3f783fe0,0xbffabee4,0xbff23f29,0xbfa03fc8,0xbe653ffe,0xbfc8bfa0,0xbffcbeac,0xbff23f29,0xbfab3fbf,
0xbf5ebfe7,0xbfc8bfa0,0xbffabee4,0xbffa3ee4,0x8000c000,0xbf44bfed,0xbfb5bfb5,0xbfedbf44,0x3f5ebfe7,0x3e65bffe,0xbee4bffa,0xbf88bfd9,0x3fc8bfa0,0x3f94bfd1,0x3f29bff2,0x3de6c000,
0x3ffabee4,0x3fe7bf5e,0x3fc8bfa0,0x3fa0bfc8,0x3ffa3ee4,0x40003de6,0x3ffebe65,0x3ff6bf0e,0x3fc83fa0,0x3fd93f88,0x3fe73f5e,0x3ff23f29,0x3f5e3fe7,0x3f783fe0,0x3f883fd9,0x3f943fd1,
0,0,0,0,0,0,0,0,0,0,0,0,0,0,0,0,
0x3ffabee4,0x3fa0bfc8,0x8000c000,0xbfa0bfc8,0x3fe73f5e,0x3ffcbeac,0x3fb5bfb5,0x3eacbffc,0x3f5e3fe7,0x3fd93f88,0x40000000,0x3fd9bf88,0xbee43ffa,0x3f0e3ff6,0x3fb53fb5,0x3ff63f0e,
0xbfc83fa0,0xbf5e3fe7,0x80004000,0x3f5e3fe7,0xc0000000,0xbfed3f44,0xbfb53fb5,0xbf443fed,0xbfc8bfa0,0xbff2bf29,0xc0000000,0xbff23f29,0xbee4bffa,0xbf78bfe0,0xbfb5bfb5,0xbfe0bf78,
0x3f5ebfe7,0x3ee4bffa,0x8000c000,0xbee4bffa,0x3fe7bf5e,0x3fd1bf94,0x3fb5bfb5,0x3f94bfd1,0x3ffa3ee4,0x3ffe3e65,0x40000000,0x3ffebe65,0x3fa03fc8,0x3fab3fbf,0x3fb53fb5,0x3fbf3fab,
0,0,0,0,0,0,0,0,0,0,0,0,0,0,0,0,
0xbffabee4,0xbfe73f5e,0xbf5e3fe7,0x3ee43ffa,0xbf5ebfe7,0xbfe0bf78,0xbffe3e65,0xbfbf3fab,0x3f5ebfe7,0xbe65bffe,0xbfa0bfc8,0xbff2bf29,0x3ffabee4,0x3fbfbfab,0x3f29bff2,0xbeacbffc,
0x3fc83fa0,0x3ffa3ee4,0x3ffabee4,0x3fc8bfa0,0x80004000,0x3f443fed,0x3fb53fb5,0x3fed3f44,0xbfc83fa0,0xbf883fd9,0xbee43ffa,0x3e653ffe,0xbffabee4,0xc0003de6,0xbff23f29,0xbfd13f94,
0xbf5ebfe7,0xbfa0bfc8,0xbfc8bfa0,0xbfe7bf5e,0x3f5ebfe7,0x3f0ebff6,0x3e65bffe,0xbde6c000,0x3ffabee4,0x3ff2bf29,0x3fe7bf5e,0x3fd9bf88,0x3fc83fa0,0x3fd13f94,0x3fd93f88,0x3fe03f78,
0,0,0,0,0,0,0,0,0,0,0,0,0,0,0,0,
0x3fc83fa0,0x40000000,0x3fc8bfa0,0x3ee4bffa,0xbee43ffa,0x3f443fed,0x3fd93f88,0x4000bde6,0xbffa3ee4,0xbfb53fb5,0xbee43ffa,0x3f293ff2,0xbfa0bfc8,0xbfedbf44,0xbffe3e65,0xbfd13f94,
0x3f5ebfe7,0x8000c000,0xbf5ebfe7,0xbfc8bfa0,0x40000000,0x3fedbf44,0x3fb5bfb5,0x3f44bfed,0x3f5e3fe7,0x3fb53fb5,0x3fe73f5e,0x3ffe3e65,0xbfa03fc8,0xbf443fed,0xbe653ffe,0x3eac3ffc,
0xbffabee4,0xc0000000,0xbffa3ee4,0xbfe73f5e,0xbee4bffa,0xbf44bfed,0xbf88bfd9,0xbfabbfbf,0x3fc8bfa0,0x3fb5bfb5,0x3fa0bfc8,0x3f88bfd9,0x3fe73f5e,0x3fed3f44,0x3ff23f29,0x3ff63f0e,
0,0,0,0,0,0,0,0,0,0,0,0,0,0,0,0,
0xbf5ebfe7,0xbfe7bf5e,0xbffa3ee4,0xbfa03fc8,0x3fc8bfa0,0x3f0ebff6,0xbf29bff2,0xbfd1bf94,0x3fc83fa0,0x3ffe3e65,0x3fe7bf5e,0x3f88bfd9,0xbf5e3fe7,0x3de64000,0x3f883fd9,0x3fe03f78,
0xbffabee4,0xbffa3ee4,0xbfc83fa0,0xbf5e3fe7,0x8000c000,0xbf44bfed,0xbfb5bfb5,0xbfedbf44,0x3ffabee4,0x3fd9bf88,0x3fa0bfc8,0x3f29bff2,0x3f5e3fe7,0x3fab3fbf,0x3fd93f88,0x3ff63f0e,
0xbfc83fa0,0xbfa03fc8,0xbf5e3fe7,0xbee43ffa,0xbfc8bfa0,0xbfe0bf78,0xbff2bf29,0xbffcbeac,0x3f5ebfe7,0x3f29bff2,0x3ee4bffa,0x3e65bffe,0x3ffa3ee4,0x3ffc3eac,0x3ffe3e65,0x40003de6,
0,0,0,0,0,0,0,0,0,0,0,0,0,0,0,0,
0x80004000,0x3fa03fc8,0x3ffa3ee4,0x3fe7bf5e,0xc0000000,0xbfd13f94,0xbf293ff2,0x3f0e3ff6,0x8000c000,0xbf88bfd9,0xbfe7bf5e,0xbffe3e65,0x40000000,0x3fe0bf78,0x3f88bfd9,0x3de6c000,
0x80004000,0x3f5e3fe7,0x3fc83fa0,0x3ffa3ee4,0xc0000000,0xbfed3f44,0xbfb53fb5,0xbf443fed,0x8000c000,0xbf29bff2,0xbfa0bfc8,0xbfd9bf88,0x40000000,0x3ff6bf0e,0x3fd9bf88,0x3fabbfbf,
0x80004000,0x3ee43ffa,0x3f5e3fe7,0x3fa03fc8,0xc0000000,0xbffc3eac,0xbff23f29,0xbfe03f78,0x8000c000,0xbe65bffe,0xbee4bffa,0xbf29bff2,0x40000000,0x4000bde6,0x3ffebe65,0x3ffcbeac,
0,0,0,0,0,0,0,0,0,0,0,0,0,0,0,0,
0x3f5ebfe7,0xbee4bffa,0xbfc8bfa0,0xc0000000,0x3fc83fa0,0x40003de6,0x3fd9bf88,0x3f44bfed,0xbfc83fa0,0xbf293ff2,0x3ee43ffa,0x3fb53fb5,0xbf5ebfe7,0xbfd1bf94,0xbffebe65,0xbfed3f44,
0x3ffabee4,0x3fc8bfa0,0x3f5ebfe7,0x8000c000,0x80004000,0x3f443fed,0x3fb53fb5,0x3fed3f44,0xbffabee4,0xbffe3e65,0xbfe73f5e,0xbfb53fb5,0x3f5ebfe7,0x3eacbffc,0xbe65bffe,0xbf44bfed,
0x3fc83fa0,0x3fe73f5e,0x3ffa3ee4,0x40000000,0xbfc83fa0,0xbfab3fbf,0xbf883fd9,0xbf443fed,0xbf5ebfe7,0xbf88bfd9,0xbfa0bfc8,0xbfb5bfb5,0x3ffabee4,0x3ff6bf0e,0x3ff2bf29,0x3fedbf44,
0,0,0,0,0,0,0,0,0,0,0,0,0,0,0,0,
0xbfc83fa0,0xbee43ffa,0x3f5e3fe7,0x3fe73f5e,0xbee4bffa,0xbfbfbfab,0xbffebe65,0xbfe03f78,0x3ffa3ee4,0x3ff2bf29,0x3fa0bfc8,0x3e65bffe,0xbfa03fc8,0xbeac3ffc,0x3f293ff2,0x3fbf3fab,
0xbf5ebfe7,0xbfc8bfa0,0xbffabee4,0xbffa3ee4,0x40000000,0x3fedbf44,0x3fb5bfb5,0x3f44bfed,0xbf5e3fe7,0xbe653ffe,0x3ee43ffa,0x3f883fd9,0xbfa0bfc8,0xbfd1bf94,0xbff2bf29,0xc000bde6,
0x3ffabee4,0x3fe7bf5e,0x3fc8bfa0,0x3fa0bfc8,0xbee43ffa,0xbde64000,0x3e653ffe,0x3f0e3ff6,0xbfc8bfa0,0xbfd9bf88,0xbfe7bf5e,0xbff2bf29,0x3fe7bf5e,0x3fe0bf78,0x3fd9bf88,0x3fd1bf94,
0,0,0,0,0,0,0,0,0,0,0,0,0,0,0,0,
0x3ffabee4,0x3fa0bfc8,0x8000c000,0xbfa0bfc8,0xbf5e3fe7,0x3eac3ffc,0x3fb53fb5,0x3ffc3eac,0xbf5ebfe7,0xbfd9bf88,0xc0000000,0xbfd93f88,0x3ffa3ee4,0x3ff6bf0e,0x3fb5bfb5,0x3f0ebff6,
0xbfc83fa0,0xbf5e3fe7,0x80004000,0x3f5e3fe7,0x8000c000,0xbf44bfed,0xbfb5bfb5,0xbfedbf44,0x3fc83fa0,0x3ff23f29,0x40000000,0x3ff2bf29,0xbffa3ee4,0xbfe03f78,0xbfb53fb5,0xbf783fe0,
0x3f5ebfe7,0x3ee4bffa,0x8000c000,0xbee4bffa,0x3f5e3fe7,0x3f943fd1,0x3fb53fb5,0x3fd13f94,0xbffabee4,0xbffebe65,0xc0000000,0xbffe3e65,0x3fc8bfa0,0x3fbfbfab,0x3fb5bfb5,0x3fabbfbf,
0,0,0,0,0,0,0,0,0,0,0,0,0,0,0,0,
0xbffabee4,0xbfe73f5e,0xbf5e3fe7,0x3ee43ffa,0x3fe7bf5e,0x3f78bfe0,0xbe65bffe,0xbfabbfbf,0xbf5e3fe7,0x3e653ffe,0x3fa03fc8,0x3ff23f29,0xbee4bffa,0xbfabbfbf,0xbff2bf29,0xbffc3eac,
0x3fc83fa0,0x3ffa3ee4,0x3ffabee4,0x3fc8bfa0,0xc0000000,0xbfed3f44,0xbfb53fb5,0xbf443fed,0x3fc8bfa0,0x3f88bfd9,0x3ee4bffa,0xbe65bffe,0xbee43ffa,0x3de64000,0x3f293ff2,0x3f943fd1,
0xbf5ebfe7,0xbfa0bfc8,0xbfc8bfa0,0xbfe7bf5e,0x3fe73f5e,0x3ff63f0e,0x3ffe3e65,0x4000bde6,0xbffa3ee4,0xbff23f29,0xbfe73f5e,0xbfd93f88,0x3fa0bfc8,0x3f94bfd1,0x3f88bfd9,0x3f78bfe0,
0,0,0,0,0,0,0,0,0,0,0,0,0,0,0,0,
0x3fc83fa0,0x40000000,0x3fc8bfa0,0x3ee4bffa,0xbffabee4,0xbfed3f44,0xbf883fd9,0x3de64000,0x3ffabee4,0x3fb5bfb5,0x3ee4bffa,0xbf29bff2,0xbfc83fa0,0xbf443fed,0x3e653ffe,0x3f943fd1,
0x3f5ebfe7,0x8000c000,0xbf5ebfe7,0xbfc8bfa0,0x80004000,0x3f443fed,0x3fb53fb5,0x3fed3f44,0xbf5ebfe7,0xbfb5bfb5,0xbfe7bf5e,0xbffebe65,0x3fc83fa0,0x3fed3f44,0x3ffe3e65,0x3ffcbeac,
0xbffabee4,0xc0000000,0xbffa3ee4,0xbfe73f5e,0x3ffabee4,0x3fedbf44,0x3fd9bf88,0x3fbfbfab,0xbfc83fa0,0xbfb53fb5,0xbfa03fc8,0xbf883fd9,0x3f5ebfe7,0x3f44bfed,0x3f29bff2,0x3f0ebff6,
0,0,0,0,0,0,0,0,0,0,0,0,0,0,0,0,
0xbf5ebfe7,0xbfe7bf5e,0xbffa3ee4,0xbfa03fc8,0x3fa03fc8,0x3ff63f0e,0x3ff2bf29,0x3f94bfd1,0xbfc8bfa0,0xbffebe65,0xbfe73f5e,0xbf883fd9,0x3fe73f5e,0x4000bde6,0x3fd9bf88,0x3f78bfe0,
0xbffabee4,0xbffa3ee4,0xbfc83fa0,0xbf5e3fe7,0x40000000,0x3fedbf44,0x3fb5bfb5,0x3f44bfed,0xbffa3ee4,0xbfd93f88,0xbfa03fc8,0xbf293ff2,0x3fe7bf5e,0x3fbfbfab,0x3f88bfd9,0x3f0ebff6,
0xbfc83fa0,0xbfa03fc8,0xbf5e3fe7,0xbee43ffa,0x3fa0bfc8,0x3f78bfe0,0x3f29bff2,0x3eacbffc,0xbf5e3fe7,0xbf293ff2,0xbee43ffa,0xbe653ffe,0x3ee4bffa,0x3eacbffc,0x3e65bffe,0x3de6c000,
0,0,0,0,0,0,0,0,0,0,0,0,0,0,0,0,
0x3f80,0,0,0,0xbf80,0,0,0,0x3f80,0,0,0,0xbf80,0,0,0,
0x3f80,0,0,0,0xbf80,0,0,0,0x3f80,0,0,0,0xbf80,0,0,0,
0x3f80,0,0,0,0xbf80,0,0,0,0x3f80,0,0,0,0xbf80,0,0,0,
0,0,0,0,0,0,0,0,0,0,0,0,0,0,0,0,
0,0,0,0,0,0,0,0,0,0,0,0,0,0,0,0,
0,0,0,0,0,0,0,0,0,0,0,0,0,0,0,0,
0,0,0,0,0,0,0,0,0,0,0,0,0,0,0,0,
0,0,0,0,0,0,0,0,0,0,0,0,0,0,0,0,
0x3f80,0x3f80,0,0,0x3f80,0xbf80,0,0,0x3f80,0x3f80,0,0,0x3f80,0xbf80,0,0,
0x3f80,0x3f80,0,0,0x3f80,0xbf80,0,0,0x3f80,0x3f80,0,0,0x3f80,0xbf80,0,0,
0,0,0x3f80,0x3f80,0,0,0x3f80,0xbf80,0,0,0x3f80,0x3f80,0,0,0x3f80,0xbf80,
0,0,0x3f80,0x3f80,0,0,0x3f80,0xbf80,0,0,0x3f80,0x3f80,0,0,0x3f80,0xbf80,
0x3f80,0x3f80,0,0,0x3f353f35,0xbf35bf35,0,0,0x3f800000,0x3f800000,0,0,0x3f35bf35,0xbf353f35,0,0,
0xbf80,0xbf80,0,0,0xbf35bf35,0x3f353f35,0,0,0xbf800000,0xbf800000,0,0,0xbf353f35,0x3f35bf35,0,0,
0,0,0x3f80,0x3f80,0,0,0x3f353f35,0xbf35bf35,0,0,0x3f800000,0x3f800000,0,0,0x3f35bf35,0xbf353f35,
0,0,0xbf80,0xbf80,0,0,0xbf35bf35,0x3f353f35,0,0,0xbf800000,0xbf800000,0,0,0xbf353f35,0x3f35bf35,
0x3f80,0x3f80,0,0,0x3f800000,0xbf800000,0,0,0xbf80,0xbf80,0,0,0xbf800000,0x3f800000,0,0,
0x3f80,0x3f80,0,0,0x3f800000,0xbf800000,0,0,0xbf80,0xbf80,0,0,0xbf800000,0x3f800000,0,0,
0,0,0x3f80,0x3f80,0,0,0x3f800000,0xbf800000,0,0,0xbf80,0xbf80,0,0,0xbf800000,0x3f800000,
0,0,0x3f80,0x3f80,0,0,0x3f800000,0xbf800000,0,0,0xbf80,0xbf80,0,0,0xbf800000,0x3f800000,
0x3f80,0x3f80,0,0,0x3f35bf35,0xbf353f35,0,0,0xbf800000,0xbf800000,0,0,0x3f353f35,0xbf35bf35,0,0,
0xbf80,0xbf80,0,0,0xbf353f35,0x3f35bf35,0,0,0x3f800000,0x3f800000,0,0,0xbf35bf35,0x3f353f35,0,0,
0,0,0x3f80,0x3f80,0,0,0x3f35bf35,0xbf353f35,0,0,0xbf800000,0xbf800000,0,0,0x3f353f35,0xbf35bf35,
0,0,0xbf80,0xbf80,0,0,0xbf353f35,0x3f35bf35,0,0,0x3f800000,0x3f800000,0,0,0xbf35bf35,0x3f353f35,
0x3f808000,0x3f808000,0,0,0x3f808000,0xbf808000,0,0,0x3f808000,0x3f808000,0,0,0x3f808000,0xbf808000,0,0,
0x3f808000,0x3f808000,0,0,0x3f808000,0xbf808000,0,0,0x3f808000,0x3f808000,0,0,0x3f808000,0xbf808000,0,0,
0,0,0x3f808000,0x3f808000,0,0,0x3f808000,0xbf808000,0,0,0x3f808000,0x3f808000,0,0,0x3f808000,0xbf808000,
0,0,0x3f808000,0x3f808000,0,0,0x3f808000,0xbf808000,0,0,0x3f808000,0x3f808000,0,0,0x3f808000,0xbf808000,
0x3f808000,0x3f808000,0,0,0x3f35bf35,0xbf353f35,0,0,0xbf80,0xbf80,0,0,0xbf35bf35,0x3f353f35,0,0,
0xbf808000,0xbf808000,0,0,0xbf353f35,0x3f35bf35,0,0,0x3f80,0x3f80,0,0,0x3f353f35,0xbf35bf35,0,0,
0,0,0x3f808000,0x3f808000,0,0,0x3f35bf35,0xbf353f35,0,0,0xbf80,0xbf80,0,0,0xbf35bf35,0x3f353f35,
0,0,0xbf808000,0xbf808000,0,0,0xbf353f35,0x3f35bf35,0,0,0x3f80,0x3f80,0,0,0x3f353f35,0xbf35bf35,
0x3f808000,0x3f808000,0,0,0xbf80,0x3f80,0,0,0xbf808000,0xbf808000,0,0,0x3f80,0xbf80,0,0,
0x3f808000,0x3f808000,0,0,0xbf80,0x3f80,0,0,0xbf808000,0xbf808000,0,0,0x3f80,0xbf80,0,0,
0,0,0x3f808000,0x3f808000,0,0,0xbf80,0x3f80,0,0,0xbf808000,0xbf808000,0,0,0x3f80,0xbf80,
0,0,0x3f808000,0x3f808000,0,0,0xbf80,0x3f80,0,0,0xbf808000,0xbf808000,0,0,0x3f80,0xbf80,
0x3f808000,0x3f808000,0,0,0xbf35bf35,0x3f353f35,0,0,0x3f80,0x3f80,0,0,0x3f35bf35,0xbf353f35,0,0,
0xbf808000,0xbf808000,0,0,0x3f353f35,0xbf35bf35,0,0,0xbf80,0xbf80,0,0,0xbf353f35,0x3f35bf35,0,0,
0,0,0x3f808000,0x3f808000,0,0,0xbf35bf35,0x3f353f35,0,0,0x3f80,0x3f80,0,0,0x3f35bf35,0xbf353f35,
0,0,0xbf808000,0xbf808000,0,0,0x3f353f35,0xbf35bf35,0,0,0xbf80,0xbf80,0,0,0xbf353f35,0x3f35bf35,
0x80003f80,0x80003f80,0x80003f80,0x80003f80,0,0,0,0,0,0,0,0,0,0,0,0,
0x80003f80,0xbf353f35,0xbf800000,0xbf35bf35,0,0,0,0,0,0,0,0,0,0,0,0,
0x80003f80,0xbf800000,0x8000bf80,0x3f800000,0,0,0,0,0,0,0,0,0,0,0,0,
0x80003f80,0xbf35bf35,0x3f800000,0xbf353f35,0,0,0,0,0,0,0,0,0,0,0,0,
0x80003f80,0x80003f80,0x80003f80,0x80003f80,0,0,0,0,0,0,0,0,0,0,0,0,
0x8000bf80,0x3f35bf35,0x3f800000,0x3f353f35,0,0,0,0,0,0,0,0,0,0,0,0,
0x80003f80,0xbf800000,0x8000bf80,0x3f800000,0,0,0,0,0,0,0,0,0,0,0,0,
0x8000bf80,0x3f353f35,0xbf800000,0x3f35bf35,0,0,0,0,0,0,0,0,0,0,0,0,
0,0,0,0,0x80003f80,0x80003f80,0x80003f80,0x80003f80,0,0,0,0,0,0,0,0,
0,0,0,0,0x80003f80,0xbf353f35,0xbf800000,0xbf35bf35,0,0,0,0,0,0,0,0,
0,0,0,0,0x80003f80,0xbf800000,0x8000bf80,0x3f800000,0,0,0,0,0,0,0,0,
0,0,0,0,0x80003f80,0xbf35bf35,0x3f800000,0xbf353f35,0,0,0,0,0,0,0,0,
0,0,0,0,0x80003f80,0x80003f80,0x80003f80,0x80003f80,0,0,0,0,0,0,0,0,
0,0,0,0,0x8000bf80,0x3f35bf35,0x3f800000,0x3f353f35,0,0,0,0,0,0,0,0,
0,0,0,0,0x80003f80,0xbf800000,0x8000bf80,0x3f800000,0,0,0,0,0,0,0,0,
0,0,0,0,0x8000bf80,0x3f353f35,0xbf800000,0x3f35bf35,0,0,0,0,0,0,0,0,
0,0,0,0,0,0,0,0,0x80003f80,0x80003f80,0x80003f80,0x80003f80,0,0,0,0,
0,0,0,0,0,0,0,0,0x80003f80,0xbf353f35,0xbf800000,0xbf35bf35,0,0,0,0,
0,0,0,0,0,0,0,0,0x80003f80,0xbf800000,0x8000bf80,0x3f800000,0,0,0,0,
0,0,0,0,0,0,0,0,0x80003f80,0xbf35bf35,0x3f800000,0xbf353f35,0,0,0,0,
0,0,0,0,0,0,0,0,0x80003f80,0x80003f80,0x80003f80,0x80003f80,0,0,0,0,
0,0,0,0,0,0,0,0,0x8000bf80,0x3f35bf35,0x3f800000,0x3f353f35,0,0,0,0,
0,0,0,0,0,0,0,0,0x80003f80,0xbf800000,0x8000bf80,0x3f800000,0,0,0,0,
0,0,0,0,0,0,0,0,0x8000bf80,0x3f353f35,0xbf800000,0x3f35bf35,0,0,0,0,
0,0,0,0,0,0,0,0,0,0,0,0,0x80003f80,0x80003f80,0x80003f80,0x80003f80,
0,0,0,0,0,0,0,0,0,0,0,0,0x80003f80,0xbf353f35,0xbf800000,0xbf35bf35,
0,0,0,0,0,0,0,0,0,0,0,0,0x80003f80,0xbf800000,0x8000bf80,0x3f800000,
0,0,0,0,0,0,0,0,0,0,0,0,0x80003f80,0xbf35bf35,0x3f800000,0xbf353f35,
0,0,0,0,0,0,0,0,0,0,0,0,0x80003f80,0x80003f80,0x80003f80,0x80003f80,
0,0,0,0,0,0,0,0,0,0,0,0,0x8000bf80,0x3f35bf35,0x3f800000,0x3f353f35,
0,0,0,0,0,0,0,0,0,0,0,0,0x80003f80,0xbf800000,0x8000bf80,0x3f800000,
0,0,0,0,0,0,0,0,0,0,0,0,0x8000bf80,0x3f353f35,0xbf800000,0x3f35bf35,
0x3f800000,0x3f800000,0x3f800000,0x3f800000,0,0,0,0,0,0,0,0,0,0,0,0,
0x3f800000,0x3f353f35,0x3f80,0xbf353f35,0,0,0,0,0,0,0,0,0,0,0,0,
0x3f800000,0x3f80,0xbf800000,0xbf80,0,0,0,0,0,0,0,0,0,0,0,0,
0x3f800000,0xbf353f35,0xbf80,0x3f353f35,0,0,0,0,0,0,0,0,0,0,0,0,
0x3f800000,0x3f800000,0x3f800000,0x3f800000,0,0,0,0,0,0,0,0,0,0,0,0,
0xbf800000,0xbf35bf35,0xbf80,0x3f35bf35,0,0,0,0,0,0,0,0,0,0,0,0,
0x3f800000,0x3f80,0xbf800000,0xbf80,0,0,0,0,0,0,0,0,0,0,0,0,
0xbf800000,0x3f35bf35,0x3f80,0xbf35bf35,0,0,0,0,0,0,0,0,0,0,0,0,
0,0,0,0,0x3f800000,0x3f800000,0x3f800000,0x3f800000,0,0,0,0,0,0,0,0,
0,0,0,0,0x3f800000,0x3f353f35,0x3f80,0xbf353f35,0,0,0,0,0,0,0,0,
0,0,0,0,0x3f800000,0x3f80,0xbf800000,0xbf80,0,0,0,0,0,0,0,0,
0,0,0,0,0x3f800000,0xbf353f35,0xbf80,0x3f353f35,0,0,0,0,0,0,0,0,
0,0,0,0,0x3f800000,0x3f800000,0x3f800000,0x3f800000,0,0,0,0,0,0,0,0,
0,0,0,0,0xbf800000,0xbf35bf35,0xbf80,0x3f35bf35,0,0,0,0,0,0,0,0,
0,0,0,0,0x3f800000,0x3f80,0xbf800000,0xbf80,0,0,0,0,0,0,0,0,
0,0,0,0,0xbf800000,0x3f35bf35,0x3f80,0xbf35bf35,0,0,0,0,0,0,0,0,
0,0,0,0,0,0,0,0,0x3f800000,0x3f800000,0x3f800000,0x3f800000,0,0,0,0,
0,0,0,0,0,0,0,0,0x3f800000,0x3f353f35,0x3f80,0xbf353f35,0,0,0,0,
0,0,0,0,0,0,0,0,0x3f800000,0x3f80,0xbf800000,0xbf80,0,0,0,0,
0,0,0,0,0,0,0,0,0x3f800000,0xbf353f35,0xbf80,0x3f353f35,0,0,0,0,
0,0,0,0,0,0,0,0,0x3f800000,0x3f800000,0x3f800000,0x3f800000,0,0,0,0,
0,0,0,0,0,0,0,0,0xbf800000,0xbf35bf35,0xbf80,0x3f35bf35,0,0,0,0,
0,0,0,0,0,0,0,0,0x3f800000,0x3f80,0xbf800000,0xbf80,0,0,0,0,
0,0,0,0,0,0,0,0,0xbf800000,0x3f35bf35,0x3f80,0xbf35bf35,0,0,0,0,
0,0,0,0,0,0,0,0,0,0,0,0,0x3f800000,0x3f800000,0x3f800000,0x3f800000,
0,0,0,0,0,0,0,0,0,0,0,0,0x3f800000,0x3f353f35,0x3f80,0xbf353f35,
0,0,0,0,0,0,0,0,0,0,0,0,0x3f800000,0x3f80,0xbf800000,0xbf80,
0,0,0,0,0,0,0,0,0,0,0,0,0x3f800000,0xbf353f35,0xbf80,0x3f353f35,
0,0,0,0,0,0,0,0,0,0,0,0,0x3f800000,0x3f800000,0x3f800000,0x3f800000,
0,0,0,0,0,0,0,0,0,0,0,0,0xbf800000,0xbf35bf35,0xbf80,0x3f35bf35,
0,0,0,0,0,0,0,0,0,0,0,0,0x3f800000,0x3f80,0xbf800000,0xbf80,
0,0,0,0,0,0,0,0,0,0,0,0,0xbf800000,0x3f35bf35,0x3f80,0xbf35bf35,
0x80003f80,0x8000bf80,0x80003f80,0x8000bf80,0,0,0,0,0,0,0,0,0,0,0,0,
0x80003f80,0x3f35bf35,0xbf800000,0x3f353f35,0,0,0,0,0,0,0,0,0,0,0,0,
0x80003f80,0x3f800000,0x8000bf80,0xbf800000,0,0,0,0,0,0,0,0,0,0,0,0,
0x80003f80,0x3f353f35,0x3f800000,0x3f35bf35,0,0,0,0,0,0,0,0,0,0,0,0,
0x80003f80,0x8000bf80,0x80003f80,0x8000bf80,0,0,0,0,0,0,0,0,0,0,0,0,
0x8000bf80,0xbf353f35,0x3f800000,0xbf35bf35,0,0,0,0,0,0,0,0,0,0,0,0,
0x80003f80,0x3f800000,0x8000bf80,0xbf800000,0,0,0,0,0,0,0,0,0,0,0,0,
0x8000bf80,0xbf35bf35,0xbf800000,0xbf353f35,0,0,0,0,0,0,0,0,0,0,0,0,
0,0,0,0,0x80003f80,0x8000bf80,0x80003f80,0x8000bf80,0,0,0,0,0,0,0,0,
0,0,0,0,0x80003f80,0x3f35bf35,0xbf800000,0x3f353f35,0,0,0,0,0,0,0,0,
0,0,0,0,0x80003f80,0x3f800000,0x8000bf80,0xbf800000,0,0,0,0,0,0,0,0,
0,0,0,0,0x80003f80,0x3f353f35,0x3f800000,0x3f35bf35,0,0,0,0,0,0,0,0,
0,0,0,0,0x80003f80,0x8000bf80,0x80003f80,0x8000bf80,0,0,0,0,0,0,0,0,
0,0,0,0,0x8000bf80,0xbf353f35,0x3f800000,0xbf35bf35,0,0,0,0,0,0,0,0,
0,0,0,0,0x80003f80,0x3f800000,0x8000bf80,0xbf800000,0,0,0,0,0,0,0,0,
0,0,0,0,0x8000bf80,0xbf35bf35,0xbf800000,0xbf353f35,0,0,0,0,0,0,0,0,
0,0,0,0,0,0,0,0,0x80003f80,0x8000bf80,0x80003f80,0x8000bf80,0,0,0,0,
0,0,0,0,0,0,0,0,0x80003f80,0x3f35bf35,0xbf800000,0x3f353f35,0,0,0,0,
0,0,0,0,0,0,0,0,0x80003f80,0x3f800000,0x8000bf80,0xbf800000,0,0,0,0,
0,0,0,0,0,0,0,0,0x80003f80,0x3f353f35,0x3f800000,0x3f35bf35,0,0,0,0,
0,0,0,0,0,0,0,0,0x80003f80,0x8000bf80,0x80003f80,0x8000bf80,0,0,0,0,
0,0,0,0,0,0,0,0,0x8000bf80,0xbf353f35,0x3f800000,0xbf35bf35,0,0,0,0,
0,0,0,0,0,0,0,0,0x80003f80,0x3f800000,0x8000bf80,0xbf800000,0,0,0,0,
0,0,0,0,0,0,0,0,0x8000bf80,0xbf35bf35,0xbf800000,0xbf353f35,0,0,0,0,
0,0,0,0,0,0,0,0,0,0,0,0,0x80003f80,0x8000bf80,0x80003f80,0x8000bf80,
0,0,0,0,0,0,0,0,0,0,0,0,0x80003f80,0x3f35bf35,0xbf800000,0x3f353f35,
0,0,0,0,0,0,0,0,0,0,0,0,0x80003f80,0x3f800000,0x8000bf80,0xbf800000,
0,0,0,0,0,0,0,0,0,0,0,0,0x80003f80,0x3f353f35,0x3f800000,0x3f35bf35,
0,0,0,0,0,0,0,0,0,0,0,0,0x80003f80,0x8000bf80,0x80003f80,0x8000bf80,
0,0,0,0,0,0,0,0,0,0,0,0,0x8000bf80,0xbf353f35,0x3f800000,0xbf35bf35,
0,0,0,0,0,0,0,0,0,0,0,0,0x80003f80,0x3f800000,0x8000bf80,0xbf800000,
0,0,0,0,0,0,0,0,0,0,0,0,0x8000bf80,0xbf35bf35,0xbf800000,0xbf353f35,
0x3f800000,0xbf800000,0x3f800000,0xbf800000,0,0,0,0,0,0,0,0,0,0,0,0,
0x3f800000,0xbf35bf35,0x3f80,0x3f35bf35,0,0,0,0,0,0,0,0,0,0,0,0,
0x3f800000,0xbf80,0xbf800000,0x3f80,0,0,0,0,0,0,0,0,0,0,0,0,
0x3f800000,0x3f35bf35,0xbf80,0xbf35bf35,0,0,0,0,0,0,0,0,0,0,0,0,
0x3f800000,0xbf800000,0x3f800000,0xbf800000,0,0,0,0,0,0,0,0,0,0,0,0,
0xbf800000,0x3f353f35,0xbf80,0xbf353f35,0,0,0,0,0,0,0,0,0,0,0,0,
0x3f800000,0xbf80,0xbf800000,0x3f80,0,0,0,0,0,0,0,0,0,0,0,0,
0xbf800000,0xbf353f35,0x3f80,0x3f353f35,0,0,0,0,0,0,0,0,0,0,0,0,
0,0,0,0,0x3f800000,0xbf800000,0x3f800000,0xbf800000,0,0,0,0,0,0,0,0,
0,0,0,0,0x3f800000,0xbf35bf35,0x3f80,0x3f35bf35,0,0,0,0,0,0,0,0,
0,0,0,0,0x3f800000,0xbf80,0xbf800000,0x3f80,0,0,0,0,0,0,0,0,
0,0,0,0,0x3f800000,0x3f35bf35,0xbf80,0xbf35bf35,0,0,0,0,0,0,0,0,
0,0,0,0,0x3f800000,0xbf800000,0x3f800000,0xbf800000,0,0,0,0,0,0,0,0,
0,0,0,0,0xbf800000,0x3f353f35,0xbf80,0xbf353f35,0,0,0,0,0,0,0,0,
0,0,0,0,0x3f800000,0xbf80,0xbf800000,0x3f80,0,0,0,0,0,0,0,0,
0,0,0,0,0xbf800000,0xbf353f35,0x3f80,0x3f353f35,0,0,0,0,0,0,0,0,
0,0,0,0,0,0,0,0,0x3f800000,0xbf800000,0x3f800000,0xbf800000,0,0,0,0,
0,0,0,0,0,0,0,0,0x3f800000,0xbf35bf35,0x3f80,0x3f35bf35,0,0,0,0,
0,0,0,0,0,0,0,0,0x3f800000,0xbf80,0xbf800000,0x3f80,0,0,0,0,
0,0,0,0,0,0,0,0,0x3f800000,0x3f35bf35,0xbf80,0xbf35bf35,0,0,0,0,
0,0,0,0,0,0,0,0,0x3f800000,0xbf800000,0x3f800000,0xbf800000,0,0,0,0,
0,0,0,0,0,0,0,0,0xbf800000,0x3f353f35,0xbf80,0xbf353f35,0,0,0,0,
0,0,0,0,0,0,0,0,0x3f800000,0xbf80,0xbf800000,0x3f80,0,0,0,0,
0,0,0,0,0,0,0,0,0xbf800000,0xbf353f35,0x3f80,0x3f353f35,0,0,0,0,
0,0,0,0,0,0,0,0,0,0,0,0,0x3f800000,0xbf800000,0x3f800000,0xbf800000,
0,0,0,0,0,0,0,0,0,0,0,0,0x3f800000,0xbf35bf35,0x3f80,0x3f35bf35,
0,0,0,0,0,0,0,0,0,0,0,0,0x3f800000,0xbf80,0xbf800000,0x3f80,
0,0,0,0,0,0,0,0,0,0,0,0,0x3f800000,0x3f35bf35,0xbf80,0xbf35bf35,
0,0,0,0,0,0,0,0,0,0,0,0,0x3f800000,0xbf800000,0x3f800000,0xbf800000,
0,0,0,0,0,0,0,0,0,0,0,0,0xbf800000,0x3f353f35,0xbf80,0xbf353f35,
0,0,0,0,0,0,0,0,0,0,0,0,0x3f800000,0xbf80,0xbf800000,0x3f80,
0,0,0,0,0,0,0,0,0,0,0,0,0xbf800000,0xbf353f35,0x3f80,0x3f353f35,
0x3f80,0x3f80,0x3f80,0x3f80,0x3f80,0x3ec43f6d,0x3f353f35,0x3f6d3ec4,0x3f80,0x3f353f35,0x3f800000,0x3f35bf35,0x3f80,0x3f6d3ec4,0x3f35bf35,0xbec4bf6d,
0x3f80,0x3f800000,0xbf80,0xbf800000,0x3f80,0x3f6dbec4,0xbf35bf35,0xbec43f6d,0x3f80,0x3f35bf35,0xbf800000,0x3f353f35,0x3f80,0x3ec4bf6d,0xbf353f35,0x3f6dbec4,
0x3f80,0xbf80,0x3f80,0xbf80,0x3f80,0xbec4bf6d,0x3f353f35,0xbf6dbec4,0x3f80,0xbf35bf35,0x3f800000,0xbf353f35,0x3f80,0xbf6dbec4,0x3f35bf35,0x3ec43f6d,
0x3f80,0xbf800000,0xbf80,0x3f800000,0x3f80,0xbf6d3ec4,0xbf35bf35,0x3ec4bf6d,0x3f80,0xbf353f35,0xbf800000,0xbf35bf35,0x3f80,0xbec43f6d,0xbf353f35,0xbf6d3ec4,
0x3f80,0x3f80,0x3f80,0x3f80,0x3f800000,0x3f6dbec4,0x3f35bf35,0x3ec4bf6d,0xbf80,0xbf35bf35,0xbf800000,0xbf353f35,0xbf800000,0xbec43f6d,0x3f353f35,0x3f6dbec4,
0x3f80,0x3f800000,0xbf80,0xbf800000,0x3f800000,0xbec4bf6d,0xbf353f35,0x3f6d3ec4,0xbf80,0xbf353f35,0x3f800000,0xbf35bf35,0xbf800000,0x3f6d3ec4,0xbf35bf35,0x3ec43f6d,
0x3f80,0xbf80,0x3f80,0xbf80,0x3f800000,0xbf6d3ec4,0x3f35bf35,0xbec43f6d,0xbf80,0x3f353f35,0xbf800000,0x3f35bf35,0xbf800000,0x3ec4bf6d,0x3f353f35,0xbf6d3ec4,
0x3f80,0xbf800000,0xbf80,0x3f800000,0x3f800000,0x3ec43f6d,0xbf353f35,0xbf6dbec4,0xbf80,0x3f35bf35,0x3f800000,0x3f353f35,0xbf800000,0xbf6dbec4,0xbf35bf35,0xbec4bf6d,
0x3f80,0x3f80,0x3f80,0x3f80,0xbf80,0xbec4bf6d,0xbf35bf35,0xbf6dbec4,0x3f80,0x3f353f35,0x3f800000,0x3f35bf35,0xbf80,0xbf6dbec4,0xbf353f35,0x3ec43f6d,
0x3f80,0x3f800000,0xbf80,0xbf800000,0xbf80,0xbf6d3ec4,0x3f353f35,0x3ec4bf6d,0x3f80,0x3f35bf35,0xbf800000,0x3f353f35,0xbf80,0xbec43f6d,0x3f35bf35,0xbf6d3ec4,
0x3f80,0xbf80,0x3f80,0xbf80,0xbf80,0x3ec43f6d,0xbf35bf35,0x3f6d3ec4,0x3f80,0xbf35bf35,0x3f800000,0xbf353f35,0xbf80,0x3f6d3ec4,0xbf353f35,0xbec4bf6d,
0x3f80,0xbf800000,0xbf80,0x3f800000,0xbf80,0x3f6dbec4,0x3f353f35,0xbec43f6d,0x3f80,0xbf353f35,0xbf800000,0xbf35bf35,0xbf80,0x3ec4bf6d,0x3f35bf35,0x3f6dbec4,
0x3f80,0x3f80,0x3f80,0x3f80,0xbf800000,0xbf6d3ec4,0xbf353f35,0xbec43f6d,0xbf80,0xbf35bf35,0xbf800000,0xbf353f35,0x3f800000,0x3ec4bf6d,0xbf35bf35,0xbf6d3ec4,
0x3f80,0x3f800000,0xbf80,0xbf800000,0xbf800000,0x3ec43f6d,0x3f35bf35,0xbf6dbec4,0xbf80,0xbf353f35,0x3f800000,0xbf35bf35,0x3f800000,0xbf6dbec4,0x3f353f35,0xbec4bf6d,
0x3f80,0xbf80,0x3f80,0xbf80,0xbf800000,0x3f6dbec4,0xbf353f35,0x3ec4bf6d,0xbf80,0x3f353f35,0xbf800000,0x3f35bf35,0x3f800000,0xbec43f6d,0xbf35bf35,0x3f6dbec4,
0x3f80,0xbf800000,0xbf80,0x3f800000,0xbf800000,0xbec4bf6d,0x3f35bf35,0x3f6d3ec4,0xbf80,0x3f35bf35,0x3f800000,0x3f353f35,0x3f800000,0x3f6d3ec4,0x3f353f35,0x3ec43f6d,
0x3f808000,0x3f808000,0x3f808000,0x3f808000,0x3f808000,0x3f6dbec4,0x3f35bf35,0x3ec4bf6d,0x3f808000,0x3f35bf35,0xbf80,0xbf35bf35,0x3f808000,0x3ec4bf6d,0xbf35bf35,0xbf6d3ec4,
0x3f808000,0xbf80,0xbf808000,0x3f80,0x3f808000,0xbec4bf6d,0xbf353f35,0x3f6d3ec4,0x3f808000,0xbf35bf35,0x3f80,0x3f35bf35,0x3f808000,0xbf6dbec4,0x3f353f35,0xbec4bf6d,
0x3f808000,0xbf808000,0x3f808000,0xbf808000,0x3f808000,0xbf6d3ec4,0x3f35bf35,0xbec43f6d,0x3f808000,0xbf353f35,0xbf80,0x3f353f35,0x3f808000,0xbec43f6d,0xbf35bf35,0x3f6dbec4,
0x3f808000,0x3f80,0xbf808000,0xbf80,0x3f808000,0x3ec43f6d,0xbf353f35,0xbf6dbec4,0x3f808000,0x3f353f35,0x3f80,0xbf353f35,0x3f808000,0x3f6d3ec4,0x3f353f35,0x3ec43f6d,
0x3f808000,0x3f808000,0x3f808000,0x3f808000,0xbf80,0xbec4bf6d,0xbf35bf35,0xbf6dbec4,0xbf808000,0xbf353f35,0x3f80,0x3f353f35,0x3f80,0x3f6d3ec4,0x3f35bf35,0xbec4bf6d,
0x3f808000,0xbf80,0xbf808000,0x3f80,0xbf80,0xbf6d3ec4,0x3f353f35,0x3ec4bf6d,0xbf808000,0x3f353f35,0xbf80,0xbf353f35,0x3f80,0x3ec4bf6d,0xbf353f35,0x3f6dbec4,
0x3f808000,0xbf808000,0x3f808000,0xbf808000,0xbf80,0x3ec43f6d,0xbf35bf35,0x3f6d3ec4,0xbf808000,0x3f35bf35,0x3f80,0xbf35bf35,0x3f80,0xbf6dbec4,0x3f35bf35,0x3ec43f6d,
0x3f808000,0x3f80,0xbf808000,0xbf80,0xbf80,0x3f6dbec4,0x3f353f35,0xbec43f6d,0xbf808000,0xbf35bf35,0xbf80,0x3f35bf35,0x3f80,0xbec43f6d,0xbf353f35,0xbf6d3ec4,
0x3f808000,0x3f808000,0x3f808000,0x3f808000,0xbf808000,0xbf6d3ec4,0xbf353f35,0xbec43f6d,0x3f808000,0x3f35bf35,0xbf80,0xbf35bf35,0xbf808000,0xbec43f6d,0x3f353f35,0x3f6dbec4,
0x3f808000,0xbf80,0xbf808000,0x3f80,0xbf808000,0x3ec43f6d,0x3f35bf35,0xbf6dbec4,0x3f808000,0xbf35bf35,0x3f80,0x3f35bf35,0xbf808000,0x3f6d3ec4,0xbf35bf35,0x3ec43f6d,
0x3f808000,0xbf808000,0x3f808000,0xbf808000,0xbf808000,0x3f6dbec4,0xbf353f35,0x3ec4bf6d,0x3f808000,0xbf353f35,0xbf80,0x3f353f35,0xbf808000,0x3ec4bf6d,0x3f353f35,0xbf6d3ec4,
0x3f808000,0x3f80,0xbf808000,0xbf80,0xbf808000,0xbec4bf6d,0x3f35bf35,0x3f6d3ec4,0x3f808000,0x3f353f35,0x3f80,0xbf353f35,0xbf808000,0xbf6dbec4,0xbf35bf35,0xbec4bf6d,
0x3f808000,0x3f808000,0x3f808000,0x3f808000,0x3f80,0x3ec43f6d,0x3f353f35,0x3f6d3ec4,0xbf808000,0xbf353f35,0x3f80,0x3f353f35,0xbf80,0xbf6dbec4,0xbf353f35,0x3ec43f6d,
0x3f808000,0xbf80,0xbf808000,0x3f80,0x3f80,0x3f6dbec4,0xbf35bf35,0xbec43f6d,0xbf808000,0x3f353f35,0xbf80,0xbf353f35,0xbf80,0xbec43f6d,0x3f35bf35,0xbf6d3ec4,
0x3f808000,0xbf808000,0x3f808000,0xbf808000,0x3f80,0xbec4bf6d,0x3f353f35,0xbf6dbec4,0xbf808000,0x3f35bf35,0x3f80,0xbf35bf35,0xbf80,0x3f6d3ec4,0xbf353f35,0xbec4bf6d,
0x3f808000,0x3f80,0xbf808000,0xbf80,0x3f80,0xbf6d3ec4,0xbf35bf35,0x3ec4bf6d,0xbf808000,0xbf35bf35,0xbf80,0x3f35bf35,0xbf80,0x3ec4bf6d,0x3f35bf35,0x3f6dbec4,
0x3f803f80,0x3f803f80,0,0,0x3f6d3f80,0x3ec43f35,0x3ec40000,0x3f6d3f35,0x3f353f80,0xbf350000,0x3f350000,0x3f353f80,0x3ec43f80,0xbf6dbf35,0x3f6d0000,0xbec43f35,
0x3f80,0xbf80,0x3f800000,0xbf800000,0xbec43f80,0x3f6dbf35,0x3f6d0000,0xbec4bf35,0xbf353f80,0x3f350000,0x3f350000,0x3f35bf80,0xbf6d3f80,0xbec43f35,0x3ec40000,0x3f6dbf35,
0xbf803f80,0xbf803f80,0,0,0xbf6d3f80,0xbec43f35,0xbec40000,0xbf6d3f35,0xbf353f80,0x3f350000,0xbf350000,0xbf353f80,0xbec43f80,0x3f6dbf35,0xbf6d0000,0x3ec43f35,
0x3f80,0xbf80,0xbf800000,0x3f800000,0x3ec43f80,0xbf6dbf35,0xbf6d0000,0x3ec4bf35,0x3f353f80,0xbf350000,0xbf350000,0xbf35bf80,0x3f6d3f80,0x3ec43f35,0xbec40000,0xbf6dbf35,
0x3f803f80,0x3f803f80,0,0,0xbec40000,0xbf6dbf35,0x3f6d3f80,0x3ec43f35,0xbf35bf80,0x3f350000,0xbf350000,0xbf35bf80,0x3f6d0000,0xbec43f35,0xbec4bf80,0x3f6d3f35,
0x3f80,0xbf80,0x3f800000,0xbf800000,0xbf6d0000,0x3ec43f35,0xbec43f80,0x3f6dbf35,0x3f35bf80,0xbf350000,0xbf350000,0xbf353f80,0x3ec40000,0x3f6dbf35,0x3f6dbf80,0x3ec4bf35,
0xbf803f80,0xbf803f80,0,0,0x3ec40000,0x3f6dbf35,0xbf6d3f80,0xbec43f35,0x3f35bf80,0xbf350000,0x3f350000,0x3f35bf80,0xbf6d0000,0x3ec43f35,0x3ec4bf80,0xbf6d3f35,
0x3f80,0xbf80,0xbf800000,0x3f800000,0x3f6d0000,0xbec43f35,0x3ec43f80,0xbf6dbf35,0xbf35bf80,0x3f350000,0x3f350000,0x3f353f80,0xbec40000,0xbf6dbf35,0xbf6dbf80,0xbec4bf35,
0x3f803f80,0x3f803f80,0,0,0xbf6dbf80,0xbec4bf35,0xbec40000,0xbf6dbf35,0x3f353f80,0xbf350000,0x3f350000,0x3f353f80,0xbec4bf80,0x3f6d3f35,0xbf6d0000,0x3ec4bf35,
0x3f80,0xbf80,0x3f800000,0xbf800000,0x3ec4bf80,0xbf6d3f35,0xbf6d0000,0x3ec43f35,0xbf353f80,0x3f350000,0x3f350000,0x3f35bf80,0x3f6dbf80,0x3ec4bf35,0xbec40000,0xbf6d3f35,
0xbf803f80,0xbf803f80,0,0,0x3f6dbf80,0x3ec4bf35,0x3ec40000,0x3f6dbf35,0xbf353f80,0x3f350000,0xbf350000,0xbf353f80,0x3ec4bf80,0xbf6d3f35,0x3f6d0000,0xbec4bf35,
0x3f80,0xbf80,0xbf800000,0x3f800000,0xbec4bf80,0x3f6d3f35,0x3f6d0000,0xbec43f35,0x3f353f80,0xbf350000,0xbf350000,0xbf35bf80,0xbf6dbf80,0xbec4bf35,0x3ec40000,0x3f6d3f35,
0x3f803f80,0x3f803f80,0,0,0x3ec40000,0x3f6d3f35,0xbf6dbf80,0xbec4bf35,0xbf35bf80,0x3f350000,0xbf350000,0xbf35bf80,0xbf6d0000,0x3ec4bf35,0x3ec43f80,0xbf6dbf35,
0x3f80,0xbf80,0x3f800000,0xbf800000,0x3f6d0000,0xbec4bf35,0x3ec4bf80,0xbf6d3f35,0x3f35bf80,0xbf350000,0xbf350000,0xbf353f80,0xbec40000,0xbf6d3f35,0xbf6d3f80,0xbec43f35,
0xbf803f80,0xbf803f80,0,0,0xbec40000,0xbf6d3f35,0x3f6dbf80,0x3ec4bf35,0x3f35bf80,0xbf350000,0x3f350000,0x3f35bf80,0x3f6d0000,0xbec4bf35,0xbec43f80,0x3f6dbf35,
0x3f80,0xbf80,0xbf800000,0x3f800000,0xbf6d0000,0x3ec4bf35,0xbec4bf80,0x3f6d3f35,0xbf35bf80,0x3f350000,0x3f350000,0x3f353f80,0x3ec40000,0x3f6d3f35,0x3f6d3f80,0x3ec43f35,
0x80008000,0x80008000,0x3f803f80,0x3f803f80,0xbec48000,0xbf6dbf35,0x3f6d3f80,0x3ec43f35,0xbf358000,0xbf35bf80,0x3f353f80,0xbf350000,0xbf6d8000,0x3ec4bf35,0x3ec43f80,0xbf6dbf35,
0xbf808000,0x3f808000,0x3f80,0xbf80,0xbf6d8000,0x3ec43f35,0xbec43f80,0x3f6dbf35,0xbf358000,0xbf353f80,0xbf353f80,0x3f350000,0xbec48000,0xbf6d3f35,0xbf6d3f80,0xbec43f35,
0x80008000,0x80008000,0xbf803f80,0xbf803f80,0x3ec48000,0x3f6dbf35,0xbf6d3f80,0xbec43f35,0x3f358000,0x3f35bf80,0xbf353f80,0x3f350000,0x3f6d8000,0xbec4bf35,0xbec43f80,0x3f6dbf35,
0x3f808000,0xbf808000,0x3f80,0xbf80,0x3f6d8000,0xbec43f35,0x3ec43f80,0xbf6dbf35,0x3f358000,0x3f353f80,0x3f353f80,0xbf350000,0x3ec48000,0x3f6d3f35,0x3f6d3f80,0x3ec43f35,
0x80008000,0x80008000,0x3f803f80,0x3f803f80,0xbf6dbf80,0xbec4bf35,0xbec40000,0xbf6dbf35,0x3f358000,0x3f353f80,0xbf35bf80,0x3f350000,0x3ec43f80,0xbf6dbf35,0x3f6d0000,0xbec43f35,
0xbf808000,0x3f808000,0x3f80,0xbf80,0x3ec4bf80,0xbf6d3f35,0xbf6d0000,0x3ec43f35,0x3f358000,0x3f35bf80,0x3f35bf80,0xbf350000,0xbf6d3f80,0xbec43f35,0x3ec40000,0x3f6dbf35,
0x80008000,0x80008000,0xbf803f80,0xbf803f80,0x3f6dbf80,0x3ec4bf35,0x3ec40000,0x3f6dbf35,0xbf358000,0xbf353f80,0x3f35bf80,0xbf350000,0xbec43f80,0x3f6dbf35,0xbf6d0000,0x3ec43f35,
0x3f808000,0xbf808000,0x3f80,0xbf80,0xbec4bf80,0x3f6d3f35,0x3f6d0000,0xbec43f35,0xbf358000,0xbf35bf80,0xbf35bf80,0x3f350000,0x3f6d3f80,0x3ec43f35,0xbec40000,0xbf6dbf35,
0x80008000,0x80008000,0x3f803f80,0x3f803f80,0x3ec48000,0x3f6d3f35,0xbf6dbf80,0xbec4bf35,0xbf358000,0xbf35bf80,0x3f353f80,0xbf350000,0x3f6d8000,0xbec43f35,0xbec4bf80,0x3f6d3f35,
0xbf808000,0x3f808000,0x3f80,0xbf80,0x3f6d8000,0xbec4bf35,0x3ec4bf80,0xbf6d3f35,0xbf358000,0xbf353f80,0xbf353f80,0x3f350000,0x3ec48000,0x3f6dbf35,0x3f6dbf80,0x3ec4bf35,
0x80008000,0x80008000,0xbf803f80,0xbf803f80,0xbec48000,0xbf6d3f35,0x3f6dbf80,0x3ec4bf35,0x3f358000,0x3f35bf80,0xbf353f80,0x3f350000,0xbf6d8000,0x3ec43f35,0x3ec4bf80,0xbf6d3f35,
0x3f808000,0xbf808000,0x3f80,0xbf80,0xbf6d8000,0x3ec4bf35,0xbec4bf80,0x3f6d3f35,0x3f358000,0x3f353f80,0x3f353f80,0xbf350000,0xbec48000,0xbf6dbf35,0xbf6dbf80,0xbec4bf35,
0x80008000,0x80008000,0x3f803f80,0x3f803f80,0x3f6d3f80,0x3ec43f35,0x3ec40000,0x3f6d3f35,0x3f358000,0x3f353f80,0xbf35bf80,0x3f350000,0xbec4bf80,0x3f6d3f35,0xbf6d0000,0x3ec4bf35,
0xbf808000,0x3f808000,0x3f80,0xbf80,0xbec43f80,0x3f6dbf35,0x3f6d0000,0xbec4bf35,0x3f358000,0x3f35bf80,0x3f35bf80,0xbf350000,0x3f6dbf80,0x3ec4bf35,0xbec40000,0xbf6d3f35,
0x80008000,0x80008000,0xbf803f80,0xbf803f80,0xbf6d3f80,0xbec43f35,0xbec40000,0xbf6d3f35,0xbf358000,0xbf353f80,0x3f35bf80,0xbf350000,0x3ec4bf80,0xbf6d3f35,0x3f6d0000,0xbec4bf35,
0x3f808000,0xbf808000,0x3f80,0xbf80,0x3ec43f80,0xbf6dbf35,0xbf6d0000,0x3ec4bf35,0xbf358000,0xbf35bf80,0xbf35bf80,0x3f350000,0xbf6dbf80,0xbec4bf35,0x3ec40000,0x3f6d3f35,
};
constexpr int TW_1 = 0, TW_2 = 512, TW_3 = 4608, TW_4 = 5120, TW_5 = 9216;
__device__ const unsigned g_tw[9728] __attribute__((aligned(16))) = {
0x3f800000,0x3f800000,0,0,0x3f800000,0x3f800000,0,0,0x3f800000,0x3f7ff98e,0,0xbc65c70c,0x3f7fe638,0x3f7fc600,0xbce5c143,0xbd2c49b7,
0x3f800000,0x3f7fe638,0,0xbce5c143,0x3f7f98e6,0x3f7f181a,0xbd65aa20,0xbdac22ae,0x3f800000,0x3f7fc600,0,0xbd2c49b7,0x3f7f181a,0x3f7df69c,0xbdac22ae,0xbe00e941,
0x3f800000,0x3f7f98e6,0,0xbd65aa20,0x3f7e63ec,0x3f7c620b,0xbde54da2,0xbe2b86c0,0x3f800000,0x3f7f5eee,0,0xbd8f7f7c,0x3f7d7c83,0x3f7a5b1d,0xbe0f2533,0xbe55d687,
0x3f800000,0x3f7f181a,0,0xbdac22ae,0x3f7c620b,0x3f77e2bd,0xbe2b86c0,0xbe7fc568,0x3f800000,0x3f7ec46d,0,0xbdc8bd36,0x3f7b14be,0x3f74fa0b,0xbe47c5c2,0xbe94a031,
0x3f800000,0x3f7e63ec,0,0xbde54da2,0x3f7994e0,0x3f71a257,0xbe63dc87,0xbea91a56,0x3f800000,0x3f7df69c,0,0xbe00e941,0x3f77e2bd,0x3f6ddd24,0xbe7fc568,0xbebd47db,
0x3f800000,0x3f7d7c83,0,0xbe0f2533,0x3f75fead,0x3f69ac2a,0xbe8dbd62,0xbed11f9b,0x3f800000,0x3f7cf5a5,0,0xbe1d59f0,0x3f73e912,0x3f65114d,0xbe9b7b85,0xbee49898,
0x3f800000,0x3f7c620b,0,0xbe2b86c0,0x3f71a257,0x3f600ea4,0xbea91a56,0xbef7aa01,0x3f800000,0x3f7bc1bb,0,0xbe39aaee,0x3f6f2af0,0x3f5aa675,0xbeb69719,0xbf052598,
0x3f800000,0x3f7b14be,0,0xbe47c5c2,0x3f6c835e,0x3f54db31,0xbec3ef15,0xbf0e39da,0x3f800000,0x3f7a5b1d,0,0xbe55d687,0x3f69ac2a,0x3f4eaf7a,0xbed11f9b,0xbf170daa,
0x3f800000,0x3f7994e0,0,0xbe63dc87,0x3f66a5e5,0x3f48261c,0xbede2602,0xbf1f9d07,0x3f800000,0x3f78c212,0,0xbe71d70e,0x3f63712c,0x3f41420b,0xbeeaffac,0xbf27e411,
0x3f800000,0x3f77e2bd,0,0xbe7fc568,0x3f600ea4,0x3f3a0669,0xbef7aa01,0xbf2fdf08,0x3f800000,0x3f76f6ed,0,0xbe86d370,0x3f5c7efc,0x3f32767c,0xbf02113a,0xbf378a4d,
0x3f800000,0x3f75fead,0,0xbe8dbd62,0x3f58c2eb,0x3f2a95b0,0xbf083341,0xbf3ee268,0x3f800000,0x3f74fa0b,0,0xbe94a031,0x3f54db31,0x3f226799,0xbf0e39da,0xbf45e403,
0x3f800000,0x3f73e912,0,0xbe9b7b85,0x3f50c899,0x3f19efeb,0xbf1423cd,0xbf4c8bf3,0x3f800000,0x3f72cbd1,0,0xbea24f04,0x3f4c8bf3,0x3f11327b,0xbf19efeb,0xbf52d734,
0x3f800000,0x3f71a257,0,0xbea91a56,0x3f48261c,0x3f083341,0xbf1f9d07,0xbf58c2eb,0x3f800000,0x3f706cb1,0,0xbeafdd25,0x3f4397f4,0x3efdec9f,0xbf2529fe,0xbf5e4c69,
0x3f800000,0x3f6f2af0,0,0xbeb69719,0x3f3ee268,0x3eeaffac,0xbf2a95b0,0xbf63712c,0x3f800000,0x3f6ddd24,0,0xbebd47db,0x3f3a0669,0x3ed7a83d,0xbf2fdf08,0xbf682ee0,
0x3f800000,0x3f6c835e,0,0xbec3ef15,0x3f3504f3,0x3ec3ef15,0xbf3504f3,0xbf6c835e,0x3f800000,0x3f6b1db0,0,0xbeca8c72,0x3f2fdf08,0x3eafdd25,0xbf3a0669,0xbf706cb1,
0x3f800000,0x3f69ac2a,0,0xbed11f9b,0x3f2a95b0,0x3e9b7b85,0xbf3ee268,0xbf73e912,0x3f800000,0x3f682ee0,0,0xbed7a83d,0x3f2529fe,0x3e86d370,0xbf4397f4,0xbf76f6ed,
0x3f800000,0x3f66a5e5,0,0xbede2602,0x3f1f9d07,0x3e63dc87,0xbf48261c,0xbf7994e0,0x3f800000,0x3f65114d,0,0xbee49898,0x3f19efeb,0x3e39aaee,0xbf4c8bf3,0xbf7bc1bb,
0x3f800000,0x3f63712c,0,0xbeeaffac,0x3f1423cd,0x3e0f2533,0xbf50c899,0xbf7d7c83,0x3f800000,0x3f61c598,0,0xbef15aea,0x3f0e39da,0x3dc8bd36,0xbf54db31,0xbf7ec46d,
0x3f800000,0x3f600ea4,0,0xbef7aa01,0x3f083341,0x3d65aa20,0xbf58c2eb,0xbf7f98e6,0x3f800000,0x3f5e4c69,0,0xbefdec9f,0x3f02113a,0x3c65c70c,0xbf5c7efc,0xbf7ff98e,
0x3f800000,0x3f5c7efc,0,0xbf02113a,0x3ef7aa01,0xbce5c143,0xbf600ea4,0xbf7fe638,0x3f800000,0x3f5aa675,0,0xbf052598,0x3eeaffac,0xbd8f7f7c,0xbf63712c,0xbf7f5eee,
0x3f800000,0x3f58c2eb,0,0xbf083341,0x3ede2602,0xbde54da2,0xbf66a5e5,0xbf7e63ec,0x3f800000,0x3f56d477,0,0xbf0b3a0f,0x3ed11f9b,0xbe1d59f0,0xbf69ac2a,0xbf7cf5a5,
0x3f800000,0x3f54db31,0,0xbf0e39da,0x3ec3ef15,0xbe47c5c2,0xbf6c835e,0xbf7b14be,0x3f800000,0x3f52d734,0,0xbf11327b,0x3eb69719,0xbe71d70e,0xbf6f2af0,0xbf78c212,
0x3f800000,0x3f50c899,0,0xbf1423cd,0x3ea91a56,0xbe8dbd62,0xbf71a257,0xbf75fead,0x3f800000,0x3f4eaf7a,0,0xbf170daa,0x3e9b7b85,0xbea24f04,0xbf73e912,0xbf72cbd1,
0x3f800000,0x3f4c8bf3,0,0xbf19efeb,0x3e8dbd62,0xbeb69719,0xbf75fead,0xbf6f2af0,0x3f800000,0x3f4a5e20,0,0xbf1cca6b,0x3e7fc568,0xbeca8c72,0xbf77e2bd,0xbf6b1db0,
0x3f800000,0x3f48261c,0,0xbf1f9d07,0x3e63dc87,0xbede2602,0xbf7994e0,0xbf66a5e5,0x3f800000,0x3f45e403,0,0xbf226799,0x3e47c5c2,0xbef15aea,0xbf7b14be,0xbf61c598,
0x3f800000,0x3f4397f4,0,0xbf2529fe,0x3e2b86c0,0xbf02113a,0xbf7c620b,0xbf5c7efc,0x3f800000,0x3f41420b,0,0xbf27e411,0x3e0f2533,0xbf0b3a0f,0xbf7d7c83,0xbf56d477,
0x3f800000,0x3f3ee268,0,0xbf2a95b0,0x3de54da2,0xbf1423cd,0xbf7e63ec,0xbf50c899,0x3f800000,0x3f3c7927,0,0xbf2d3eb9,0x3dac22ae,0xbf1cca6b,0xbf7f181a,0xbf4a5e20,
0x3f800000,0x3f3a0669,0,0xbf2fdf08,0x3d65aa20,0xbf2529fe,0xbf7f98e6,0xbf4397f4,0x3f800000,0x3f378a4d,0,0xbf32767c,0x3ce5c143,0xbf2d3eb9,0xbf7fe638,0xbf3c7927,
0x3f800000,0x3f3504f3,0,0xbf3504f3,0,0xbf3504f3,0xbf800000,0xbf3504f3,0x3f800000,0x3f32767c,0,0xbf378a4d,0xbce5c143,0xbf3c7927,0xbf7fe638,0xbf2d3eb9,
0x3f800000,0x3f2fdf08,0,0xbf3a0669,0xbd65aa20,0xbf4397f4,0xbf7f98e6,0xbf2529fe,0x3f800000,0x3f2d3eb9,0,0xbf3c7927,0xbdac22ae,0xbf4a5e20,0xbf7f181a,0xbf1cca6b,
0x3f800000,0x3f2a95b0,0,0xbf3ee268,0xbde54da2,0xbf50c899,0xbf7e63ec,0xbf1423cd,0x3f800000,0x3f27e411,0,0xbf41420b,0xbe0f2533,0xbf56d477,0xbf7d7c83,0xbf0b3a0f,
0x3f800000,0x3f2529fe,0,0xbf4397f4,0xbe2b86c0,0xbf5c7efc,0xbf7c620b,0xbf02113a,0x3f800000,0x3f226799,0,0xbf45e403,0xbe47c5c2,0xbf61c598,0xbf7b14be,0xbef15aea,
0x3f800000,0x3f800000,0,0,0x3f800000,0x3f800000,0,0,0x3f800000,0x3f800000,0,0,0x3f800000,0x3f800000,0,0,
0x3f800000,0x3f7fffb1,0,0xbb490fc6,0x3f7ffec4,0x3f7ffd39,0xbbc90f88,0xbc16cb58,0x3f7ffb11,0x3f7ff84a,0xbc490e90,0xbc7b514b,0x3f7ff4e6,0x3f7ff0e3,0xbc96c9b6,0xbcafea69,
0x3f800000,0x3f7ffec4,0,0xbbc90f88,0x3f7ffb11,0x3f7ff4e6,0xbc490e90,0xbc96c9b6,0x3f7fec43,0x3f7fe129,0xbcc90ab0,0xbcfb49ba,0x3f7fd397,0x3f7fc38f,0xbd16c32c,0xbd2fe007,
0x3f800000,0x3f7ffd39,0,0xbc16cb58,0x3f7ff4e6,0x3f7fe705,0xbc96c9b6,0xbce22a7a,0x3f7fd397,0x3f7fba9e,0xbd16c32c,0xbd3c6dd5,0x3f7f9c18,0x3f7f7808,0xbd621469,0xbd83db0a,
0x3f800000,0x3f7ffb11,0,0xbc490e90,0x3f7fec43,0x3f7fd397,0xbcc90ab0,0xbd16c32c,0x3f7fb10f,0x3f7f84ab,0xbd48fb30,0xbd7b2b74,0x3f7f4e6d,0x3f7f0e58,0xbd96a905,0xbdafb680,
0x3f800000,0x3f7ff84a,0,0xbc7b514b,0x3f7fe129,0x3f7fba9e,0xbcfb49ba,0xbd3c6dd5,0x3f7f84ab,0x3f7f3f54,0xbd7b2b74,0xbd9cecf9,0x3f7eea9d,0x3f7e868b,0xbdbc3ac3,0xbddb7d37,
0x3f800000,0x3f7ff4e6,0,0xbc96c9b6,0x3f7fd397,0x3f7f9c18,0xbd16c32c,0xbd621469,0x3f7f4e6d,0x3f7eea9d,0xbd96a905,0xbdbc3ac3,0x3f7e70b0,0x3f7de0b1,0xbde1bc2e,0xbe039502,
0x3f800000,0x3f7ff0e3,0,0xbcafea69,0x3f7fc38f,0x3f7f7808,0xbd2fe007,0xbd83db0a,0x3f7f0e58,0x3f7e868b,0xbdafb680,0xbddb7d37,0x3f7de0b1,0x3f7d1cdd,0xbe039502,0xbe195be0,
0x3f800000,0x3f7fec43,0,0xbcc90ab0,0x3f7fb10f,0x3f7f4e6d,0xbd48fb30,0xbd96a905,0x3f7ec46d,0x3f7e1324,0xbdc8bd36,0xbdfab273,0x3f7d3aac,0x3f7c3b28,0xbe164083,0xbe2f10a2,
0x3f800000,0x3f7fe705,0,0xbce22a7a,0x3f7f9c18,0x3f7f1f49,0xbd621469,0xbda973ba,0x3f7e70b0,0x3f7d906e,0xbde1bc2e,0xbe0cec4a,0x3f7c7eb0,0x3f7b3bab,0xbe28defc,0xbe44b0b9,
0x3f800000,0x3f7fe129,0,0xbcfb49ba,0x3f7f84ab,0x3f7eea9d,0xbd7b2b74,0xbdbc3ac3,0x3f7e1324,0x3f7cfe73,0xbdfab273,0xbe1c76de,0x3f7baccd,0x3f7a1e84,0xbe3b6ecf,0xbe5a3997,
0x3f800000,0x3f7fdaaf,0,0xbd0a342f,0x3f7f6ac7,0x3f7eb069,0xbd8a200a,0xbdcefdb7,0x3f7dabcc,0x3f7c5d3a,0xbe09cf86,0xbe2bf804,0x3f7ac516,0x3f78e3d6,0xbe4dee60,0xbe6fa8b2,
0x3f800000,0x3f7fd397,0,0xbd16c32c,0x3f7f4e6d,0x3f7e70b0,0xbd96a905,0xbde1bc2e,0x3f7d3aac,0x3f7baccd,0xbe164083,0xbe3b6ecf,0x3f79c79d,0x3f778bc5,0xbe605c13,0xbe827dc0,
0x3f800000,0x3f7fcbe2,0,0xbd2351cb,0x3f7f2f9d,0x3f7e2b72,0xbda3308c,0xbdf475c0,0x3f7cbfc9,0x3f7aed37,0xbe22abb6,0xbe4ada4f,0x3f78b47b,0x3f76167a,0xbe72b651,0xbe8d17c0,
0x3f800000,0x3f7fc38f,0,0xbd2fe007,0x3f7f0e58,0x3f7de0b1,0xbdafb680,0xbe039502,0x3f7c3b28,0x3f7a1e84,0xbe2f10a2,0xbe5a3997,0x3f778bc5,0x3f748422,0xbe827dc0,0xbe97a117,
0x3f800000,0x3f7fba9e,0,0xbd3c6dd5,0x3f7eea9d,0x3f7d906e,0xbdbc3ac3,0xbe0cec4a,0x3f7baccd,0x3f7940c0,0xbe3b6ecf,0xbe698bba,0x3f764d97,0x3f72d4eb,0xbe8b9507,0xbea21887,
0x3f800000,0x3f7fb10f,0,0xbd48fb30,0x3f7ec46d,0x3f7d3aac,0xbdc8bd36,0xbe164083,0x3f7b14be,0x3f7853f8,0xbe47c5c2,0xbe78cfcc,0x3f74fa0b,0x3f710908,0xbe94a031,0xbeac7cd4,
0x3f800000,0x3f7fa6e3,0,0xbd55880e,0x3f7e9bc9,0x3f7cdf6c,0xbdd53db9,0xbe1f917b,0x3f7a7302,0x3f77583a,0xbe541501,0xbe840270,0x3f73913f,0x3f6f20b0,0xbe9d9e78,0xbeb6ccc3,
0x3f800000,0x3f7f9c18,0,0xbd621469,0x3f7e70b0,0x3f7c7eb0,0xbde1bc2e,0xbe28defc,0x3f79c79d,0x3f764d97,0xbe605c13,0xbe8b9507,0x3f721352,0x3f6d1c1d,0xbea68f12,0xbec1071e,
0x3f800000,0x3f7f90b1,0,0xbd6ea038,0x3f7e4323,0x3f7c187a,0xbdee3876,0xbe3228d4,0x3f791298,0x3f75341d,0xbe6c9a7f,0xbe931f35,0x3f708066,0x3f6afb8c,0xbeaf713a,0xbecb2aae,
0x3f800000,0x3f7f84ab,0,0xbd7b2b74,0x3f7e1324,0x3f7baccd,0xbdfab273,0xbe3b6ecf,0x3f7853f8,0x3f740bdd,0xbe78cfcc,0xbe9aa086,0x3f6ed89e,0x3f68bf3c,0xbeb8442a,0xbed53641,
0x3f800000,0x3f7f7808,0,0xbd83db0a,0x3f7de0b1,0x3f7b3bab,0xbe039502,0xbe44b0b9,0x3f778bc5,0x3f72d4eb,0xbe827dc0,0xbea21887,0x3f6d1c1d,0x3f666771,0xbec1071e,0xbedf28a9,
0x3f800000,0x3f7f6ac7,0,0xbd8a200a,0x3f7dabcc,0x3f7ac516,0xbe09cf86,0xbe4dee60,0x3f76ba07,0x3f718f57,0xbe888e93,0xbea986c4,0x3f6b4b0c,0x3f63f473,0xbec9b953,0xbee900b7,
0x3f800000,0x3f7f5ce9,0,0xbd9064b4,0x3f7d7474,0x3f7a4910,0xbe1008b7,0xbe57278f,0x3f75dec6,0x3f703b37,0xbe8e9a22,0xbeb0eacb,0x3f696591,0x3f61668a,0xbed25a09,0xbef2bd43,
0x3f800000,0x3f7f4e6d,0,0xbd96a905,0x3f7d3aac,0x3f79c79d,0xbe164083,0xbe605c13,0x3f74fa0b,0x3f6ed89e,0xbe94a031,0xbeb8442a,0x3f676bd8,0x3f5ebe05,0xbedae880,0xbefc5d27,
0x3f800000,0x3f7f3f54,0,0xbd9cecf9,0x3f7cfe73,0x3f7940c0,0xbe1c76de,0xbe698bba,0x3f740bdd,0x3f6d67a1,0xbe9aa086,0xbebf926f,0x3f655e0b,0x3f5bfb34,0xbee363fa,0xbf02ef9f,
0x3f800000,0x3f7f2f9d,0,0xbda3308c,0x3f7cbfc9,0x3f78b47b,0xbe22abb6,0xbe72b651,0x3f731447,0x3f6be858,0xbea09ae5,0xbec6d529,0x3f633c5a,0x3f591e6a,0xbeebcbbb,0xbf07a136,
0x3f800000,0x3f7f1f49,0,0xbda973ba,0x3f7c7eb0,0x3f7822d1,0xbe28defc,0xbe7bdba4,0x3f721352,0x3f6a5ad9,0xbea68f12,0xbece0bea,0x3f6106f2,0x3f5627fe,0xbef41f07,0xbf0c42c9,
0x3f800000,0x3f7f0e58,0,0xbdafb680,0x3f7c3b28,0x3f778bc5,0xbe2f10a2,0xbe827dc0,0x3f710908,0x3f68bf3c,0xbeac7cd4,0xbed53641,0x3f5ebe05,0x3f531849,0xbefc5d27,0xbf10d3cd,
0x3f800000,0x3f7efcc9,0,0xbdb5f8da,0x3f7bf531,0x3f76ef5b,0xbe354098,0xbe870ada,0x3f6ff573,0x3f671599,0xbeb263ef,0xbedc53c1,0x3f5c61c7,0x3f4fefa8,0xbf0242b1,0xbf1553b7,
0x3f800000,0x3f7eea9d,0,0xbdbc3ac3,0x3f7baccd,0x3f764d97,0xbe3b6ecf,0xbe8b9507,0x3f6ed89e,0x3f655e0b,0xbeb8442a,0xbee363fa,0x3f59f26a,0x3f4cae79,0xbf064b82,0xbf19c200,
0x3f800000,0x3f7ed7d4,0,0xbdc27c39,0x3f7b61fc,0x3f75a67b,0xbe419b37,0xbe901c2c,0x3f6db293,0x3f6398ac,0xbebe1d4a,0xbeea6681,0x3f577026,0x3f495521,0xbf0a48ad,0xbf1e1e22,
0x3f800000,0x3f7ec46d,0,0xbdc8bd36,0x3f7b14be,0x3f74fa0b,0xbe47c5c2,0xbe94a031,0x3f6c835e,0x3f61c598,0xbec3ef15,0xbef15aea,0x3f54db31,0x3f45e403,0xbf0e39da,0xbf226799,
0x3f800000,0x3f7eb069,0,0xbdcefdb7,0x3f7ac516,0x3f74484b,0xbe4dee60,0xbe9920fe,0x3f6b4b0c,0x3f5fe4e9,0xbec9b953,0xbef840c8,0x3f5233c6,0x3f425b89,0xbf121eb0,0xbf269de3,
0x3f800000,0x3f7e9bc9,0,0xbdd53db9,0x3f7a7302,0x3f73913f,0xbe541501,0xbe9d9e78,0x3f6a09a7,0x3f5df6be,0xbecf7bca,0xbeff17b2,0x3f4f7a1f,0x3f3ebc1b,0xbf15f6d9,0xbf2ac082,
0x3f800000,0x3f7e868b,0,0xbddb7d37,0x3f7a1e84,0x3f72d4eb,0xbe5a3997,0xbea21887,0x3f68bf3c,0x3f5bfb34,0xbed53641,0xbf02ef9f,0x3f4cae79,0x3f3b0629,0xbf19c200,0xbf2ecef7,
0x3f800000,0x3f7e70b0,0,0xbde1bc2e,0x3f79c79d,0x3f721352,0xbe605c13,0xbea68f12,0x3f676bd8,0x3f59f26a,0xbedae880,0xbf064b82,0x3f49d112,0x3f373a23,0xbf1d7fd1,0xbf32c8c9,
0x3f800000,0x3f7e5a38,0,0xbde7fa9a,0x3f796e4e,0x3f714c7a,0xbe667c66,0xbeab0201,0x3f660f88,0x3f57dc7f,0xbee0924f,0xbf099f4e,0x3f46e22a,0x3f33587a,0xbf212ff9,0xbf36ad7f,
0x3f800000,0x3f7e4323,0,0xbdee3876,0x3f791298,0x3f708066,0xbe6c9a7f,0xbeaf713a,0x3f64aa59,0x3f55b993,0xbee63375,0xbf0cead0,0x3f43e200,0x3f2f61a5,0xbf24d225,0xbf3a7ca4,
0x3f800000,0x3f7e2b72,0,0xbdf475c0,0x3f78b47b,0x3f6faf1b,0xbe72b651,0xbeb3dca5,0x3f633c5a,0x3f5389c7,0xbeebcbbb,0xbf102dd5,0x3f40d0da,0x3f2b561b,0xbf286605,0xbf3e35c5,
0x3f800000,0x3f7e1324,0,0xbdfab273,0x3f7853f8,0x3f6ed89e,0xbe78cfcc,0xbeb8442a,0x3f61c598,0x3f514d3d,0xbef15aea,0xbf13682a,0x3f3daef9,0x3f273656,0xbf2beb4a,0xbf41d870,
0x3f800000,0x3f7dfa38,0,0xbe007745,0x3f77f110,0x3f6dfcf2,0xbe7ee6e1,0xbebca7af,0x3f604621,0x3f4f0417,0xbef6e0cb,0xbf16999f,0x3f3a7ca4,0x3f2302d3,0xbf2f61a5,0xbf456439,
0x3f800000,0x3f7de0b1,0,0xbe039502,0x3f778bc5,0x3f6d1c1d,0xbe827dc0,0xbec1071e,0x3f5ebe05,0x3f4cae79,0xbefc5d27,0xbf19c200,0x3f373a23,0x3f1ebc12,0xbf32c8c9,0xbf48d8b3,
0x3f800000,0x3f7dc68c,0,0xbe06b26e,0x3f772417,0x3f6c3624,0xbe8586ce,0xbec5625c,0x3f5d2d53,0x3f4a4c87,0xbf00e7e4,0xbf1ce11f,0x3f33e7bc,0x3f1a6293,0xbf36206c,0xbf4c3578,
0x3f800000,0x3f7dabcc,0,0xbe09cf86,0x3f76ba07,0x3f6b4b0c,0xbe888e93,0xbec9b953,0x3f5b941a,0x3f47de65,0xbf039c3d,0xbf1ff6cb,0x3f3085bb,0x3f15f6d9,0xbf396842,0xbf4f7a1f,
0x3f800000,0x3f7d906e,0,0xbe0cec4a,0x3f764d97,0x3f6a5ad9,0xbe8b9507,0xbece0bea,0x3f59f26a,0x3f456439,0xbf064b82,0xbf2302d3,0x3f2d1469,0x3f11796b,0xbf3ca003,0xbf52a649,
0x3f800000,0x3f7d7474,0,0xbe1008b7,0x3f75dec6,0x3f696591,0xbe8e9a22,0xbed25a09,0x3f584853,0x3f42de29,0xbf08f59b,0xbf26050a,0x3f299415,0x3f0cead0,0xbf3fc767,0xbf55b993,
0x3f800000,0x3f7d57de,0,0xbe1324ca,0x3f756d97,0x3f686b39,0xbe919ddd,0xbed6a399,0x3f5695e5,0x3f404c5c,0xbf0b9a6b,0xbf28fd41,0x3f26050a,0x3f084b92,0xbf42de29,0xbf58b3a1,
0x3f800000,0x3f7d3aac,0,0xbe164083,0x3f74fa0b,0x3f676bd8,0xbe94a031,0xbedae880,0x3f54db31,0x3f3daef9,0xbf0e39da,0xbf2beb4a,0x3f226799,0x3f039c3d,0xbf45e403,0xbf5b941a,
0x3f800000,0x3f7d1cdd,0,0xbe195be0,0x3f748422,0x3f666771,0xbe97a117,0xbedf28a9,0x3f531849,0x3f3b0629,0xbf10d3cd,0xbf2ecef7,0x3f1ebc12,0x3efdbabb,0xbf48d8b3,0xbf5e5aa6,
0x3f800000,0x3f7cfe73,0,0xbe1c76de,0x3f740bdd,0x3f655e0b,0xbe9aa086,0xbee363fa,0x3f514d3d,0x3f385216,0xbf13682a,0xbf31a81d,0x3f1b02c6,0x3ef41f07,0xbf4bbbf8,0xbf6106f2,
0x3f800000,0x3f7cdf6c,0,0xbe1f917b,0x3f73913f,0x3f644fac,0xbe9d9e78,0xbee79a5d,0x3f4f7a1f,0x3f3592e7,0xbf15f6d9,0xbf34768f,0x3f173c07,0x3eea6681,0xbf4e8d90,0xbf6398ac,
0x3f800000,0x3f7cbfc9,0,0xbe22abb6,0x3f731447,0x3f633c5a,0xbea09ae5,0xbeebcbbb,0x3f4d9f02,0x3f32c8c9,0xbf187fc0,0xbf373a23,0x3f13682a,0x3ee0924f,0xbf514d3d,0xbf660f88,
0x3f800000,0x3f7c9f8a,0,0xbe25c58c,0x3f7294f8,0x3f62241a,0xbea395c5,0xbeeff7fb,0x3f4bbbf8,0x3f2ff3e6,0xbf1b02c6,0xbf39f2ac,0x3f0f8784,0x3ed6a399,0xbf53fac3,0xbf686b39,
0x3f800000,0x3f7c7eb0,0,0xbe28defc,0x3f721352,0x3f6106f2,0xbea68f12,0xbef41f07,0x3f49d112,0x3f2d1469,0xbf1d7fd1,0xbf3ca003,0x3f0b9a6b,0x3ecc9b8b,0xbf5695e5,0xbf6aab7b,
0x3f800000,0x3f7c5d3a,0,0xbe2bf804,0x3f718f57,0x3f5fe4e9,0xbea986c4,0xbef840c8,0x3f47de65,0x3f2a2a80,0xbf1ff6cb,0xbf3f41fc,0x3f07a136,0x3ec27b55,0xbf591e6a,0xbf6cd007,
0x3f800000,0x3f7c3b28,0,0xbe2f10a2,0x3f710908,0x3f5ebe05,0xbeac7cd4,0xbefc5d27,0x3f45e403,0x3f273656,0xbf226799,0xbf41d870,0x3f039c3d,0x3eb8442a,0xbf5b941a,0xbf6ed89e,
0x3f800000,0x3f7c187a,0,0xbe3228d4,0x3f708066,0x3f5d924d,0xbeaf713a,0xbf003a06,0x3f43e200,0x3f24381a,0xbf24d225,0xbf446337,0x3eff17b2,0x3eadf73c,0xbf5df6be,0xbf70c501,
0x3f800000,0x3f7bf531,0,0xbe354098,0x3f6ff573,0x3f5c61c7,0xbeb263ef,0xbf0242b1,0x3f41d870,0x3f212ff9,0xbf273656,0xbf46e22a,0x3ef6e0cb,0x3ea395c5,0xbf604621,0xbf7294f8,
0x3f800000,0x3f7bd14d,0,0xbe3857ec,0x3f6f6830,0x3f5b2c79,0xbeb554ec,0xbf044889,0x3f3fc767,0x3f1e1e22,0xbf299415,0xbf495521,0x3eee9479,0x3e9920fe,0xbf628210,0xbf74484b,
0x3f800000,0x3f7baccd,0,0xbe3b6ecf,0x3f6ed89e,0x3f59f26a,0xbeb8442a,0xbf064b82,0x3f3daef9,0x3f1b02c6,0xbf2beb4a,0xbf4bbbf8,0x3ee63375,0x3e8e9a22,0xbf64aa59,0xbf75dec6,
0x3f800000,0x3f7b87b2,0,0xbe3e853e,0x3f6e46be,0x3f58b3a1,0xbebb31a0,0xbf084b92,0x3f3b8f3b,0x3f17de12,0xbf2e3bde,0xbf4e1689,0x3eddbe79,0x3e840270,0xbf66becc,0xbf77583a,
0x3f800000,0x3f7b61fc,0,0xbe419b37,0x3f6db293,0x3f577026,0xbebe1d4a,0xbf0a48ad,0x3f396842,0x3f14b039,0xbf3085bb,0xbf5064af,0x3ed53641,0x3e72b651,0xbf68bf3c,0xbf78b47b,
0x3f800000,0x3f7b3bab,0,0xbe44b0b9,0x3f6d1c1d,0x3f5627fe,0xbec1071e,0xbf0c42c9,0x3f373a23,0x3f11796b,0xbf32c8c9,0xbf52a649,0x3ecc9b8b,0x3e5d4b1a,0xbf6aab7b,0xbf79f35e,
0x3f800000,0x3f7b14be,0,0xbe47c5c2,0x3f6c835e,0x3f54db31,0xbec3ef15,0xbf0e39da,0x3f3504f3,0x3f0e39da,0xbf3504f3,0xbf54db31,0x3ec3ef15,0x3e47c5c2,0xbf6c835e,0xbf7b14be,
0x3f800000,0x3f7aed37,0,0xbe4ada4f,0x3f6be858,0x3f5389c7,0xbec6d529,0xbf102dd5,0x3f32c8c9,0x3f0af1b7,0xbf373a23,0xbf570348,0x3ebb31a0,0x3e3228d4,0xbf6e46be,0xbf7c187a,
0x3f800000,0x3f7ac516,0,0xbe4dee60,0x3f6b4b0c,0x3f5233c6,0xbec9b953,0xbf121eb0,0x3f3085bb,0x3f07a136,0xbf396842,0xbf591e6a,0x3eb263ef,0x3e1c76de,0xbf6ff573,0xbf7cfe73,
0x3f800000,0x3f7a9c59,0,0xbe5101f1,0x3f6aab7b,0x3f50d937,0xbecc9b8b,0xbf140c5f,0x3f2e3bde,0x3f044889,0xbf3b8f3b,0xbf5b2c79,0x3ea986c4,0x3e06b26e,0xbf718f57,0xbf7dc68c,
0x3f800000,0x3f7a7302,0,0xbe541501,0x3f6a09a7,0x3f4f7a1f,0xbecf7bca,0xbf15f6d9,0x3f2beb4a,0x3f00e7e4,0xbf3daef9,0xbf5d2d53,0x3ea09ae5,0x3de1bc2e,0xbf731447,0xbf7e70b0,
0x3f800000,0x3f7a4910,0,0xbe57278f,0x3f696591,0x3f4e1689,0xbed25a09,0xbf17de12,0x3f299415,0x3efafef7,0xbf3fc767,0xbf5f20db,0x3e97a117,0x3db5f8da,0xbf748422,0xbf7efcc9,
0x3f800000,0x3f7a1e84,0,0xbe5a3997,0x3f68bf3c,0x3f4cae79,0xbed53641,0xbf19c200,0x3f273656,0x3ef41f07,0xbf41d870,0xbf6106f2,0x3e8e9a22,0x3d8a200a,0xbf75dec6,0xbf7f6ac7,
0x3f800000,0x3f79f35e,0,0xbe5d4b1a,0x3f6816a8,0x3f4b41fa,0xbed8106b,0xbf1ba299,0x3f24d225,0x3eed3063,0xbf43e200,0xbf62df7b,0x3e8586ce,0x3d3c6dd5,0xbf772417,0xbf7fba9e,
0x3f800000,0x3f79c79d,0,0xbe605c13,0x3f676bd8,0x3f49d112,0xbedae880,0xbf1d7fd1,0x3f226799,0x3ee63375,0xbf45e403,0xbf64aa59,0x3e78cfcc,0x3cc90ab0,0xbf7853f8,0xbf7fec43,
0x3f800000,0x3f799b43,0,0xbe636c83,0x3f66becc,0x3f485bca,0xbeddbe79,0xbf1f599f,0x3f1ff6cb,0x3edf28a9,0xbf47de65,0xbf666771,0x3e667c66,0x3b490fc6,0xbf796e4e,0xbf7fffb1,
0x3f800000,0x3f796e4e,0,0xbe667c66,0x3f660f88,0x3f46e22a,0xbee0924f,0xbf212ff9,0x3f1d7fd1,0x3ed8106b,0xbf49d112,0xbf6816a8,0x3e541501,0xbc96c9b6,0xbf7a7302,0xbf7ff4e6,
0x3f800000,0x3f7940c0,0,0xbe698bba,0x3f655e0b,0x3f456439,0xbee363fa,0xbf2302d3,0x3f1b02c6,0x3ed0eb2a,0xbf4bbbf8,0xbf69b7e4,0x3e419b37,0xbd2351cb,0xbf7b61fc,0xbf7fcbe2,
0x3f800000,0x3f791298,0,0xbe6c9a7f,0x3f64aa59,0x3f43e200,0xbee63375,0xbf24d225,0x3f187fc0,0x3ec9b953,0xbf4d9f02,0xbf6b4b0c,0x3e2f10a2,0xbd7b2b74,0xbf7c3b28,0xbf7f84ab,
0x3f800000,0x3f78e3d6,0,0xbe6fa8b2,0x3f63f473,0x3f425b89,0xbee900b7,0xbf269de3,0x3f15f6d9,0x3ec27b55,0xbf4f7a1f,0xbf6cd007,0x3e1c76de,0xbda973ba,0xbf7cfe73,0xbf7f1f49,
0x3f800000,0x3f78b47b,0,0xbe72b651,0x3f633c5a,0x3f40d0da,0xbeebcbbb,0xbf286605,0x3f13682a,0x3ebb31a0,0xbf514d3d,0xbf6e46be,0x3e09cf86,0xbdd53db9,0xbf7dabcc,0xbf7e9bc9,
0x3f800000,0x3f788486,0,0xbe75c35a,0x3f628210,0x3f3f41fc,0xbeee9479,0xbf2a2a80,0x3f10d3cd,0x3eb3dca5,0xbf531849,0xbf6faf1b,0x3dee3876,0xbe007745,0xbf7e4323,0xbf7dfa38,
0x3f800000,0x3f7853f8,0,0xbe78cfcc,0x3f61c598,0x3f3daef9,0xbef15aea,0xbf2beb4a,0x3f0e39da,0x3eac7cd4,0xbf54db31,0xbf710908,0x3dc8bd36,0xbe164083,0xbf7ec46d,0xbf7d3aac,
0x3f800000,0x3f7822d1,0,0xbe7bdba4,0x3f6106f2,0x3f3c17d9,0xbef41f07,0xbf2da859,0x3f0b9a6b,0x3ea5129f,0xbf5695e5,0xbf725470,0x3da3308c,0xbe2bf804,0xbf7f2f9d,0xbf7c5d3a,
0x3f800000,0x3f77f110,0,0xbe7ee6e1,0x3f604621,0x3f3a7ca4,0xbef6e0cb,0xbf2f61a5,0x3f08f59b,0x3e9d9e78,0xbf584853,0xbf73913f,0x3d7b2b74,0xbe419b37,0xbf7f84ab,0xbf7b61fc,
0x3f800000,0x3f77beb7,0,0xbe80f8c0,0x3f5f8327,0x3f38dd65,0xbef9a02d,0xbf311722,0x3f064b82,0x3e9620d2,0xbf59f26a,0xbf74bf62,0x3d2fe007,0xbe57278f,0xbf7fc38f,0xbf7a4910,
0x3f800000,0x3f778bc5,0,0xbe827dc0,0x3f5ebe05,0x3f373a23,0xbefc5d27,0xbf32c8c9,0x3f039c3d,0x3e8e9a22,0xbf5b941a,0xbf75dec6,0x3cc90ab0,0xbe6c9a7f,0xbf7fec43,0xbf791298,
0x3f800000,0x3f77583a,0,0xbe840270,0x3f5df6be,0x3f3592e7,0xbeff17b2,0xbf34768f,0x3f00e7e4,0x3e870ada,0xbf5d2d53,0xbf76ef5b,0x3bc90f88,0xbe80f8c0,0xbf7ffec4,0xbf77beb7,
0x3f800000,0x3f772417,0,0xbe8586ce,0x3f5d2d53,0x3f33e7bc,0xbf00e7e4,0xbf36206c,0x3efc5d27,0x3e7ee6e1,0xbf5ebe05,0xbf77f110,0xbc490e90,0xbe8b9507,0xbf7ffb11,0xbf764d97,
0x3f800000,0x3f76ef5b,0,0xbe870ada,0x3f5c61c7,0x3f3238aa,0xbf0242b1,0xbf37c655,0x3ef6e0cb,0x3e6fa8b2,0xbf604621,0xbf78e3d6,0xbcfb49ba,0xbe9620d2,0xbf7fe129,0xbf74bf62,
0x3f800000,0x3f76ba07,0,0xbe888e93,0x3f5b941a,0x3f3085bb,0xbf039c3d,0xbf396842,0x3ef15aea,0x3e605c13,0xbf61c598,0xbf79c79d,0xbd48fb30,0xbea09ae5,0xbf7fb10f,0xbf731447,
0x3f800000,0x3f76841b,0,0xbe8a11f7,0x3f5ac450,0x3f2ecef7,0xbf04f484,0xbf3b0629,0x3eebcbbb,0x3e5101f1,0xbf633c5a,0xbf7a9c59,0xbd8a200a,0xbeab0201,0xbf7f6ac7,0xbf714c7a,
0x3f800000,0x3f764d97,0,0xbe8b9507,0x3f59f26a,0x3f2d1469,0xbf064b82,0xbf3ca003,0x3ee63375,0x3e419b37,0xbf64aa59,0xbf7b61fc,0xbdafb680,0xbeb554ec,0xbf7f0e58,0xbf6f6830,
0x3f800000,0x3f76167a,0,0xbe8d17c0,0x3f591e6a,0x3f2b561b,0xbf07a136,0xbf3e35c5,0x3ee0924f,0x3e3228d4,0xbf660f88,0xbf7c187a,0xbdd53db9,0xbebf926f,0xbf7e9bc9,0xbf6d67a1,
0x3f800000,0x3f75dec6,0,0xbe8e9a22,0x3f584853,0x3f299415,0xbf08f59b,0xbf3fc767,0x3edae880,0x3e22abb6,0xbf676bd8,0xbf7cbfc9,0xbdfab273,0xbec9b953,0xbf7e1324,0xbf6b4b0c,
0x3f800000,0x3f75a67b,0,0xbe901c2c,0x3f577026,0x3f27ce61,0xbf0a48ad,0xbf4154e1,0x3ed53641,0x3e1324ca,0xbf68bf3c,0xbf7d57de,0xbe1008b7,0xbed3c867,0xbf7d7474,0xbf6912ae,
0x3f800000,0x3f756d97,0,0xbe919ddd,0x3f5695e5,0x3f26050a,0xbf0b9a6b,0xbf42de29,0x3ecf7bca,0x3e039502,0xbf6a09a7,0xbf7de0b1,0xbe22abb6,0xbeddbe79,0xbf7cbfc9,0xbf66becc,
0x3f800000,0x3f75341d,0,0xbe931f35,0x3f55b993,0x3f24381a,0xbf0cead0,0xbf446337,0x3ec9b953,0x3de7fa9a,0xbf6b4b0c,0xbf7e5a38,0xbe354098,0xbee79a5d,0xbf7bf531,0xbf644fac,
0x3f800000,0x3f74fa0b,0,0xbe94a031,0x3f54db31,0x3f226799,0xbf0e39da,0xbf45e403,0x3ec3ef15,0x3dc8bd36,0xbf6c835e,0xbf7ec46d,0xbe47c5c2,0xbef15aea,0xbf7b14be,0xbf61c598,
0x3f800000,0x3f74bf62,0,0xbe9620d2,0x3f53fac3,0x3f209393,0xbf0f8784,0xbf476085,0x3ebe1d4a,0x3da973ba,0xbf6db293,0xbf7f1f49,0xbe5a3997,0xbefafef7,0xbf7a1e84,0xbf5f20db,
0x3f800000,0x3f748422,0,0xbe97a117,0x3f531849,0x3f1ebc12,0xbf10d3cd,0xbf48d8b3,0x3eb8442a,0x3d8a200a,0xbf6ed89e,0xbf7f6ac7,0xbe6c9a7f,0xbf0242b1,0xbf791298,0xbf5c61c7,
0x3f800000,0x3f74484b,0,0xbe9920fe,0x3f5233c6,0x3f1ce11f,0xbf121eb0,0xbf4a4c87,0x3eb263ef,0x3d55880e,0xbf6ff573,0xbf7fa6e3,0xbe7ee6e1,0xbf06f686,0xbf77f110,0xbf5988ad,
0x3f800000,0x3f740bdd,0,0xbe9aa086,0x3f514d3d,0x3f1b02c6,0xbf13682a,0xbf4bbbf8,0x3eac7cd4,0x3d16c32c,0xbf710908,0xbf7fd397,0xbe888e93,0xbf0b9a6b,0xbf76ba07,0xbf5695e5,
0x3f800000,0x3f73ced9,0,0xbe9c1faf,0x3f5064af,0x3f19210f,0xbf14b039,0xbf4d26fd,0x3ea68f12,0x3cafea69,0xbf721352,0xbf7ff0e3,0xbe919ddd,0xbf102dd5,0xbf756d97,0xbf5389c7,
0x3f800000,0x3f73913f,0,0xbe9d9e78,0x3f4f7a1f,0x3f173c07,0xbf15f6d9,0xbf4e8d90,0x3ea09ae5,0x3bc90f88,0xbf731447,0xbf7ffec4,0xbe9aa086,0xbf14b039,0xbf740bdd,0xbf5064af,
0x3f800000,0x3f73530e,0,0xbe9f1cdf,0x3f4e8d90,0x3f1553b7,0xbf173c07,0xbf4fefa8,0x3e9aa086,0xbc16cb58,0xbf740bdd,0xbf7ffd39,0xbea395c5,0xbf19210f,0xbf7294f8,0xbf4d26fd,
0x3f800000,0x3f731447,0,0xbea09ae5,0x3f4d9f02,0x3f13682a,0xbf187fc0,0xbf514d3d,0x3e94a031,0xbcc90ab0,0xbf74fa0b,0xbf7fec43,0xbeac7cd4,0xbf1d7fd1,0xbf710908,0xbf49d112,
0x3f800000,0x3f72d4eb,0,0xbea21887,0x3f4cae79,0x3f11796b,0xbf19c200,0xbf52a649,0x3e8e9a22,0xbd2351cb,0xbf75dec6,0xbf7fcbe2,0xbeb554ec,0xbf21cbfb,0xbf6f6830,0xbf466354,
0x3f800000,0x3f7294f8,0,0xbea395c5,0x3f4bbbf8,0x3f0f8784,0xbf1b02c6,0xbf53fac3,0x3e888e93,0xbd621469,0xbf76ba07,0xbf7f9c18,0xbebe1d4a,0xbf26050a,0xbf6db293,0xbf42de29,
0x3f800000,0x3f725470,0,0xbea5129f,0x3f4ac77f,0x3f0d9281,0xbf1c420c,0xbf554aa4,0x3e827dc0,0xbd9064b4,0xbf778bc5,0xbf7f5ce9,0xbec6d529,0xbf2a2a80,0xbf6be858,0xbf3f41fc,
0x3f800000,0x3f721352,0,0xbea68f12,0x3f49d112,0x3f0b9a6b,0xbf1d7fd1,0xbf5695e5,0x3e78cfcc,0xbdafb680,0xbf7853f8,0xbf7f0e58,0xbecf7bca,0xbf2e3bde,0xbf6a09a7,0xbf3b8f3b,
0x3f800000,0x3f71d19f,0,0xbea80b1f,0x3f48d8b3,0x3f099f4e,0xbf1ebc12,0xbf57dc7f,0x3e6c9a7f,0xbdcefdb7,0xbf791298,0xbf7eb069,0xbed8106b,0xbf3238aa,0xbf6816a8,0xbf37c655,
0x3f800000,0x3f718f57,0,0xbea986c4,0x3f47de65,0x3f07a136,0xbf1ff6cb,0xbf591e6a,0x3e605c13,0xbdee3876,0xbf79c79d,0xbf7e4323,0xbee0924f,0xbf36206c,0xbf660f88,0xbf33e7bc,
0x3f800000,0x3f714c7a,0,0xbeab0201,0x3f46e22a,0x3f05a02c,0xbf212ff9,0xbf5a5ba0,0x3e541501,0xbe06b26e,0xbf7a7302,0xbf7dc68c,0xbee900b7,0xbf39f2ac,0xbf63f473,0xbf2ff3e6,
0x3f800000,0x3f710908,0,0xbeac7cd4,0x3f45e403,0x3f039c3d,0xbf226799,0xbf5b941a,0x3e47c5c2,0xbe164083,0xbf7b14be,0xbf7d3aac,0xbef15aea,0xbf3daef9,0xbf61c598,0xbf2beb4a,
0x3f800000,0x3f70c501,0,0xbeadf73c,0x3f44e3f5,0x3f019573,0xbf239da9,0xbf5cc7d1,0x3e3b6ecf,0xbe25c58c,0xbf7baccd,0xbf7c9f8a,0xbef9a02d,0xbf4154e1,0xbf5f8327,0xbf27ce61,
0x3f800000,0x3f708066,0,0xbeaf713a,0x3f43e200,0x3eff17b2,0xbf24d225,0xbf5df6be,0x3e2f10a2,0xbe354098,0xbf7c3b28,0xbf7bf531,0xbf00e7e4,0xbf44e3f5,0xbf5d2d53,0xbf239da9,
0x3f800000,0x3f703b37,0,0xbeb0eacb,0x3f42de29,0x3efafef7,0xbf26050a,0xbf5f20db,0x3e22abb6,0xbe44b0b9,0xbf7cbfc9,0xbf7b3bab,0xbf04f484,0xbf485bca,0xbf5ac450,0xbf1f599f,
0x3f800000,0x3f6ff573,0,0xbeb263ef,0x3f41d870,0x3ef6e0cb,0xbf273656,0xbf604621,0x3e164083,0xbe541501,0xbf7d3aac,0xbf7a7302,0xbf08f59b,0xbf4bbbf8,0xbf584853,0xbf1b02c6,
0x3f800000,0x3f6faf1b,0,0xbeb3dca5,0x3f40d0da,0x3ef2bd43,0xbf286605,0xbf61668a,0x3e09cf86,0xbe636c83,0xbf7dabcc,0xbf799b43,0xbf0cead0,0xbf4f0417,0xbf55b993,0xbf16999f,
0x3f800000,0x3f6f6830,0,0xbeb554ec,0x3f3fc767,0x3eee9479,0xbf299415,0xbf628210,0x3dfab273,0xbe72b651,0xbf7e1324,0xbf78b47b,0xbf10d3cd,0xbf5233c6,0xbf531849,0xbf121eb0,
0x3f800000,0x3f6f20b0,0,0xbeb6ccc3,0x3f3ebc1b,0x3eea6681,0xbf2ac082,0xbf6398ac,0x3de1bc2e,0xbe80f8c0,0xbf7e70b0,0xbf77beb7,0xbf14b039,0xbf554aa4,0xbf5064af,0xbf0d9281,
0x3f800000,0x3f6ed89e,0,0xbeb8442a,0x3f3daef9,0x3ee63375,0xbf2beb4a,0xbf64aa59,0x3dc8bd36,0xbe888e93,0xbf7ec46d,0xbf76ba07,0xbf187fc0,0xbf584853,0xbf4d9f02,0xbf08f59b,
0x3f800000,0x3f6e8ff8,0,0xbeb9bb1e,0x3f3ca003,0x3ee1fb6a,0xbf2d1469,0xbf65b710,0x3dafb680,0xbe901c2c,0xbf7f0e58,0xbf75a67b,0xbf1c420c,0xbf5b2c79,0xbf4ac77f,0xbf044889,
0x3f800000,0x3f6e46be,0,0xbebb31a0,0x3f3b8f3b,0x3eddbe79,0xbf2e3bde,0xbf66becc,0x3d96a905,0xbe97a117,0xbf7f4e6d,0xbf748422,0xbf1ff6cb,0xbf5df6be,0xbf47de65,0xbeff17b2,
0x3f800000,0x3f6dfcf2,0,0xbebca7af,0x3f3a7ca4,0x3ed97cb9,0xbf2f61a5,0xbf67c187,0x3d7b2b74,0xbe9f1cdf,0xbf7f84ab,0xbf73530e,0xbf239da9,0xbf60a6cf,0xbf44e3f5,0xbef58035,
0x3f800000,0x3f6db293,0,0xbebe1d4a,0x3f396842,0x3ed53641,0xbf3085bb,0xbf68bf3c,0x3d48fb30,0xbea68f12,0xbf7fb10f,0xbf721352,0xbf273656,0xbf633c5a,0xbf41d870,0xbeebcbbb,
0x3f800000,0x3f6d67a1,0,0xbebf926f,0x3f385216,0x3ed0eb2a,0xbf31a81d,0xbf69b7e4,0x3d16c32c,0xbeadf73c,0xbf7fd397,0xbf70c501,0xbf2ac082,0xbf65b710,0xbf3ebc1b,0xbee1fb6a,
0x3f800000,0x3f6d1c1d,0,0xbec1071e,0x3f373a23,0x3ecc9b8b,0xbf32c8c9,0xbf6aab7b,0x3cc90ab0,0xbeb554ec,0xbf7fec43,0xbf6f6830,0xbf2e3bde,0xbf6816a8,0xbf3b8f3b,0xbed8106b,
0x3f800000,0x3f6cd007,0,0xbec27b55,0x3f36206c,0x3ec8477c,0xbf33e7bc,0xbf6b99fb,0x3c490e90,0xbebca7af,0xbf7ffb11,0xbf6dfcf2,0xbf31a81d,0xbf6a5ad9,0xbf385216,0xbece0bea,
0x3f800000,0x3f6c835e,0,0xbec3ef15,0x3f3504f3,0x3ec3ef15,0xbf3504f3,0xbf6c835e,0,0xbec3ef15,0xbf800000,0xbf6c835e,0xbf3504f3,0xbf6c835e,0xbf3504f3,0xbec3ef15,
0x3f800000,0x3f6c3624,0,0xbec5625c,0x3f33e7bc,0x3ebf926f,0xbf36206c,0xbf6d67a1,0xbc490e90,0xbecb2aae,0xbf7ffb11,0xbf6afb8c,0xbf385216,0xbf6e8ff8,0xbf31a81d,0xbeb9bb1e,
0x3f800000,0x3f6be858,0,0xbec6d529,0x3f32c8c9,0x3ebb31a0,0xbf373a23,0xbf6e46be,0xbcc90ab0,0xbed25a09,0xbf7fec43,0xbf696591,0xbf3b8f3b,0xbf708066,0xbf2e3bde,0xbeaf713a,
0x3f800000,0x3f6b99fb,0,0xbec8477c,0x3f31a81d,0x3eb6ccc3,0xbf385216,0xbf6f20b0,0xbd16c32c,0xbed97cb9,0xbf7fd397,0xbf67c187,0xbf3ebc1b,0xbf725470,0xbf2ac082,0xbea5129f,
0x3f800000,0x3f6b4b0c,0,0xbec9b953,0x3f3085bb,0x3eb263ef,0xbf396842,0xbf6ff573,0xbd48fb30,0xbee0924f,0xbf7fb10f,0xbf660f88,0xbf41d870,0xbf740bdd,0xbf273656,0xbe9aa086,
0x3f800000,0x3f6afb8c,0,0xbecb2aae,0x3f2f61a5,0x3eadf73c,0xbf3a7ca4,0xbf70c501,0xbd7b2b74,0xbee79a5d,0xbf7f84ab,0xbf644fac,0xbf44e3f5,0xbf75a67b,0xbf239da9,0xbe901c2c,
0x3f800000,0x3f6aab7b,0,0xbecc9b8b,0x3f2e3bde,0x3ea986c4,0xbf3b8f3b,0xbf718f57,0xbd96a905,0xbeee9479,0xbf7f4e6d,0xbf628210,0xbf47de65,0xbf772417,0xbf1ff6cb,0xbe8586ce,
0x3f800000,0x3f6a5ad9,0,0xbece0bea,0x3f2d1469,0x3ea5129f,0xbf3ca003,0xbf725470,0xbdafb680,0xbef58035,0xbf7f0e58,0xbf60a6cf,0xbf4ac77f,0xbf788486,0xbf1c420c,0xbe75c35a,
0x3f800000,0x3f6a09a7,0,0xbecf7bca,0x3f2beb4a,0x3ea09ae5,0xbf3daef9,0xbf731447,0xbdc8bd36,0xbefc5d27,0xbf7ec46d,0xbf5ebe05,0xbf4d9f02,0xbf79c79d,0xbf187fc0,0xbe605c13,
0x3f800000,0x3f69b7e4,0,0xbed0eb2a,0x3f2ac082,0x3e9c1faf,0xbf3ebc1b,0xbf73ced9,0xbde1bc2e,0xbf019573,0xbf7e70b0,0xbf5cc7d1,0xbf5064af,0xbf7aed37,0xbf14b039,0xbe4ada4f,
0x3f800000,0x3f696591,0,0xbed25a09,0x3f299415,0x3e97a117,0xbf3fc767,0xbf748422,0xbdfab273,0xbf04f484,0xbf7e1324,0xbf5ac450,0xbf531849,0xbf7bf531,0xbf10d3cd,0xbe354098,
0x3f800000,0x3f6912ae,0,0xbed3c867,0x3f286605,0x3e931f35,0xbf40d0da,0xbf75341d,0xbe09cf86,0xbf084b92,0xbf7dabcc,0xbf58b3a1,0xbf55b993,0xbf7cdf6c,0xbf0cead0,0xbe1f917b,
0x3f800000,0x3f68bf3c,0,0xbed53641,0x3f273656,0x3e8e9a22,0xbf41d870,0xbf75dec6,0xbe164083,0xbf0b9a6b,0xbf7d3aac,0xbf5695e5,0xbf584853,0xbf7dabcc,0xbf08f59b,0xbe09cf86,
0x3f800000,0x3f686b39,0,0xbed6a399,0x3f26050a,0x3e8a11f7,0xbf42de29,0xbf76841b,0xbe22abb6,0xbf0ee0db,0xbf7cbfc9,0xbf546b3b,0xbf5ac450,0xbf7e5a38,0xbf04f484,0xbde7fa9a,
0x3f800000,0x3f6816a8,0,0xbed8106b,0x3f24d225,0x3e8586ce,0xbf43e200,0xbf772417,0xbe2f10a2,0xbf121eb0,0xbf7c3b28,0xbf5233c6,0xbf5d2d53,0xbf7eea9d,0xbf00e7e4,0xbdbc3ac3,
0x3f800000,0x3f67c187,0,0xbed97cb9,0x3f239da9,0x3e80f8c0,0xbf44e3f5,0xbf77beb7,0xbe3b6ecf,0xbf1553b7,0xbf7baccd,0xbf4fefa8,0xbf5f8327,0xbf7f5ce9,0xbef9a02d,0xbd9064b4,
0x3f800000,0x3f676bd8,0,0xbedae880,0x3f226799,0x3e78cfcc,0xbf45e403,0xbf7853f8,0xbe47c5c2,0xbf187fc0,0xbf7b14be,0xbf4d9f02,0xbf61c598,0xbf7fb10f,0xbef15aea,0xbd48fb30,
0x3f800000,0x3f671599,0,0xbedc53c1,0x3f212ff9,0x3e6fa8b2,0xbf46e22a,0xbf78e3d6,0xbe541501,0xbf1ba299,0xbf7a7302,0xbf4b41fa,0xbf63f473,0xbf7fe705,0xbee900b7,0xbce22a7a,
0x3f800000,0x3f66becc,0,0xbeddbe79,0x3f1ff6cb,0x3e667c66,0xbf47de65,0xbf796e4e,0xbe605c13,0xbf1ebc12,0xbf79c79d,0xbf48d8b3,0xbf660f88,0xbf7ffec4,0xbee0924f,0xbbc90f88,
0x3f800000,0x3f666771,0,0xbedf28a9,0x3f1ebc12,0x3e5d4b1a,0xbf48d8b3,0xbf79f35e,0xbe6c9a7f,0xbf21cbfb,0xbf791298,0xbf466354,0xbf6816a8,0xbf7ff84a,0xbed8106b,0x3c7b514b,
0x3f800000,0x3f660f88,0,0xbee0924f,0x3f1d7fd1,0x3e541501,0xbf49d112,0xbf7a7302,0xbe78cfcc,0xbf24d225,0xbf7853f8,0xbf43e200,0xbf6a09a7,0xbf7fd397,0xbecf7bca,0x3d16c32c,
0x3f800000,0x3f65b710,0,0xbee1fb6a,0x3f1c420c,0x3e4ada4f,0xbf4ac77f,0xbf7aed37,0xbe827dc0,0xbf27ce61,0xbf778bc5,0xbf4154e1,0xbf6be858,0xbf7f90b1,0xbec6d529,0x3d6ea038,
0x3f800000,0x3f655e0b,0,0xbee363fa,0x3f1b02c6,0x3e419b37,0xbf4bbbf8,0xbf7b61fc,0xbe888e93,0xbf2ac082,0xbf76ba07,0xbf3ebc1b,0xbf6db293,0xbf7f2f9d,0xbebe1d4a,0x3da3308c,
0x3f800000,0x3f650479,0,0xbee4cbfe,0x3f19c200,0x3e3857ec,0xbf4cae79,0xbf7bd14d,0xbe8e9a22,0xbf2da859,0xbf75dec6,0xbf3c17d9,0xbf6f6830,0xbf7eb069,0xbeb554ec,0x3dcefdb7,
0x3f800000,0x3f64aa59,0,0xbee63375,0x3f187fc0,0x3e2f10a2,0xbf4d9f02,0xbf7c3b28,0xbe94a031,0xbf3085bb,0xbf74fa0b,0xbf396842,0xbf710908,0xbf7e1324,0xbeac7cd4,0x3dfab273,
0x3f800000,0x3f644fac,0,0xbee79a5d,0x3f173c07,0x3e25c58c,0xbf4e8d90,0xbf7c9f8a,0xbe9aa086,0xbf33587a,0xbf740bdd,0xbf36ad7f,0xbf7294f8,0xbf7d57de,0xbea395c5,0x3e1324ca,
0x3f800000,0x3f63f473,0,0xbee900b7,0x3f15f6d9,0x3e1c76de,0xbf4f7a1f,0xbf7cfe73,0xbea09ae5,0xbf36206c,0xbf731447,0xbf33e7bc,0xbf740bdd,0xbf7c7eb0,0xbe9aa086,0x3e28defc,
0x3f800000,0x3f6398ac,0,0xbeea6681,0x3f14b039,0x3e1324ca,0xbf5064af,0xbf7d57de,0xbea68f12,0xbf38dd65,0xbf721352,0xbf311722,0xbf756d97,0xbf7b87b2,0xbe919ddd,0x3e3e853e,
0x3f800000,0x3f633c5a,0,0xbeebcbbb,0x3f13682a,0x3e09cf86,0xbf514d3d,0xbf7dabcc,0xbeac7cd4,0xbf3b8f3b,0xbf710908,0xbf2e3bde,0xbf76ba07,0xbf7a7302,0xbe888e93,0x3e541501,
0x3f800000,0x3f62df7b,0,0xbeed3063,0x3f121eb0,0x3e007745,0xbf5233c6,0xbf7dfa38,0xbeb263ef,0xbf3e35c5,0xbf6ff573,0xbf2b561b,0xbf77f110,0xbf7940c0,0xbe7ee6e1,0x3e698bba,
0x3f800000,0x3f628210,0,0xbeee9479,0x3f10d3cd,0x3dee3876,0xbf531849,0xbf7e4323,0xbeb8442a,0xbf40d0da,0xbf6ed89e,0xbf286605,0xbf791298,0xbf77f110,0xbe6c9a7f,0x3e7ee6e1,
0x3f800000,0x3f62241a,0,0xbeeff7fb,0x3f0f8784,0x3ddb7d37,0xbf53fac3,0xbf7e868b,0xbebe1d4a,0xbf436051,0xbf6db293,0xbf256bcb,0xbf7a1e84,0xbf76841b,0xbe5a3997,0x3e8a11f7,
0x3f800000,0x3f61c598,0,0xbef15aea,0x3f0e39da,0x3dc8bd36,0xbf54db31,0xbf7ec46d,0xbec3ef15,0xbf45e403,0xbf6c835e,0xbf226799,0xbf7b14be,0xbf74fa0b,0xbe47c5c2,0x3e94a031,
0x3f800000,0x3f61668a,0,0xbef2bd43,0x3f0cead0,0x3db5f8da,0xbf55b993,0xbf7efcc9,0xbec9b953,0xbf485bca,0xbf6b4b0c,0xbf1f599f,0xbf7bf531,0xbf73530e,0xbe354098,0x3e9f1cdf,
0x3f800000,0x3f6106f2,0,0xbef41f07,0x3f0b9a6b,0x3da3308c,0xbf5695e5,0xbf7f2f9d,0xbecf7bca,0xbf4ac77f,0xbf6a09a7,0xbf1c420c,0xbf7cbfc9,0xbf718f57,0xbe22abb6,0x3ea986c4,
0x3f800000,0x3f60a6cf,0,0xbef58035,0x3f0a48ad,0x3d9064b4,0xbf577026,0xbf7f5ce9,0xbed53641,0xbf4d26fd,0xbf68bf3c,0xbf19210f,0xbf7d7474,0xbf6faf1b,0xbe1008b7,0x3eb3dca5,
0x3f800000,0x3f604621,0,0xbef6e0cb,0x3f08f59b,0x3d7b2b74,0xbf584853,0xbf7f84ab,0xbedae880,0xbf4f7a1f,0xbf676bd8,0xbf15f6d9,0xbf7e1324,0xbf6db293,0xbdfab273,0x3ebe1d4a,
0x3f800000,0x3f5fe4e9,0,0xbef840c8,0x3f07a136,0x3d55880e,0xbf591e6a,0xbf7fa6e3,0xbee0924f,0xbf51c0c2,0xbf660f88,0xbf12c39a,0xbf7e9bc9,0xbf6b99fb,0xbdd53db9,0x3ec8477c,
0x3f800000,0x3f5f8327,0,0xbef9a02d,0x3f064b82,0x3d2fe007,0xbf59f26a,0xbf7fc38f,0xbee63375,0xbf53fac3,0xbf64aa59,0xbf0f8784,0xbf7f0e58,0xbf696591,0xbdafb680,0x3ed25a09,
0x3f800000,0x3f5f20db,0,0xbefafef7,0x3f04f484,0x3d0a342f,0xbf5ac450,0xbf7fdaaf,0xbeebcbbb,0xbf5627fe,0xbf633c5a,0xbf0c42c9,0xbf7f6ac7,0xbf671599,0xbd8a200a,0x3edc53c1,
0x3f800000,0x3f5ebe05,0,0xbefc5d27,0x3f039c3d,0x3cc90ab0,0xbf5b941a,0xbf7fec43,0xbef15aea,0xbf584853,0xbf61c598,0xbf08f59b,0xbf7fb10f,0xbf64aa59,0xbd48fb30,0x3ee63375,
0x3f800000,0x3f5e5aa6,0,0xbefdbabb,0x3f0242b1,0x3c7b514b,0xbf5c61c7,0xbf7ff84a,0xbef6e0cb,0xbf5a5ba0,0xbf604621,0xbf05a02c,0xbf7fe129,0xbf62241a,0xbcfb49ba,0x3eeff7fb,
0x3f800000,0x3f5df6be,0,0xbeff17b2,0x3f00e7e4,0x3bc90f88,0xbf5d2d53,0xbf7ffec4,0xbefc5d27,0xbf5c61c7,0xbf5ebe05,0xbf0242b1,0xbf7ffb11,0xbf5f8327,0xbc490e90,0x3ef9a02d,
0x3f800000,0x3f5d924d,0,0xbf003a06,0x3eff17b2,0xbb490fc6,0xbf5df6be,0xbf7fffb1,0xbf00e7e4,0xbf5e5aa6,0xbf5d2d53,0xbefdbabb,0xbf7ffec4,0xbf5cc7d1,0x3bc90f88,0x3f019573,
0x3f800000,0x3f5d2d53,0,0xbf00e7e4,0x3efc5d27,0xbc490e90,0xbf5ebe05,0xbf7ffb11,0xbf039c3d,0xbf604621,0xbf5b941a,0xbef6e0cb,0xbf7fec43,0xbf59f26a,0x3cc90ab0,0x3f064b82,
0x3f800000,0x3f5cc7d1,0,0xbf019573,0x3ef9a02d,0xbcafea69,0xbf5f8327,0xbf7ff0e3,0xbf064b82,0xbf62241a,0xbf59f26a,0xbeeff7fb,0xbf7fc38f,0xbf570348,0x3d2fe007,0x3f0af1b7,
0x3f800000,0x3f5c61c7,0,0xbf0242b1,0x3ef6e0cb,0xbcfb49ba,0xbf604621,0xbf7fe129,0xbf08f59b,0xbf63f473,0xbf584853,0xbee900b7,0xbf7f84ab,0xbf53fac3,0x3d7b2b74,0x3f0f8784,
0x3f800000,0x3f5bfb34,0,0xbf02ef9f,0x3ef41f07,0xbd2351cb,0xbf6106f2,0xbf7fcbe2,0xbf0b9a6b,0xbf65b710,0xbf5695e5,0xbee1fb6a,0xbf7f2f9d,0xbf50d937,0x3da3308c,0x3f140c5f,
0x3f800000,0x3f5b941a,0,0xbf039c3d,0x3ef15aea,0xbd48fb30,0xbf61c598,0xbf7fb10f,0xbf0e39da,0xbf676bd8,0xbf54db31,0xbedae880,0xbf7ec46d,0xbf4d9f02,0x3dc8bd36,0x3f187fc0,
0x3f800000,0x3f5b2c79,0,0xbf044889,0x3eee9479,0xbd6ea038,0xbf628210,0xbf7f90b1,0xbf10d3cd,0xbf6912ae,0xbf531849,0xbed3c867,0xbf7e4323,0xbf4a4c87,0x3dee3876,0x3f1ce11f,
0x3f800000,0x3f5ac450,0,0xbf04f484,0x3eebcbbb,0xbd8a200a,0xbf633c5a,0xbf7f6ac7,0xbf13682a,0xbf6aab7b,0xbf514d3d,0xbecc9b8b,0xbf7dabcc,0xbf46e22a,0x3e09cf86,0x3f212ff9,
0x3f800000,0x3f5a5ba0,0,0xbf05a02c,0x3ee900b7,0xbd9cecf9,0xbf63f473,0xbf7f3f54,0xbf15f6d9,0xbf6c3624,0xbf4f7a1f,0xbec5625c,0xbf7cfe73,0xbf436051,0x3e1c76de,0x3f256bcb,
0x3f800000,0x3f59f26a,0,0xbf064b82,0x3ee63375,0xbdafb680,0xbf64aa59,0xbf7f0e58,0xbf187fc0,0xbf6db293,0xbf4d9f02,0xbebe1d4a,0xbf7c3b28,0xbf3fc767,0x3e2f10a2,0x3f299415,
0x3f800000,0x3f5988ad,0,0xbf06f686,0x3ee363fa,0xbdc27c39,0xbf655e0b,0xbf7ed7d4,0xbf1b02c6,0xbf6f20b0,0xbf4bbbf8,0xbeb6ccc3,0xbf7b61fc,0xbf3c17d9,0x3e419b37,0x3f2da859,
0x3f800000,0x3f591e6a,0,0xbf07a136,0x3ee0924f,0xbdd53db9,0xbf660f88,0xbf7e9bc9,0xbf1d7fd1,0xbf708066,0xbf49d112,0xbeaf713a,0xbf7a7302,0xbf385216,0x3e541501,0x3f31a81d,
0x3f800000,0x3f58b3a1,0,0xbf084b92,0x3eddbe79,0xbde7fa9a,0xbf66becc,0xbf7e5a38,0xbf1ff6cb,0xbf71d19f,0xbf47de65,0xbea80b1f,0xbf796e4e,0xbf34768f,0x3e667c66,0x3f3592e7,
0x3f800000,0x3f584853,0,0xbf08f59b,0x3edae880,0xbdfab273,0xbf676bd8,0xbf7e1324,0xbf226799,0xbf731447,0xbf45e403,0xbea09ae5,0xbf7853f8,0xbf3085bb,0x3e78cfcc,0x3f396842,
0x3f800000,0x3f57dc7f,0,0xbf099f4e,0x3ed8106b,0xbe06b26e,0xbf6816a8,0xbf7dc68c,0xbf24d225,0xbf74484b,0xbf43e200,0xbe9920fe,0xbf772417,0xbf2c800f,0x3e8586ce,0x3f3d27b8,
0x3f800000,0x3f577026,0,0xbf0a48ad,0x3ed53641,0xbe1008b7,0xbf68bf3c,0xbf7d7474,0xbf273656,0xbf756d97,0xbf41d870,0xbe919ddd,0xbf75dec6,0xbf286605,0x3e8e9a22,0x3f40d0da,
0x3f800000,0x3f570348,0,0xbf0af1b7,0x3ed25a09,0xbe195be0,0xbf696591,0xbf7d1cdd,0xbf299415,0xbf76841b,0xbf3fc767,0xbe8a11f7,0xbf748422,0xbf24381a,0x3e97a117,0x3f446337,
0x3f800000,0x3f5695e5,0,0xbf0b9a6b,0x3ecf7bca,0xbe22abb6,0xbf6a09a7,0xbf7cbfc9,0xbf2beb4a,0xbf778bc5,0xbf3daef9,0xbe827dc0,0xbf731447,0xbf1ff6cb,0x3ea09ae5,0x3f47de65,
0x3f800000,0x3f5627fe,0,0xbf0c42c9,0x3ecc9b8b,0xbe2bf804,0xbf6aab7b,0xbf7c5d3a,0xbf2e3bde,0xbf788486,0xbf3b8f3b,0xbe75c35a,0xbf718f57,0xbf1ba299,0x3ea986c4,0x3f4b41fa,
0x3f800000,0x3f55b993,0,0xbf0cead0,0x3ec9b953,0xbe354098,0xbf6b4b0c,0xbf7bf531,0xbf3085bb,0xbf796e4e,0xbf396842,0xbe667c66,0xbf6ff573,0xbf173c07,0x3eb263ef,0x3f4e8d90,
0x3f800000,0x3f554aa4,0,0xbf0d9281,0x3ec6d529,0xbe3e853e,0xbf6be858,0xbf7b87b2,0xbf32c8c9,0xbf7a4910,0xbf373a23,0xbe57278f,0xbf6e46be,0xbf12c39a,0x3ebb31a0,0x3f51c0c2,
0x3f800000,0x3f54db31,0,0xbf0e39da,0x3ec3ef15,0xbe47c5c2,0xbf6c835e,0xbf7b14be,0xbf3504f3,0xbf7b14be,0xbf3504f3,0xbe47c5c2,0xbf6c835e,0xbf0e39da,0x3ec3ef15,0x3f54db31,
0x3f800000,0x3f546b3b,0,0xbf0ee0db,0x3ec1071e,0xbe5101f1,0xbf6d1c1d,0xbf7a9c59,0xbf373a23,0xbf7bd14d,0xbf32c8c9,0xbe3857ec,0xbf6aab7b,0xbf099f4e,0x3ecc9b8b,0x3f57dc7f,
0x3f800000,0x3f53fac3,0,0xbf0f8784,0x3ebe1d4a,0xbe5a3997,0xbf6db293,0xbf7a1e84,0xbf396842,0xbf7c7eb0,0xbf3085bb,0xbe28defc,0xbf68bf3c,0xbf04f484,0x3ed53641,0x3f5ac450,
0x3f800000,0x3f5389c7,0,0xbf102dd5,0x3ebb31a0,0xbe636c83,0xbf6e46be,0xbf799b43,0xbf3b8f3b,0xbf7d1cdd,0xbf2e3bde,0xbe195be0,0xbf66becc,0xbf003a06,0x3eddbe79,0x3f5d924d,
0x3f800000,0x3f531849,0,0xbf10d3cd,0x3eb8442a,0xbe6c9a7f,0xbf6ed89e,0xbf791298,0xbf3daef9,0xbf7dabcc,0xbf2beb4a,0xbe09cf86,0xbf64aa59,0xbef6e0cb,0x3ee63375,0x3f604621,
0x3f800000,0x3f52a649,0,0xbf11796b,0x3eb554ec,0xbe75c35a,0xbf6f6830,0xbf788486,0xbf3fc767,0xbf7e2b72,0xbf299415,0xbdf475c0,0xbf628210,0xbeed3063,0x3eee9479,0x3f62df7b,
0x3f800000,0x3f5233c6,0,0xbf121eb0,0x3eb263ef,0xbe7ee6e1,0xbf6ff573,0xbf77f110,0xbf41d870,0xbf7e9bc9,0xbf273656,0xbdd53db9,0xbf604621,0xbee363fa,0x3ef6e0cb,0x3f655e0b,
0x3f800000,0x3f51c0c2,0,0xbf12c39a,0x3eaf713a,0xbe840270,0xbf708066,0xbf77583a,0xbf43e200,0xbf7efcc9,0xbf24d225,0xbdb5f8da,0xbf5df6be,0xbed97cb9,0x3eff17b2,0x3f67c187,
0x3f800000,0x3f514d3d,0,0xbf13682a,0x3eac7cd4,0xbe888e93,0xbf710908,0xbf76ba07,0xbf45e403,0xbf7f4e6d,0xbf226799,0xbd96a905,0xbf5b941a,0xbecf7bca,0x3f039c3d,0x3f6a09a7,
0x3f800000,0x3f50d937,0,0xbf140c5f,0x3ea986c4,0xbe8d17c0,0xbf718f57,0xbf76167a,0xbf47de65,0xbf7f90b1,0xbf1ff6cb,0xbd6ea038,0xbf591e6a,0xbec5625c,0x3f07a136,0x3f6c3624,
0x3f800000,0x3f5064af,0,0xbf14b039,0x3ea68f12,0xbe919ddd,0xbf721352,0xbf756d97,0xbf49d112,0xbf7fc38f,0xbf1d7fd1,0xbd2fe007,0xbf5695e5,0xbebb31a0,0x3f0b9a6b,0x3f6e46be,
0x3f800000,0x3f4fefa8,0,0xbf1553b7,0x3ea395c5,0xbe9620d2,0xbf7294f8,0xbf74bf62,0xbf4bbbf8,0xbf7fe705,0xbf1b02c6,0xbce22a7a,0xbf53fac3,0xbeb0eacb,0x3f0f8784,0x3f703b37,
0x3f800000,0x3f4f7a1f,0,0xbf15f6d9,0x3ea09ae5,0xbe9aa086,0xbf731447,0xbf740bdd,0xbf4d9f02,0xbf7ffb11,0xbf187fc0,0xbc490e90,0xbf514d3d,0xbea68f12,0x3f13682a,0x3f721352,
0x3f800000,0x3f4f0417,0,0xbf16999f,0x3e9d9e78,0xbe9f1cdf,0xbf73913f,0xbf73530e,0xbf4f7a1f,0xbf7fffb1,0xbf15f6d9,0x3b490fc6,0xbf4e8d90,0xbe9c1faf,0x3f173c07,0x3f73ced9,
0x3f800000,0x3f4e8d90,0,0xbf173c07,0x3e9aa086,0xbea395c5,0xbf740bdd,0xbf7294f8,0xbf514d3d,0xbf7ff4e6,0xbf13682a,0x3c96c9b6,0xbf4bbbf8,0xbe919ddd,0x3f1b02c6,0x3f756d97,
0x3f800000,0x3f4e1689,0,0xbf17de12,0x3e97a117,0xbea80b1f,0xbf748422,0xbf71d19f,0xbf531849,0xbf7fdaaf,0xbf10d3cd,0x3d0a342f,0xbf48d8b3,0xbe870ada,0x3f1ebc12,0x3f76ef5b,
0x3f800000,0x3f4d9f02,0,0xbf187fc0,0x3e94a031,0xbeac7cd4,0xbf74fa0b,0xbf710908,0xbf54db31,0xbf7fb10f,0xbf0e39da,0x3d48fb30,0xbf45e403,0xbe78cfcc,0x3f226799,0x3f7853f8,
0x3f800000,0x3f4d26fd,0,0xbf19210f,0x3e919ddd,0xbeb0eacb,0xbf756d97,0xbf703b37,0xbf5695e5,0xbf7f7808,0xbf0b9a6b,0x3d83db0a,0xbf42de29,0xbe636c83,0x3f26050a,0x3f799b43,
0x3f800000,0x3f4cae79,0,0xbf19c200,0x3e8e9a22,0xbeb554ec,0xbf75dec6,0xbf6f6830,0xbf584853,0xbf7f2f9d,0xbf08f59b,0x3da3308c,0xbf3fc767,0xbe4dee60,0x3f299415,0x3f7ac516,
0x3f800000,0x3f4c3578,0,0xbf1a6293,0x3e8b9507,0xbeb9bb1e,0xbf764d97,0xbf6e8ff8,0xbf59f26a,0xbf7ed7d4,0xbf064b82,0x3dc27c39,0xbf3ca003,0xbe3857ec,0x3f2d1469,0x3f7bd14d,
0x3f800000,0x3f4bbbf8,0,0xbf1b02c6,0x3e888e93,0xbebe1d4a,0xbf76ba07,0xbf6db293,0xbf5b941a,0xbf7e70b0,0xbf039c3d,0x3de1bc2e,0xbf396842,0xbe22abb6,0x3f3085bb,0x3f7cbfc9,
0x3f800000,0x3f4b41fa,0,0xbf1ba299,0x3e8586ce,0xbec27b55,0xbf772417,0xbf6cd007,0xbf5d2d53,0xbf7dfa38,0xbf00e7e4,0x3e007745,0xbf36206c,0xbe0cec4a,0x3f33e7bc,0x3f7d906e,
0x3f800000,0x3f4ac77f,0,0xbf1c420c,0x3e827dc0,0xbec6d529,0xbf778bc5,0xbf6be858,0xbf5ebe05,0xbf7d7474,0xbefc5d27,0x3e1008b7,0xbf32c8c9,0xbdee3876,0x3f373a23,0x3f7e4323,
0x3f800000,0x3f4a4c87,0,0xbf1ce11f,0x3e7ee6e1,0xbecb2aae,0xbf77f110,0xbf6afb8c,0xbf604621,0xbf7cdf6c,0xbef6e0cb,0x3e1f917b,0xbf2f61a5,0xbdc27c39,0x3f3a7ca4,0x3f7ed7d4,
0x3f800000,0x3f49d112,0,0xbf1d7fd1,0x3e78cfcc,0xbecf7bca,0xbf7853f8,0xbf6a09a7,0xbf61c598,0xbf7c3b28,0xbef15aea,0x3e2f10a2,0xbf2beb4a,0xbd96a905,0x3f3daef9,0x3f7f4e6d,
0x3f800000,0x3f495521,0,0xbf1e1e22,0x3e72b651,0xbed3c867,0xbf78b47b,0xbf6912ae,0xbf633c5a,0xbf7b87b2,0xbeebcbbb,0x3e3e853e,0xbf286605,0xbd55880e,0x3f40d0da,0x3f7fa6e3,
0x3f800000,0x3f48d8b3,0,0xbf1ebc12,0x3e6c9a7f,0xbed8106b,0xbf791298,0xbf6816a8,0xbf64aa59,0xbf7ac516,0xbee63375,0x3e4dee60,0xbf24d225,0xbcfb49ba,0x3f43e200,0x3f7fe129,
0x3f800000,0x3f485bca,0,0xbf1f599f,0x3e667c66,0xbedc53c1,0xbf796e4e,0xbf671599,0xbf660f88,0xbf79f35e,0xbee0924f,0x3e5d4b1a,0xbf212ff9,0xbc16cb58,0x3f46e22a,0x3f7ffd39,
0x3f800000,0x3f47de65,0,0xbf1ff6cb,0x3e605c13,0xbee0924f,0xbf79c79d,0xbf660f88,0xbf676bd8,0xbf791298,0xbedae880,0x3e6c9a7f,0xbf1d7fd1,0x3c490e90,0x3f49d112,0x3f7ffb11,
0x3f800000,0x3f476085,0,0xbf209393,0x3e5a3997,0xbee4cbfe,0xbf7a1e84,0xbf650479,0xbf68bf3c,0xbf7822d1,0xbed53641,0x3e7bdba4,0xbf19c200,0x3d0a342f,0x3f4cae79,0x3f7fdaaf,
0x3f800000,0x3f46e22a,0,0xbf212ff9,0x3e541501,0xbee900b7,0xbf7a7302,0xbf63f473,0xbf6a09a7,0xbf772417,0xbecf7bca,0x3e8586ce,0xbf15f6d9,0x3d621469,0x3f4f7a1f,0x3f7f9c18,
0x3f800000,0x3f466354,0,0xbf21cbfb,0x3e4dee60,0xbeed3063,0xbf7ac516,0xbf62df7b,0xbf6b4b0c,0xbf76167a,0xbec9b953,0x3e8d17c0,0xbf121eb0,0x3d9cecf9,0x3f5233c6,0x3f7f3f54,
0x3f800000,0x3f45e403,0,0xbf226799,0x3e47c5c2,0xbef15aea,0xbf7b14be,0xbf61c598,0xbf6c835e,0xbf74fa0b,0xbec3ef15,0x3e94a031,0xbf0e39da,0x3dc8bd36,0x3f54db31,0x3f7ec46d,
0x3f800000,0x3f456439,0,0xbf2302d3,0x3e419b37,0xbef58035,0xbf7b61fc,0xbf60a6cf,0xbf6db293,0xbf73ced9,0xbebe1d4a,0x3e9c1faf,0xbf0a48ad,0x3df475c0,0x3f577026,0x3f7e2b72,
0x3f800000,0x3f44e3f5,0,0xbf239da9,0x3e3b6ecf,0xbef9a02d,0xbf7baccd,0xbf5f8327,0xbf6ed89e,0xbf7294f8,0xbeb8442a,0x3ea395c5,0xbf064b82,0x3e1008b7,0x3f59f26a,0x3f7d7474,
0x3f800000,0x3f446337,0,0xbf24381a,0x3e354098,0xbefdbabb,0xbf7bf531,0xbf5e5aa6,0xbf6ff573,0xbf714c7a,0xbeb263ef,0x3eab0201,0xbf0242b1,0x3e25c58c,0x3f5c61c7,0x3f7c9f8a,
0x3f800000,0x3f43e200,0,0xbf24d225,0x3e2f10a2,0xbf00e7e4,0xbf7c3b28,0xbf5d2d53,0xbf710908,0xbf6ff573,0xbeac7cd4,0x3eb263ef,0xbefc5d27,0x3e3b6ecf,0x3f5ebe05,0x3f7baccd,
0x3f800000,0x3f436051,0,0xbf256bcb,0x3e28defc,0xbf02ef9f,0xbf7c7eb0,0xbf5bfb34,0xbf721352,0xbf6e8ff8,0xbea68f12,0x3eb9bb1e,0xbef41f07,0x3e5101f1,0x3f6106f2,0x3f7a9c59,
0x3f800000,0x3f42de29,0,0xbf26050a,0x3e22abb6,0xbf04f484,0xbf7cbfc9,0xbf5ac450,0xbf731447,0xbf6d1c1d,0xbea09ae5,0x3ec1071e,0xbeebcbbb,0x3e667c66,0x3f633c5a,0x3f796e4e,
0x3f800000,0x3f425b89,0,0xbf269de3,0x3e1c76de,0xbf06f686,0xbf7cfe73,0xbf5988ad,0xbf740bdd,0xbf6b99fb,0xbe9aa086,0x3ec8477c,0xbee363fa,0x3e7bdba4,0x3f655e0b,0x3f7822d1,
0x3f800000,0x3f41d870,0,0xbf273656,0x3e164083,0xbf08f59b,0xbf7d3aac,0xbf584853,0xbf74fa0b,0xbf6a09a7,0xbe94a031,0x3ecf7bca,0xbedae880,0x3e888e93,0x3f676bd8,0x3f76ba07,
0x3f800000,0x3f4154e1,0,0xbf27ce61,0x3e1008b7,0xbf0af1b7,0xbf7d7474,0xbf570348,0xbf75dec6,0xbf686b39,0xbe8e9a22,0x3ed6a399,0xbed25a09,0x3e931f35,0x3f696591,0x3f75341d,
0x3f800000,0x3f40d0da,0,0xbf286605,0x3e09cf86,0xbf0cead0,0xbf7dabcc,0xbf55b993,0xbf76ba07,0xbf66becc,0xbe888e93,0x3eddbe79,0xbec9b953,0x3e9d9e78,0x3f6b4b0c,0x3f73913f,
0x3f800000,0x3f404c5c,0,0xbf28fd41,0x3e039502,0xbf0ee0db,0xbf7de0b1,0xbf546b3b,0xbf778bc5,0xbf650479,0xbe827dc0,0x3ee4cbfe,0xbec1071e,0x3ea80b1f,0x3f6d1c1d,0x3f71d19f,
0x3f800000,0x3f3fc767,0,0xbf299415,0x3dfab273,0xbf10d3cd,0xbf7e1324,0xbf531849,0xbf7853f8,0xbf633c5a,0xbe78cfcc,0x3eebcbbb,0xbeb8442a,0x3eb263ef,0x3f6ed89e,0x3f6ff573,
0x3f800000,0x3f3f41fc,0,0xbf2a2a80,0x3dee3876,0xbf12c39a,0xbf7e4323,0xbf51c0c2,0xbf791298,0xbf61668a,0xbe6c9a7f,0x3ef2bd43,0xbeaf713a,0x3ebca7af,0x3f708066,0x3f6dfcf2,
0x3f800000,0x3f3ebc1b,0,0xbf2ac082,0x3de1bc2e,0xbf14b039,0xbf7e70b0,0xbf5064af,0xbf79c79d,0xbf5f8327,0xbe605c13,0x3ef9a02d,0xbea68f12,0x3ec6d529,0x3f721352,0x3f6be858,
0x3f800000,0x3f3e35c5,0,0xbf2b561b,0x3dd53db9,0xbf16999f,0xbf7e9bc9,0xbf4f0417,0xbf7a7302,0xbf5d924d,0xbe541501,0x3f003a06,0xbe9d9e78,0x3ed0eb2a,0x3f73913f,0x3f69b7e4,
0x3f800000,0x3f3daef9,0,0xbf2beb4a,0x3dc8bd36,0xbf187fc0,0xbf7ec46d,0xbf4d9f02,0xbf7b14be,0xbf5b941a,0xbe47c5c2,0x3f039c3d,0xbe94a031,0x3edae880,0x3f74fa0b,0x3f676bd8,
0x3f800000,0x3f3d27b8,0,0xbf2c800f,0x3dbc3ac3,0xbf1a6293,0xbf7eea9d,0xbf4c3578,0xbf7baccd,0xbf5988ad,0xbe3b6ecf,0x3f06f686,0xbe8b9507,0x3ee4cbfe,0x3f764d97,0x3f650479,
0x3f800000,0x3f3ca003,0,0xbf2d1469,0x3dafb680,0xbf1c420c,0xbf7f0e58,0xbf4ac77f,0xbf7c3b28,0xbf577026,0xbe2f10a2,0x3f0a48ad,0xbe827dc0,0x3eee9479,0x3f778bc5,0x3f628210,
0x3f800000,0x3f3c17d9,0,0xbf2da859,0x3da3308c,0xbf1e1e22,0xbf7f2f9d,0xbf495521,0xbf7cbfc9,0xbf554aa4,0xbe22abb6,0x3f0d9281,0xbe72b651,0x3ef840c8,0x3f78b47b,0x3f5fe4e9,
0x3f800000,0x3f3b8f3b,0,0xbf2e3bde,0x3d96a905,0xbf1ff6cb,0xbf7f4e6d,0xbf47de65,0xbf7d3aac,0xbf531849,0xbe164083,0x3f10d3cd,0xbe605c13,0x3f00e7e4,0x3f79c79d,0x3f5d2d53,
0x3f800000,0x3f3b0629,0,0xbf2ecef7,0x3d8a200a,0xbf21cbfb,0xbf7f6ac7,0xbf466354,0xbf7dabcc,0xbf50d937,0xbe09cf86,0x3f140c5f,0xbe4dee60,0x3f05a02c,0x3f7ac516,0x3f5a5ba0,
0x3f800000,0x3f3a7ca4,0,0xbf2f61a5,0x3d7b2b74,0xbf239da9,0xbf7f84ab,0xbf44e3f5,0xbf7e1324,0xbf4e8d90,0xbdfab273,0x3f173c07,0xbe3b6ecf,0x3f0a48ad,0x3f7baccd,0x3f577026,
0x3f800000,0x3f39f2ac,0,0xbf2ff3e6,0x3d621469,0xbf256bcb,0xbf7f9c18,0xbf436051,0xbf7e70b0,0xbf4c3578,0xbde1bc2e,0x3f1a6293,0xbe28defc,0x3f0ee0db,0x3f7c7eb0,0x3f546b3b,
0x3f800000,0x3f396842,0,0xbf3085bb,0x3d48fb30,0xbf273656,0xbf7fb10f,0xbf41d870,0xbf7ec46d,0xbf49d112,0xbdc8bd36,0x3f1d7fd1,0xbe164083,0x3f13682a,0x3f7d3aac,0x3f514d3d,
0x3f800000,0x3f38dd65,0,0xbf311722,0x3d2fe007,0xbf28fd41,0xbf7fc38f,0xbf404c5c,0xbf7f0e58,0xbf476085,0xbdafb680,0x3f209393,0xbe039502,0x3f17de12,0x3f7de0b1,0x3f4e1689,
0x3f800000,0x3f385216,0,0xbf31a81d,0x3d16c32c,0xbf2ac082,0xbf7fd397,0xbf3ebc1b,0xbf7f4e6d,0xbf44e3f5,0xbd96a905,0x3f239da9,0xbde1bc2e,0x3f1c420c,0x3f7e70b0,0x3f4ac77f,
0x3f800000,0x3f37c655,0,0xbf3238aa,0x3cfb49ba,0xbf2c800f,0xbf7fe129,0xbf3d27b8,0xbf7f84ab,0xbf425b89,0xbd7b2b74,0x3f269de3,0xbdbc3ac3,0x3f209393,0x3f7eea9d,0x3f476085,
0x3f800000,0x3f373a23,0,0xbf32c8c9,0x3cc90ab0,0xbf2e3bde,0xbf7fec43,0xbf3b8f3b,0xbf7fb10f,0xbf3fc767,0xbd48fb30,0x3f299415,0xbd96a905,0x3f24d225,0x3f7f4e6d,0x3f43e200,
0x3f800000,0x3f36ad7f,0,0xbf33587a,0x3c96c9b6,0xbf2ff3e6,0xbf7ff4e6,0xbf39f2ac,0xbf7fd397,0xbf3d27b8,0xbd16c32c,0x3f2c800f,0xbd621469,0x3f28fd41,0x3f7f9c18,0x3f404c5c,
0x3f800000,0x3f36206c,0,0xbf33e7bc,0x3c490e90,0xbf31a81d,0xbf7ffb11,0xbf385216,0xbf7fec43,0xbf3a7ca4,0xbcc90ab0,0x3f2f61a5,0xbd16c32c,0x3f2d1469,0x3f7fd397,0x3f3ca003,
0x3f800000,0x3f3592e7,0,0xbf34768f,0x3bc90f88,0xbf33587a,0xbf7ffec4,0xbf36ad7f,0xbf7ffb11,0xbf37c655,0xbc490e90,0x3f3238aa,0xbc96c9b6,0x3f311722,0x3f7ff4e6,0x3f38dd65,
0x3f800000,0x3f800000,0,0,0x3f800000,0x3f800000,0,0,0x3f800000,0x3f7fec43,0,0xbcc90ab0,0x3f7fb10f,0x3f7f4e6d,0xbd48fb30,0xbd96a905,
0x3f800000,0x3f7fb10f,0,0xbd48fb30,0x3f7ec46d,0x3f7d3aac,0xbdc8bd36,0xbe164083,0x3f800000,0x3f7f4e6d,0,0xbd96a905,0x3f7d3aac,0x3f79c79d,0xbe164083,0xbe605c13,
0x3f800000,0x3f7ec46d,0,0xbdc8bd36,0x3f7b14be,0x3f74fa0b,0xbe47c5c2,0xbe94a031,0x3f800000,0x3f7e1324,0,0xbdfab273,0x3f7853f8,0x3f6ed89e,0xbe78cfcc,0xbeb8442a,
0x3f800000,0x3f7d3aac,0,0xbe164083,0x3f74fa0b,0x3f676bd8,0xbe94a031,0xbedae880,0x3f800000,0x3f7c3b28,0,0xbe2f10a2,0x3f710908,0x3f5ebe05,0xbeac7cd4,0xbefc5d27,
0x3f800000,0x3f7b14be,0,0xbe47c5c2,0x3f6c835e,0x3f54db31,0xbec3ef15,0xbf0e39da,0x3f800000,0x3f79c79d,0,0xbe605c13,0x3f676bd8,0x3f49d112,0xbedae880,0xbf1d7fd1,
0x3f800000,0x3f7853f8,0,0xbe78cfcc,0x3f61c598,0x3f3daef9,0xbef15aea,0xbf2beb4a,0x3f800000,0x3f76ba07,0,0xbe888e93,0x3f5b941a,0x3f3085bb,0xbf039c3d,0xbf396842,
0x3f800000,0x3f74fa0b,0,0xbe94a031,0x3f54db31,0x3f226799,0xbf0e39da,0xbf45e403,0x3f800000,0x3f731447,0,0xbea09ae5,0x3f4d9f02,0x3f13682a,0xbf187fc0,0xbf514d3d,
0x3f800000,0x3f710908,0,0xbeac7cd4,0x3f45e403,0x3f039c3d,0xbf226799,0xbf5b941a,0x3f800000,0x3f6ed89e,0,0xbeb8442a,0x3f3daef9,0x3ee63375,0xbf2beb4a,0xbf64aa59,
0x3f800000,0x3f800000,0,0,0x3f800000,0x3f800000,0,0,0x3f7ec46d,0x3f7e1324,0xbdc8bd36,0xbdfab273,0x3f7d3aac,0x3f7c3b28,0xbe164083,0xbe2f10a2,
0x3f7b14be,0x3f7853f8,0xbe47c5c2,0xbe78cfcc,0x3f74fa0b,0x3f710908,0xbe94a031,0xbeac7cd4,0x3f74fa0b,0x3f6ed89e,0xbe94a031,0xbeb8442a,0x3f676bd8,0x3f5ebe05,0xbedae880,0xbefc5d27,
0x3f6c835e,0x3f61c598,0xbec3ef15,0xbef15aea,0x3f54db31,0x3f45e403,0xbf0e39da,0xbf226799,0x3f61c598,0x3f514d3d,0xbef15aea,0xbf13682a,0x3f3daef9,0x3f273656,0xbf2beb4a,0xbf41d870,
0x3f54db31,0x3f3daef9,0xbf0e39da,0xbf2beb4a,0x3f226799,0x3f039c3d,0xbf45e403,0xbf5b941a,0x3f45e403,0x3f273656,0xbf226799,0xbf41d870,0x3f039c3d,0x3eb8442a,0xbf5b941a,0xbf6ed89e,
0x3f3504f3,0x3f0e39da,0xbf3504f3,0xbf54db31,0x3ec3ef15,0x3e47c5c2,0xbf6c835e,0xbf7b14be,0x3f226799,0x3ee63375,0xbf45e403,0xbf64aa59,0x3e78cfcc,0x3cc90ab0,0xbf7853f8,0xbf7fec43,
0x3f0e39da,0x3eac7cd4,0xbf54db31,0xbf710908,0x3dc8bd36,0xbe164083,0xbf7ec46d,0xbf7d3aac,0x3ef15aea,0x3e605c13,0xbf61c598,0xbf79c79d,0xbd48fb30,0xbea09ae5,0xbf7fb10f,0xbf731447,
0x3ec3ef15,0x3dc8bd36,0xbf6c835e,0xbf7ec46d,0xbe47c5c2,0xbef15aea,0xbf7b14be,0xbf61c598,0x3e94a031,0xbcc90ab0,0xbf74fa0b,0xbf7fec43,0xbeac7cd4,0xbf1d7fd1,0xbf710908,0xbf49d112,
0x3e47c5c2,0xbe164083,0xbf7b14be,0xbf7d3aac,0xbef15aea,0xbf3daef9,0xbf61c598,0xbf2beb4a,0x3dc8bd36,0xbe888e93,0xbf7ec46d,0xbf76ba07,0xbf187fc0,0xbf584853,0xbf4d9f02,0xbf08f59b,
0x3f800000,0x3f800000,0,0,0x3f800000,0x3f800000,0,0,0x3f7b14be,0x3f79c79d,0xbe47c5c2,0xbe605c13,0x3f7853f8,0x3f76ba07,0xbe78cfcc,0xbe888e93,
0x3f6c835e,0x3f676bd8,0xbec3ef15,0xbedae880,0x3f61c598,0x3f5b941a,0xbef15aea,0xbf039c3d,0x3f54db31,0x3f49d112,0xbf0e39da,0xbf1d7fd1,0x3f3daef9,0x3f3085bb,0xbf2beb4a,0xbf396842,
0x3f3504f3,0x3f226799,0xbf3504f3,0xbf45e403,0x3f0e39da,0x3ef15aea,0xbf54db31,0xbf61c598,0x3f0e39da,0x3ee63375,0xbf54db31,0xbf64aa59,0x3eac7cd4,0x3e605c13,0xbf710908,0xbf79c79d,
0x3ec3ef15,0x3e78cfcc,0xbf6c835e,0xbf7853f8,0x3dc8bd36,0xbd48fb30,0xbf7ec46d,0xbf7fb10f,0x3e47c5c2,0x3cc90ab0,0xbf7b14be,0xbf7fec43,0xbe164083,0xbea09ae5,0xbf7d3aac,0xbf731447,
0,0xbe47c5c2,0xbf800000,0xbf7b14be,0xbec3ef15,0xbf0e39da,0xbf6c835e,0xbf54db31,0xbe47c5c2,0xbecf7bca,0xbf7b14be,0xbf6a09a7,0xbf187fc0,0xbf41d870,0xbf4d9f02,0xbf273656,
0xbec3ef15,0xbf187fc0,0xbf6c835e,0xbf4d9f02,0xbf45e403,0xbf676bd8,0xbf226799,0xbedae880,0xbf0e39da,0xbf41d870,0xbf54db31,0xbf273656,0xbf676bd8,0xbf7c3b28,0xbedae880,0xbe2f10a2,
0xbf3504f3,0xbf61c598,0xbf3504f3,0xbef15aea,0xbf7b14be,0xbf7ec46d,0xbe47c5c2,0x3dc8bd36,0xbf54db31,0xbf76ba07,0xbf0e39da,0xbe888e93,0xbf7fb10f,0xbf6ed89e,0x3d48fb30,0x3eb8442a,
0xbf6c835e,0xbf7fb10f,0xbec3ef15,0xbd48fb30,0xbf74fa0b,0xbf4d9f02,0x3e94a031,0x3f187fc0,0xbf7b14be,0xbf7c3b28,0xbe47c5c2,0x3e2f10a2,0xbf5b941a,0xbf1d7fd1,0x3f039c3d,0x3f49d112,
0x3f800000,0x3f800000,0,0,0x3f800000,0x3f800000,0,0,0x3f74fa0b,0x3f731447,0xbe94a031,0xbea09ae5,0x3f710908,0x3f6ed89e,0xbeac7cd4,0xbeb8442a,
0x3f54db31,0x3f4d9f02,0xbf0e39da,0xbf187fc0,0x3f45e403,0x3f3daef9,0xbf226799,0xbf2beb4a,0x3f226799,0x3f13682a,0xbf45e403,0xbf514d3d,0x3f039c3d,0x3ee63375,0xbf5b941a,0xbf64aa59,
0x3ec3ef15,0x3e94a031,0xbf6c835e,0xbf74fa0b,0x3e47c5c2,0x3dc8bd36,0xbf7b14be,0xbf7ec46d,0x3dc8bd36,0xbcc90ab0,0xbf7ec46d,0xbf7fec43,0xbe164083,0xbe888e93,0xbf7d3aac,0xbf76ba07,
0xbe47c5c2,0xbeac7cd4,0xbf7b14be,0xbf710908,0xbef15aea,0xbf187fc0,0xbf61c598,0xbf4d9f02,0xbef15aea,0xbf1d7fd1,0xbf61c598,0xbf49d112,0xbf3daef9,0xbf584853,0xbf2beb4a,0xbf08f59b,
0xbf3504f3,0xbf54db31,0xbf3504f3,0xbf0e39da,0xbf6c835e,0xbf7b14be,0xbec3ef15,0xbe47c5c2,0xbf61c598,0xbf76ba07,0xbef15aea,0xbe888e93,0xbf7fb10f,0xbf7c3b28,0xbd48fb30,0x3e2f10a2,
0xbf7b14be,0xbf7fb10f,0xbe47c5c2,0x3d48fb30,0xbf74fa0b,0xbf5b941a,0x3e94a031,0x3f039c3d,0xbf7ec46d,0xbf6ed89e,0x3dc8bd36,0x3eb8442a,0xbf4d9f02,0xbf1d7fd1,0x3f187fc0,0x3f49d112,
0xbf6c835e,0xbf45e403,0x3ec3ef15,0x3f226799,0xbf0e39da,0xbe94a031,0x3f54db31,0x3f74fa0b,0xbf45e403,0xbf08f59b,0x3f226799,0x3f584853,0xbe78cfcc,0x3d96a905,0x3f7853f8,0x3f7f4e6d,
0xbf0e39da,0xbe78cfcc,0x3f54db31,0x3f7853f8,0x3dc8bd36,0x3edae880,0x3f7ec46d,0x3f676bd8,0xbe94a031,0x3d96a905,0x3f74fa0b,0x3f7f4e6d,0x3edae880,0x3f396842,0x3f676bd8,0x3f3085bb,
0x35924925,0x35924925,0,0,0x35924925,0x35924925,0,0,0x35924925,0x35924925,0,0,0x35924925,0x35924925,0,0,
0x35924925,0x35924925,0,0,0x35924925,0x35924925,0,0,0x35924925,0x35924925,0,0,0x35924925,0x35924925,0,0,
0x35924925,0x35924925,0,0,0x35924925,0x35924925,0,0,0x35924925,0x35924925,0,0,0x35924925,0x35924925,0,0,
0x35924925,0x35924925,0,0,0x35924925,0x35924925,0,0,0x35924925,0x35924925,0,0,0x35924925,0x35924925,0,0,
0x35924925,0x35924925,0,0,0x35924925,0x35924925,0,0,0x35924925,0x35924925,0,0,0x35924925,0x35924925,0,0,
0x35924925,0x35924925,0,0,0x35924925,0x35924925,0,0,0x35924925,0x35924925,0,0,0x35924925,0x35924925,0,0,
0x35924925,0x35924925,0,0,0x35924925,0x35924925,0,0,0x35924925,0x35924925,0,0,0x35924925,0x35924925,0,0,
0x35924925,0x35924925,0,0,0x35924925,0x35924925,0,0,0x35924925,0x35924925,0,0,0x35924925,0x35924925,0,0,
0x35924925,0x35924925,0,0,0x35924925,0x35924925,0,0,0x35924925,0x35924925,0,0,0x35924925,0x35924925,0,0,
0x35924925,0x35924925,0,0,0x35924925,0x35924925,0,0,0x35924925,0x35924925,0,0,0x35924925,0x35924925,0,0,
0x35924925,0x35924925,0,0,0x35924925,0x35924925,0,0,0x35924925,0x35924925,0,0,0x35924925,0x35924925,0,0,
0x35924925,0x35924925,0,0,0x35924925,0x35924925,0,0,0x35924925,0x35924925,0,0,0x35924925,0x35924925,0,0,
0x35924925,0x35924925,0,0,0x35924925,0x35924925,0,0,0x35924925,0x35924925,0,0,0x35924925,0x35924925,0,0,
0x35924925,0x35924925,0,0,0x35924925,0x35924925,0,0,0x35924925,0x35924925,0,0,0x35924925,0x35924925,0,0,
0x35924925,0x35924925,0,0,0x35924925,0x35924925,0,0,0x35924925,0x35924925,0,0,0x35924925,0x35924925,0,0,
0x35924925,0x35924925,0,0,0x35924925,0x35924925,0,0,0x35924925,0x35924925,0,0,0x35924925,0x35924925,0,0,
0x35924925,0x35924925,0,0,0x35924925,0x35924925,0,0,0x35924925,0x35924925,0,0,0x35924925,0x35924925,0,0,
0x35924925,0x35924925,0,0,0x35924925,0x35924925,0,0,0x35924925,0x35924925,0,0,0x35924925,0x35924925,0,0,
0x35924925,0x35924925,0,0,0x35924925,0x35924925,0,0,0x35924925,0x35924925,0,0,0x35924925,0x35924925,0,0,
0x35924925,0x35924925,0,0,0x35924925,0x35924925,0,0,0x35924925,0x35924925,0,0,0x35924925,0x35924925,0,0,
0x35924925,0x35924925,0,0,0x35924925,0x35924925,0,0,0x35924925,0x35924925,0,0,0x35924925,0x35924925,0,0,
0x35924925,0x35924925,0,0,0x35924925,0x35924925,0,0,0x35924925,0x35924925,0,0,0x35924925,0x35924925,0,0,
0x35924925,0x35924925,0,0,0x35924925,0x35924925,0,0,0x35924925,0x35924925,0,0,0x35924925,0x35924925,0,0,
0x35924925,0x35924925,0,0,0x35924925,0x35924925,0,0,0x35924925,0x35924925,0,0,0x35924925,0x35924925,0,0,
0x35924925,0x35924925,0,0,0x35924925,0x35924925,0,0,0x35924925,0x35924925,0,0,0x35924925,0x35924925,0,0,
0x35924925,0x35924925,0,0,0x35924925,0x35924925,0,0,0x35924925,0x35924925,0,0,0x35924925,0x35924925,0,0,
0x35924925,0x35924925,0,0,0x35924925,0x35924925,0,0,0x35924925,0x35924925,0,0,0x35924925,0x35924925,0,0,
0x35924925,0x35924925,0,0,0x35924925,0x35924925,0,0,0x35924925,0x35924925,0,0,0x35924925,0x35924925,0,0,
0x35924925,0x35924925,0,0,0x35924925,0x35924925,0,0,0x35924925,0x35924925,0,0,0x35924925,0x35924925,0,0,
0x35924925,0x35924925,0,0,0x35924925,0x35924925,0,0,0x35924925,0x35924925,0,0,0x35924925,0x35924925,0,0,
0x35924925,0x35924925,0,0,0x35924925,0x35924925,0,0,0x35924925,0x35924925,0,0,0x35924925,0x35924925,0,0,
0x35924925,0x35924925,0,0,0x35924925,0x35924925,0,0,0x35924925,0x35924925,0,0,0x35924925,0x35924925,0,0,
0x35924925,0x35921c09,0,0xb365b15b,0x359194d1,0x3590b3d0,0xb3e56a87,0xb42bb772,0x359248f7,0x35921638,0xb165c8e2,0xb3740934,0x35918961,0x3590a2c8,0xb3ec8fad,0xb42f446d,
0x35924870,0x3592100e,0xb1e5c89b,0xb381303c,0x35917d97,0x35909166,0xb3f3b441,0xb432d0fd,0x3592478f,0x3592098a,0xb22c561c,0xb3885b8e,0x35917174,0x35907fab,0xb3fad83f,0xb4365d1f,
0x35924653,0x359202ab,0xb265c780,0xb38f868b,0x359164f7,0x35906d98,0xb400fdd1,0xb439e8cf,0x359244bd,0x3591fb72,0xb28f9c2b,0xb396b131,0x35915820,0x35905b2b,0xb4048f33,0xb43d740e,
0x359242cc,0x3591f3e0,0xb2ac543e,0xb39ddb79,0x35914af0,0x35904865,0xb4082044,0xb440fed7,0x35924082,0x3591ebf3,0xb2c90be6,0xb3a50560,0x35913d66,0x35903546,0xb40bb100,0xb4448929,
0x35923ddd,0x3591e3ac,0xb2e5c312,0xb3ac2ee1,0x35912f82,0x359021ce,0xb40f4166,0xb4481302,0x35923ade,0x3591db0c,0xb3013cd8,0xb3b357f8,0x35912145,0x35900dfd,0xb412d174,0xb44b9c60,
0x35923785,0x3591d211,0xb30f97d8,0xb3ba80a0,0x359112ae,0x358ff9d3,0xb4166127,0xb44f2540,0x359233d2,0x3591c8bc,0xb31df27f,0xb3c1a8d5,0x359103be,0x358fe551,0xb419f07e,0xb452ada0,
0x35922fc4,0x3591bf0e,0xb32c4cc4,0xb3c8d093,0x3590f474,0x358fd075,0xb41d7f75,0xb456357f,0x35922b5d,0x3591b505,0xb33aa69f,0xb3cff7d5,0x3590e4d1,0x358fbb41,0xb4210e0b,0xb459bcd9,
0x3592269b,0x3591aaa3,0xb3490008,0xb3d71e96,0x3590d4d5,0x358fa5b5,0xb4249c3e,0xb45d43ad,0x3592217f,0x35919fe7,0xb35758f4,0xb3de44d3,0x3590c47f,0x358f8fcf,0xb4282a0c,0xb460c9f8,
0x358f7991,0x358de6d7,0xb4644fb9,0xb48e2d99,0x358bfc98,0x3589bc05,0xb4a9dba6,0xb4c520f2,0x358f62fb,0x358dcac0,0xb467d4ed,0xb48feb39,0x358bdb13,0x35899525,0xb4ab933a,0xb4c6d16a,
0x358f4c0c,0x358dae53,0xb46b5992,0xb491a880,0x358bb938,0x35896df1,0xb4ad4a63,0xb4c88167,0x358f34c5,0x358d918d,0xb46edda6,0xb493656e,0x358b9706,0x35894668,0xb4af0122,0xb4ca30e8,
0x358f1d26,0x358d7471,0xb4726126,0xb4952201,0x358b747e,0x35891e8b,0xb4b0b775,0xb4cbdfec,0x358f052e,0x358d56fd,0xb475e411,0xb496de37,0x358b51a1,0x3588f659,0xb4b26d5a,0xb4cd8e73,
0x358eecde,0x358d3932,0xb4796664,0xb4989a11,0x358b2e6d,0x3588cdd2,0xb4b422d2,0xb4cf3c7b,0x358ed436,0x358d1b10,0xb47ce81d,0xb49a558c,0x358b0ae4,0x3588a4f7,0xb4b5d7db,0xb4d0ea04,
0x358ebb35,0x358cfc96,0xb480349d,0xb49c10a8,0x358ae704,0x35887bc8,0xb4b78c73,0xb4d2970b,0x358ea1dd,0x358cddc6,0xb481f4dd,0xb49dcb64,0x358ac2cf,0x35885244,0xb4b9409a,0xb4d44390,
0x358e882d,0x358cbe9f,0xb483b4cc,0xb49f85bf,0x358a9e45,0x3588286d,0xb4baf44f,0xb4d5ef93,0x358e6e25,0x358c9f21,0xb485746b,0xb4a13fb7,0x358a7965,0x3587fe41,0xb4bca791,0xb4d79b11,
0x358e53c4,0x358c7f4d,0xb48733b6,0xb4a2f94b,0x358a542f,0x3587d3c2,0xb4be5a5e,0xb4d9460b,0x358e390d,0x358c5f21,0xb488f2af,0xb4a4b27c,0x358a2ea4,0x3587a8ef,0xb4c00cb6,0xb4daf07e,
0x358e1dfd,0x358c3ea0,0xb48ab153,0xb4a66b46,0x358a08c4,0x35877dc8,0xb4c1be97,0xb4dc9a6b,0x358e0296,0x358c1dc7,0xb48c6fa1,0xb4a823aa,0x3589e28f,0x3587524d,0xb4c37001,0xb4de43cf,
0x3587267f,0x35843da0,0xb4dfecab,0xb4fa2e49,0x35810332,0x357af267,0xb509eacf,0xb516696a,0x3586fa5e,0x35840c58,0xb4e194fc,0xb4fbcd6e,0x3580cce1,0x357a7bf8,0xb50ab54b,0xb5172e53,
0x3586cde9,0x3583dabe,0xb4e33cc2,0xb4fd6bf8,0x35809641,0x357a04ee,0xb50b7f72,0xb517f2e0,0x3586a121,0x3583a8d3,0xb4e4e3fb,0xb4ff09e5,0x35805f52,0x35798d49,0xb50c4943,0xb518b70e,
0x35867407,0x35837697,0xb4e68aa8,0xb500539b,0x35802813,0x3579150b,0xb50d12bd,0xb5197ade,0x35864699,0x35834409,0xb4e830c7,0xb50121f4,0x357fe10a,0x35789c34,0xb50ddbe0,0xb51a3e50,
0x358618d8,0x3583112b,0xb4e9d656,0xb501effd,0x357f7151,0x357822c3,0xb50ea4ac,0xb51b0162,0x3585eac5,0x3582ddfc,0xb4eb7b55,0xb502bdb6,0x357f00fa,0x3577a8b8,0xb50f6d20,0xb51bc415,
0x3585bc5f,0x3582aa7c,0xb4ed1fc2,0xb5038b1e,0x357e9006,0x35772e15,0xb510353b,0xb51c8668,0x35858da7,0x358276ac,0xb4eec39e,0xb5045835,0x357e1e75,0x3576b2da,0xb510fcfd,0xb51d485a,
0x35855e9c,0x3582428b,0xb4f066e6,0xb50524fb,0x357dac47,0x35763706,0xb511c466,0xb51e09eb,0x35852f3f,0x35820e19,0xb4f2099a,0xb505f16e,0x357d397d,0x3575ba9b,0xb5128b75,0xb51ecb1a,
0x3584ff90,0x3581d958,0xb4f3abb8,0xb506bd8f,0x357cc616,0x35753d98,0xb5135229,0xb51f8be8,0x3584cf8f,0x3581a446,0xb4f54d41,0xb507895d,0x357c5213,0x3574bffe,0xb5141883,0xb5204c53,
0x35849f3b,0x35816ee5,0xb4f6ee32,0xb50854d7,0x357bdd75,0x357441cc,0xb514de81,0xb5210c5c,0x35846e96,0x35813933,0xb4f88e8a,0xb5091ffd,0x357b683c,0x3573c304,0xb515a424,0xb521cc01,
0x357343a6,0x356afede,0xb5228b42,0xb52e48db,0x35622928,0x3558c7f8,0xb5399af8,0xb5447a9d,0x3572c3b2,0x356a75b4,0xb5234a1f,0xb52f0136,0x3561971d,0x35582d65,0xb53a4c5f,0xb54524a3,
0x35724328,0x3569ebf9,0xb5240897,0xb52fb925,0x35610486,0x3557924c,0xb53afd53,0xb545ce2f,0x3571c208,0x356961ad,0xb524c6aa,0xb53070a8,0x35607164,0x3556f6af,0xb53badd4,0xb5467741,
0x35714053,0x3568d6d2,0xb5258458,0xb53127bd,0x355fddb7,0x35565a8d,0xb53c5de1,0xb5471fd9,0x3570be0a,0x35684b67,0xb526419f,0xb531de66,0x355f4981,0x3555bde6,0xb53d0d7a,0xb547c7f6,
0x35703b2c,0x3567bf6d,0xb526fe80,0xb53294a0,0x355eb4c1,0x355520bc,0xb53dbc9e,0xb5486f98,0x356fb7ba,0x356732e4,0xb527bafa,0xb5334a6d,0x355e1f77,0x3554830e,0xb53e6b4d,0xb54916be,
0x356f33b3,0x3566a5cc,0xb528770c,0xb533ffcb,0x355d89a5,0x3553e4dd,0xb53f1987,0xb549bd68,0x356eaf1a,0x35661826,0xb52932b6,0xb534b4b9,0x355cf34a,0x3553462a,0xb53fc74a,0xb54a6395,
0x356e29ed,0x356589f2,0xb529edf8,0xb5356939,0x355c5c66,0x3552a6f4,0xb5407498,0xb54b0946,0x356da42d,0x3564fb30,0xb52aa8d1,0xb5361d48,0x355bc4fb,0x3552073c,0xb541216f,0xb54bae79,
0x356d1ddb,0x35646be1,0xb52b6341,0xb536d0e8,0x355b2d08,0x35516703,0xb541cdce,0xb54c532f,0x356c96f6,0x3563dc06,0xb52c1d48,0xb5378416,0x355a948e,0x3550c649,0xb54279b6,0xb54cf767,
0x356c0f80,0x35634b9d,0xb52cd6e4,0xb53836d3,0x3559fb8d,0x3550250d,0xb5432527,0xb54d9b20,0x356b8777,0x3562baa9,0xb52d9015,0xb538e91f,0x35596206,0x354f8352,0xb543d01e,0xb54e3e5b,
0x35924925,0x359194d1,0,0xb3e56a87,0x358f7991,0x358bfc98,0xb4644fb9,0xb4a9dba6,0x35924870,0x35917d97,0xb1e5c89b,0xb3f3b441,0x358f4c0c,0x358bb938,0xb46b5992,0xb4ad4a63,
0x35924653,0x359164f7,0xb265c780,0xb400fdd1,0x358f1d26,0x358b747e,0xb4726126,0xb4b0b775,0x359242cc,0x35914af0,0xb2ac543e,0xb4082044,0x358eecde,0x358b2e6d,0xb4796664,0xb4b422d2,
0x35923ddd,0x35912f82,0xb2e5c312,0xb40f4166,0x358ebb35,0x358ae704,0xb480349d,0xb4b78c73,0x35923785,0x359112ae,0xb30f97d8,0xb4166127,0x358e882d,0x358a9e45,0xb483b4cc,0xb4baf44f,
0x35922fc4,0x3590f474,0xb32c4cc4,0xb41d7f75,0x358e53c4,0x358a542f,0xb48733b6,0xb4be5a5e,0x3592269b,0x3590d4d5,0xb3490008,0xb4249c3e,0x358e1dfd,0x358a08c4,0xb48ab153,0xb4c1be97,
0x35921c09,0x3590b3d0,0xb365b15b,0xb42bb772,0x358de6d7,0x3589bc05,0xb48e2d99,0xb4c520f2,0x3592100e,0x35909166,0xb381303c,0xb432d0fd,0x358dae53,0x35896df1,0xb491a880,0xb4c88167,
0x359202ab,0x35906d98,0xb38f868b,0xb439e8cf,0x358d7471,0x35891e8b,0xb4952201,0xb4cbdfec,0x3591f3e0,0x35904865,0xb39ddb79,0xb440fed7,0x358d3932,0x3588cdd2,0xb4989a11,0xb4cf3c7b,
0x3591e3ac,0x359021ce,0xb3ac2ee1,0xb4481302,0x358cfc96,0x35887bc8,0xb49c10a8,0xb4d2970b,0x3591d211,0x358ff9d3,0xb3ba80a0,0xb44f2540,0x358cbe9f,0x3588286d,0xb49f85bf,0xb4d5ef93,
0x3591bf0e,0x358fd075,0xb3c8d093,0xb456357f,0x358c7f4d,0x3587d3c2,0xb4a2f94b,0xb4d9460b,0x3591aaa3,0x358fa5b5,0xb3d71e96,0xb45d43ad,0x358c3ea0,0x35877dc8,0xb4a66b46,0xb4dc9a6b,
0x3587267f,0x35810332,0xb4dfecab,0xb509eacf,0x357343a6,0x35622928,0xb5228b42,0xb5399af8,0x3586cde9,0x35809641,0xb4e33cc2,0xb50b7f72,0x35724328,0x35610486,0xb5240897,0xb53afd53,
0x35867407,0x35802813,0xb4e68aa8,0xb50d12bd,0x35714053,0x355fddb7,0xb5258458,0xb53c5de1,0x358618d8,0x357f7151,0xb4e9d656,0xb50ea4ac,0x35703b2c,0x355eb4c1,0xb526fe80,0xb53dbc9e,
0x3585bc5f,0x357e9006,0xb4ed1fc2,0xb510353b,0x356f33b3,0x355d89a5,0xb528770c,0xb53f1987,0x35855e9c,0x357dac47,0xb4f066e6,0xb511c466,0x356e29ed,0x355c5c66,0xb529edf8,0xb5407498,
0x3584ff90,0x357cc616,0xb4f3abb8,0xb5135229,0x356d1ddb,0x355b2d08,0xb52b6341,0xb541cdce,0x35849f3b,0x357bdd75,0xb4f6ee32,0xb514de81,0x356c0f80,0x3559fb8d,0xb52cd6e4,0xb5432527,
0x35843da0,0x357af267,0xb4fa2e49,0xb516696a,0x356afede,0x3558c7f8,0xb52e48db,0xb5447a9d,0x3583dabe,0x357a04ee,0xb4fd6bf8,0xb517f2e0,0x3569ebf9,0x3557924c,0xb52fb925,0xb545ce2f,
0x35837697,0x3579150b,0xb500539b,0xb5197ade,0x3568d6d2,0x35565a8d,0xb53127bd,0xb5471fd9,0x3583112b,0x357822c3,0xb501effd,0xb51b0162,0x3567bf6d,0x355520bc,0xb53294a0,0xb5486f98,
0x3582aa7c,0x35772e15,0xb5038b1e,0xb51c8668,0x3566a5cc,0x3553e4dd,0xb533ffcb,0xb549bd68,0x3582428b,0x35763706,0xb50524fb,0xb51e09eb,0x356589f2,0x3552a6f4,0xb5356939,0xb54b0946,
0x3581d958,0x35753d98,0xb506bd8f,0xb51f8be8,0x35646be1,0x35516703,0xb536d0e8,0xb54c532f,0x35816ee5,0x357441cc,0xb50854d7,0xb5210c5c,0x35634b9d,0x3550250d,0xb53836d3,0xb54d9b20,
0x354ee116,0x35399af8,0xb54ee116,0xb5622928,0x35228b42,0x3509eacf,0xb57343a6,0xb5810332,0x354d9b20,0x353836d3,0xb550250d,0xb5634b9d,0x35210c5c,0x350854d7,0xb57441cc,0xb5816ee5,
0x354c532f,0x3536d0e8,0xb5516703,0xb5646be1,0x351f8be8,0x3506bd8f,0xb5753d98,0xb581d958,0x354b0946,0x35356939,0xb552a6f4,0xb56589f2,0x351e09eb,0x350524fb,0xb5763706,0xb582428b,
0x3549bd68,0x3533ffcb,0xb553e4dd,0xb566a5cc,0x351c8668,0x35038b1e,0xb5772e15,0xb582aa7c,0x35486f98,0x353294a0,0xb55520bc,0xb567bf6d,0x351b0162,0x3501effd,0xb57822c3,0xb583112b,
0x35471fd9,0x353127bd,0xb5565a8d,0xb568d6d2,0x35197ade,0x3500539b,0xb579150b,0xb5837697,0x3545ce2f,0x352fb925,0xb557924c,0xb569ebf9,0x3517f2e0,0x34fd6bf8,0xb57a04ee,0xb583dabe,
0x35447a9d,0x352e48db,0xb558c7f8,0xb56afede,0x3516696a,0x34fa2e49,0xb57af267,0xb5843da0,0x35432527,0x352cd6e4,0xb559fb8d,0xb56c0f80,0x3514de81,0x34f6ee32,0xb57bdd75,0xb5849f3b,
0x3541cdce,0x352b6341,0xb55b2d08,0xb56d1ddb,0x35135229,0x34f3abb8,0xb57cc616,0xb584ff90,0x35407498,0x3529edf8,0xb55c5c66,0xb56e29ed,0x3511c466,0x34f066e6,0xb57dac47,0xb5855e9c,
0x353f1987,0x3528770c,0xb55d89a5,0xb56f33b3,0x3510353b,0x34ed1fc2,0xb57e9006,0xb585bc5f,0x353dbc9e,0x3526fe80,0xb55eb4c1,0xb5703b2c,0x350ea4ac,0x34e9d656,0xb57f7151,0xb58618d8,
0x353c5de1,0x35258458,0xb55fddb7,0xb5714053,0x350d12bd,0x34e68aa8,0xb5802813,0xb5867407,0x353afd53,0x35240897,0xb5610486,0xb5724328,0x350b7f72,0x34e33cc2,0xb5809641,0xb586cde9,
0x34dfecab,0x34a9dba6,0xb587267f,0xb58bfc98,0x34644fb9,0x33e56a87,0xb58f7991,0xb59194d1,0x34dc9a6b,0x34a66b46,0xb5877dc8,0xb58c3ea0,0x345d43ad,0x33d71e96,0xb58fa5b5,0xb591aaa3,
0x34d9460b,0x34a2f94b,0xb587d3c2,0xb58c7f4d,0x3456357f,0x33c8d093,0xb58fd075,0xb591bf0e,0x34d5ef93,0x349f85bf,0xb588286d,0xb58cbe9f,0x344f2540,0x33ba80a0,0xb58ff9d3,0xb591d211,
0x34d2970b,0x349c10a8,0xb5887bc8,0xb58cfc96,0x34481302,0x33ac2ee1,0xb59021ce,0xb591e3ac,0x34cf3c7b,0x34989a11,0xb588cdd2,0xb58d3932,0x3440fed7,0x339ddb79,0xb5904865,0xb591f3e0,
0x34cbdfec,0x34952201,0xb5891e8b,0xb58d7471,0x3439e8cf,0x338f868b,0xb5906d98,0xb59202ab,0x34c88167,0x3491a880,0xb5896df1,0xb58dae53,0x3432d0fd,0x3381303c,0xb5909166,0xb592100e,
0x34c520f2,0x348e2d99,0xb589bc05,0xb58de6d7,0x342bb772,0x3365b15b,0xb590b3d0,0xb5921c09,0x34c1be97,0x348ab153,0xb58a08c4,0xb58e1dfd,0x34249c3e,0x33490008,0xb590d4d5,0xb592269b,
0x34be5a5e,0x348733b6,0xb58a542f,0xb58e53c4,0x341d7f75,0x332c4cc4,0xb590f474,0xb5922fc4,0x34baf44f,0x3483b4cc,0xb58a9e45,0xb58e882d,0x34166127,0x330f97d8,0xb59112ae,0xb5923785,
0x34b78c73,0x3480349d,0xb58ae704,0xb58ebb35,0x340f4166,0x32e5c312,0xb5912f82,0xb5923ddd,0x34b422d2,0x34796664,0xb58b2e6d,0xb58eecde,0x34082044,0x32ac543e,0xb5914af0,0xb59242cc,
0x34b0b775,0x34726126,0xb58b747e,0xb58f1d26,0x3400fdd1,0x3265c780,0xb59164f7,0xb5924653,0x34ad4a63,0x346b5992,0xb58bb938,0xb58f4c0c,0x33f3b441,0x31e5c89b,0xb5917d97,0xb5924870,
0x35924925,0x3590b3d0,0,0xb42bb772,0x358bfc98,0x35843da0,0xb4a9dba6,0xb4fa2e49,0x3592478f,0x35907fab,0xb22c561c,0xb4365d1f,0x358b9706,0x3583a8d3,0xb4af0122,0xb4ff09e5,
0x359242cc,0x35904865,0xb2ac543e,0xb440fed7,0x358b2e6d,0x3583112b,0xb4b422d2,0xb501effd,0x35923ade,0x35900dfd,0xb3013cd8,0xb44b9c60,0x358ac2cf,0x358276ac,0xb4b9409a,0xb5045835,
0x35922fc4,0x358fd075,0xb32c4cc4,0xb456357f,0x358a542f,0x3581d958,0xb4be5a5e,0xb506bd8f,0x3592217f,0x358f8fcf,0xb35758f4,0xb460c9f8,0x3589e28f,0x35813933,0xb4c37001,0xb5091ffd,
0x3592100e,0x358f4c0c,0xb381303c,0xb46b5992,0x35896df1,0x35809641,0xb4c88167,0xb50b7f72,0x3591fb72,0x358f052e,0xb396b131,0xb475e411,0x3588f659,0x357fe10a,0xb4cd8e73,0xb50ddbe0,
0x3591e3ac,0x358ebb35,0xb3ac2ee1,0xb480349d,0x35887bc8,0x357e9006,0xb4d2970b,0xb510353b,0x3591c8bc,0x358e6e25,0xb3c1a8d5,0xb485746b,0x3587fe41,0x357d397d,0xb4d79b11,0xb5128b75,
0x3591aaa3,0x358e1dfd,0xb3d71e96,0xb48ab153,0x35877dc8,0x357bdd75,0xb4dc9a6b,0xb514de81,0x35918961,0x358dcac0,0xb3ec8fad,0xb48feb39,0x3586fa5e,0x357a7bf8,0xb4e194fc,0xb5172e53,
0x359164f7,0x358d7471,0xb400fdd1,0xb4952201,0x35867407,0x3579150b,0xb4e68aa8,0xb5197ade,0x35913d66,0x358d1b10,0xb40bb100,0xb49a558c,0x3585eac5,0x3577a8b8,0xb4eb7b55,0xb51bc415,
0x359112ae,0x358cbe9f,0xb4166127,0xb49f85bf,0x35855e9c,0x35763706,0xb4f066e6,0xb51e09eb,0x3590e4d1,0x358c5f21,0xb4210e0b,0xb4a4b27c,0x3584cf8f,0x3574bffe,0xb4f54d41,0xb5204c53,
0x357343a6,0x3558c7f8,0xb5228b42,0xb5447a9d,0x35399af8,0x3516696a,0xb5622928,0xb57af267,0x3571c208,0x3556f6af,0xb524c6aa,0xb5467741,0x35378416,0x35141883,0xb563dc06,0xb57c5213,
0x35703b2c,0x355520bc,0xb526fe80,0xb5486f98,0x35356939,0x3511c466,0xb56589f2,0xb57dac47,0x356eaf1a,0x3553462a,0xb52932b6,0xb54a6395,0x35334a6d,0x350f6d20,0xb56732e4,0xb57f00fa,
0x356d1ddb,0x35516703,0xb52b6341,0xb54c532f,0x353127bd,0x350d12bd,0xb568d6d2,0xb5802813,0x356b8777,0x354f8352,0xb52d9015,0xb54e3e5b,0x352f0136,0x350ab54b,0xb56a75b4,0xb580cce1,
0x3569ebf9,0x354d9b20,0xb52fb925,0xb550250d,0x352cd6e4,0x350854d7,0xb56c0f80,0xb5816ee5,0x35684b67,0x354bae79,0xb531de66,0xb552073c,0x352aa8d1,0x3505f16e,0xb56da42d,0xb5820e19,
0x3566a5cc,0x3549bd68,0xb533ffcb,0xb553e4dd,0x3528770c,0x35038b1e,0xb56f33b3,0xb582aa7c,0x3564fb30,0x3547c7f6,0xb5361d48,0xb555bde6,0x3526419f,0x350121f4,0xb570be0a,0xb5834409,
0x35634b9d,0x3545ce2f,0xb53836d3,0xb557924c,0x35240897,0x34fd6bf8,0xb5724328,0xb583dabe,0x3561971d,0x3543d01e,0xb53a4c5f,0xb5596206,0x3521cc01,0x34f88e8a,0xb573c304,0xb5846e96,
0x355fddb7,0x3541cdce,0xb53c5de1,0xb55b2d08,0x351f8be8,0x34f3abb8,0xb5753d98,0xb584ff90,0x355e1f77,0x353fc74a,0xb53e6b4d,0xb55cf34a,0x351d485a,0x34eec39e,0xb576b2da,0xb5858da7,
0x355c5c66,0x353dbc9e,0xb5407498,0xb55eb4c1,0x351b0162,0x34e9d656,0xb57822c3,0xb58618d8,0x355a948e,0x353badd4,0xb54279b6,0xb5607164,0x3518b70e,0x34e4e3fb,0xb5798d49,0xb586a121,
0x34dfecab,0x348e2d99,0xb587267f,0xb58de6d7,0x33e56a87,0xb365b15b,0xb59194d1,0xb5921c09,0x34daf07e,0x3488f2af,0xb587a8ef,0xb58e390d,0x33cff7d5,0xb3885b8e,0xb591b505,0xb592098a,
0x34d5ef93,0x3483b4cc,0xb588286d,0xb58e882d,0x33ba80a0,0xb39ddb79,0xb591d211,0xb591f3e0,0x34d0ea04,0x347ce81d,0xb588a4f7,0xb58ed436,0x33a50560,0xb3b357f8,0xb591ebf3,0xb591db0c,
0x34cbdfec,0x34726126,0xb5891e8b,0xb58f1d26,0x338f868b,0xb3c8d093,0xb59202ab,0xb591bf0e,0x34c6d16a,0x3467d4ed,0xb5899525,0xb58f62fb,0x33740934,0xb3de44d3,0xb5921638,0xb5919fe7,
0x34c1be97,0x345d43ad,0xb58a08c4,0xb58fa5b5,0x33490008,0xb3f3b441,0xb592269b,0xb5917d97,0x34bca791,0x3452ada0,0xb58a7965,0xb58fe551,0x331df27f,0xb4048f33,0xb59233d2,0xb5915820,
0x34b78c73,0x34481302,0xb58ae704,0xb59021ce,0x32e5c312,0xb40f4166,0xb5923ddd,0xb5912f82,0x34b26d5a,0x343d740e,0xb58b51a1,0xb5905b2b,0x328f9c2b,0xb419f07e,0xb59244bd,0xb59103be,
0x34ad4a63,0x3432d0fd,0xb58bb938,0xb5909166,0x31e5c89b,0xb4249c3e,0xb5924870,0xb590d4d5,0x34a823aa,0x34282a0c,0xb58c1dc7,0xb590c47f,0xb165c8e2,0xb42f446d,0xb59248f7,0xb590a2c8,
0x34a2f94b,0x341d7f75,0xb58c7f4d,0xb590f474,0xb265c780,0xb439e8cf,0xb5924653,0xb5906d98,0x349dcb64,0x3412d174,0xb58cddc6,0xb5912145,0xb2c90be6,0xb4448929,0xb5924082,0xb5903546,
0x34989a11,0x34082044,0xb58d3932,0xb5914af0,0xb30f97d8,0xb44f2540,0xb5923785,0xb58ff9d3,0x3493656e,0x33fad83f,0xb58d918d,0xb5917174,0xb33aa69f,0xb459bcd9,0xb5922b5d,0xb58fbb41,
0xb4644fb9,0xb4c520f2,0xb58f7991,0xb589bc05,0xb509eacf,0xb52e48db,0xb5810332,0xb56afede,0xb46edda6,0xb4ca30e8,0xb58f34c5,0xb5894668,0xb50c4943,0xb53070a8,0xb5805f52,0xb56961ad,
0xb4796664,0xb4cf3c7b,0xb58eecde,0xb588cdd2,0xb50ea4ac,0xb53294a0,0xb57f7151,0xb567bf6d,0xb481f4dd,0xb4d44390,0xb58ea1dd,0xb5885244,0xb510fcfd,0xb534b4b9,0xb57e1e75,0xb5661826,
0xb48733b6,0xb4d9460b,0xb58e53c4,0xb587d3c2,0xb5135229,0xb536d0e8,0xb57cc616,0xb5646be1,0xb48c6fa1,0xb4de43cf,0xb58e0296,0xb587524d,0xb515a424,0xb538e91f,0xb57b683c,0xb562baa9,
0xb491a880,0xb4e33cc2,0xb58dae53,0xb586cde9,0xb517f2e0,0xb53afd53,0xb57a04ee,0xb5610486,0xb496de37,0xb4e830c7,0xb58d56fd,0xb5864699,0xb51a3e50,0xb53d0d7a,0xb5789c34,0xb55f4981,
0xb49c10a8,0xb4ed1fc2,0xb58cfc96,0xb585bc5f,0xb51c8668,0xb53f1987,0xb5772e15,0xb55d89a5,0xb4a13fb7,0xb4f2099a,0xb58c9f21,0xb5852f3f,0xb51ecb1a,0xb541216f,0xb575ba9b,0xb55bc4fb,
0xb4a66b46,0xb4f6ee32,0xb58c3ea0,0xb5849f3b,0xb5210c5c,0xb5432527,0xb57441cc,0xb559fb8d,0xb4ab933a,0xb4fbcd6e,0xb58bdb13,0xb5840c58,0xb5234a1f,0xb54524a3,0xb572c3b2,0xb5582d65,
0xb4b0b775,0xb500539b,0xb58b747e,0xb5837697,0xb5258458,0xb5471fd9,0xb5714053,0xb5565a8d,0xb4b5d7db,0xb502bdb6,0xb58b0ae4,0xb582ddfc,0xb527bafa,0xb54916be,0xb56fb7ba,0xb554830e,
0xb4baf44f,0xb50524fb,0xb58a9e45,0xb582428b,0xb529edf8,0xb54b0946,0xb56e29ed,0xb552a6f4,0xb4c00cb6,0xb507895d,0xb58a2ea4,0xb581a446,0xb52c1d48,0xb54cf767,0xb56c96f6,0xb550c649,
0x35924925,0x358f7991,0,0xb4644fb9,0x3587267f,0x357343a6,0xb4dfecab,0xb5228b42,0x35924653,0x358f1d26,0xb265c780,0xb4726126,0x35867407,0x35714053,0xb4e68aa8,0xb5258458,
0x35923ddd,0x358ebb35,0xb2e5c312,0xb480349d,0x3585bc5f,0x356f33b3,0xb4ed1fc2,0xb528770c,0x35922fc4,0x358e53c4,0xb32c4cc4,0xb48733b6,0x3584ff90,0x356d1ddb,0xb4f3abb8,0xb52b6341,
0x35921c09,0x358de6d7,0xb365b15b,0xb48e2d99,0x35843da0,0x356afede,0xb4fa2e49,0xb52e48db,0x359202ab,0x358d7471,0xb38f868b,0xb4952201,0x35837697,0x3568d6d2,0xb500539b,0xb53127bd,
0x3591e3ac,0x358cfc96,0xb3ac2ee1,0xb49c10a8,0x3582aa7c,0x3566a5cc,0xb5038b1e,0xb533ffcb,0x3591bf0e,0x358c7f4d,0xb3c8d093,0xb4a2f94b,0x3581d958,0x35646be1,0xb506bd8f,0xb536d0e8,
0x359194d1,0x358bfc98,0xb3e56a87,0xb4a9dba6,0x35810332,0x35622928,0xb509eacf,0xb5399af8,0x359164f7,0x358b747e,0xb400fdd1,0xb4b0b775,0x35802813,0x355fddb7,0xb50d12bd,0xb53c5de1,
0x35912f82,0x358ae704,0xb40f4166,0xb4b78c73,0x357e9006,0x355d89a5,0xb510353b,0xb53f1987,0x3590f474,0x358a542f,0xb41d7f75,0xb4be5a5e,0x357cc616,0x355b2d08,0xb5135229,0xb541cdce,
0x3590b3d0,0x3589bc05,0xb42bb772,0xb4c520f2,0x357af267,0x3558c7f8,0xb516696a,0xb5447a9d,0x35906d98,0x35891e8b,0xb439e8cf,0xb4cbdfec,0x3579150b,0x35565a8d,0xb5197ade,0xb5471fd9,
0x359021ce,0x35887bc8,0xb4481302,0xb4d2970b,0x35772e15,0x3553e4dd,0xb51c8668,0xb549bd68,0x358fd075,0x3587d3c2,0xb456357f,0xb4d9460b,0x35753d98,0x35516703,0xb51f8be8,0xb54c532f,
0x354ee116,0x35228b42,0xb54ee116,0xb57343a6,0x34dfecab,0x34644fb9,0xb587267f,0xb58f7991,0x354c532f,0x351f8be8,0xb5516703,0xb5753d98,0x34d9460b,0x3456357f,0xb587d3c2,0xb58fd075,
0x3549bd68,0x351c8668,0xb553e4dd,0xb5772e15,0x34d2970b,0x34481302,0xb5887bc8,0xb59021ce,0x35471fd9,0x35197ade,0xb5565a8d,0xb579150b,0x34cbdfec,0x3439e8cf,0xb5891e8b,0xb5906d98,
0x35447a9d,0x3516696a,0xb558c7f8,0xb57af267,0x34c520f2,0x342bb772,0xb589bc05,0xb590b3d0,0x3541cdce,0x35135229,0xb55b2d08,0xb57cc616,0x34be5a5e,0x341d7f75,0xb58a542f,0xb590f474,
0x353f1987,0x3510353b,0xb55d89a5,0xb57e9006,0x34b78c73,0x340f4166,0xb58ae704,0xb5912f82,0x353c5de1,0x350d12bd,0xb55fddb7,0xb5802813,0x34b0b775,0x3400fdd1,0xb58b747e,0xb59164f7,
0x35399af8,0x3509eacf,0xb5622928,0xb5810332,0x34a9dba6,0x33e56a87,0xb58bfc98,0xb59194d1,0x3536d0e8,0x3506bd8f,0xb5646be1,0xb581d958,0x34a2f94b,0x33c8d093,0xb58c7f4d,0xb591bf0e,
0x3533ffcb,0x35038b1e,0xb566a5cc,0xb582aa7c,0x349c10a8,0x33ac2ee1,0xb58cfc96,0xb591e3ac,0x353127bd,0x3500539b,0xb568d6d2,0xb5837697,0x34952201,0x338f868b,0xb58d7471,0xb59202ab,
0x352e48db,0x34fa2e49,0xb56afede,0xb5843da0,0x348e2d99,0x3365b15b,0xb58de6d7,0xb5921c09,0x352b6341,0x34f3abb8,0xb56d1ddb,0xb584ff90,0x348733b6,0x332c4cc4,0xb58e53c4,0xb5922fc4,
0x3528770c,0x34ed1fc2,0xb56f33b3,0xb585bc5f,0x3480349d,0x32e5c312,0xb58ebb35,0xb5923ddd,0x35258458,0x34e68aa8,0xb5714053,0xb5867407,0x34726126,0x3265c780,0xb58f1d26,0xb5924653,
0,0xb4644fb9,0xb5924925,0xb58f7991,0xb4dfecab,0xb5228b42,0xb587267f,0xb57343a6,0xb265c780,0xb4726126,0xb5924653,0xb58f1d26,0xb4e68aa8,0xb5258458,0xb5867407,0xb5714053,
0xb2e5c312,0xb480349d,0xb5923ddd,0xb58ebb35,0xb4ed1fc2,0xb528770c,0xb585bc5f,0xb56f33b3,0xb32c4cc4,0xb48733b6,0xb5922fc4,0xb58e53c4,0xb4f3abb8,0xb52b6341,0xb584ff90,0xb56d1ddb,
0xb365b15b,0xb48e2d99,0xb5921c09,0xb58de6d7,0xb4fa2e49,0xb52e48db,0xb5843da0,0xb56afede,0xb38f868b,0xb4952201,0xb59202ab,0xb58d7471,0xb500539b,0xb53127bd,0xb5837697,0xb568d6d2,
0xb3ac2ee1,0xb49c10a8,0xb591e3ac,0xb58cfc96,0xb5038b1e,0xb533ffcb,0xb582aa7c,0xb566a5cc,0xb3c8d093,0xb4a2f94b,0xb591bf0e,0xb58c7f4d,0xb506bd8f,0xb536d0e8,0xb581d958,0xb5646be1,
0xb3e56a87,0xb4a9dba6,0xb59194d1,0xb58bfc98,0xb509eacf,0xb5399af8,0xb5810332,0xb5622928,0xb400fdd1,0xb4b0b775,0xb59164f7,0xb58b747e,0xb50d12bd,0xb53c5de1,0xb5802813,0xb55fddb7,
0xb40f4166,0xb4b78c73,0xb5912f82,0xb58ae704,0xb510353b,0xb53f1987,0xb57e9006,0xb55d89a5,0xb41d7f75,0xb4be5a5e,0xb590f474,0xb58a542f,0xb5135229,0xb541cdce,0xb57cc616,0xb55b2d08,
0xb42bb772,0xb4c520f2,0xb590b3d0,0xb589bc05,0xb516696a,0xb5447a9d,0xb57af267,0xb558c7f8,0xb439e8cf,0xb4cbdfec,0xb5906d98,0xb5891e8b,0xb5197ade,0xb5471fd9,0xb579150b,0xb5565a8d,
0xb4481302,0xb4d2970b,0xb59021ce,0xb5887bc8,0xb51c8668,0xb549bd68,0xb5772e15,0xb553e4dd,0xb456357f,0xb4d9460b,0xb58fd075,0xb587d3c2,0xb51f8be8,0xb54c532f,0xb5753d98,0xb5516703,
0xb54ee116,0xb57343a6,0xb54ee116,0xb5228b42,0xb587267f,0xb58f7991,0xb4dfecab,0xb4644fb9,0xb5516703,0xb5753d98,0xb54c532f,0xb51f8be8,0xb587d3c2,0xb58fd075,0xb4d9460b,0xb456357f,
0xb553e4dd,0xb5772e15,0xb549bd68,0xb51c8668,0xb5887bc8,0xb59021ce,0xb4d2970b,0xb4481302,0xb5565a8d,0xb579150b,0xb5471fd9,0xb5197ade,0xb5891e8b,0xb5906d98,0xb4cbdfec,0xb439e8cf,
0xb558c7f8,0xb57af267,0xb5447a9d,0xb516696a,0xb589bc05,0xb590b3d0,0xb4c520f2,0xb42bb772,0xb55b2d08,0xb57cc616,0xb541cdce,0xb5135229,0xb58a542f,0xb590f474,0xb4be5a5e,0xb41d7f75,
0xb55d89a5,0xb57e9006,0xb53f1987,0xb510353b,0xb58ae704,0xb5912f82,0xb4b78c73,0xb40f4166,0xb55fddb7,0xb5802813,0xb53c5de1,0xb50d12bd,0xb58b747e,0xb59164f7,0xb4b0b775,0xb400fdd1,
0xb5622928,0xb5810332,0xb5399af8,0xb509eacf,0xb58bfc98,0xb59194d1,0xb4a9dba6,0xb3e56a87,0xb5646be1,0xb581d958,0xb536d0e8,0xb506bd8f,0xb58c7f4d,0xb591bf0e,0xb4a2f94b,0xb3c8d093,
0xb566a5cc,0xb582aa7c,0xb533ffcb,0xb5038b1e,0xb58cfc96,0xb591e3ac,0xb49c10a8,0xb3ac2ee1,0xb568d6d2,0xb5837697,0xb53127bd,0xb500539b,0xb58d7471,0xb59202ab,0xb4952201,0xb38f868b,
0xb56afede,0xb5843da0,0xb52e48db,0xb4fa2e49,0xb58de6d7,0xb5921c09,0xb48e2d99,0xb365b15b,0xb56d1ddb,0xb584ff90,0xb52b6341,0xb4f3abb8,0xb58e53c4,0xb5922fc4,0xb48733b6,0xb32c4cc4,
0xb56f33b3,0xb585bc5f,0xb528770c,0xb4ed1fc2,0xb58ebb35,0xb5923ddd,0xb480349d,0xb2e5c312,0xb5714053,0xb5867407,0xb5258458,0xb4e68aa8,0xb58f1d26,0xb5924653,0xb4726126,0xb265c780,
0x35924925,0x358de6d7,0,0xb48e2d99,0x35810332,0x3558c7f8,0xb509eacf,0xb5447a9d,0x359244bd,0x358d56fd,0xb28f9c2b,0xb496de37,0x357fe10a,0x3555bde6,0xb50ddbe0,0xb547c7f6,
0x35923785,0x358cbe9f,0xb30f97d8,0xb49f85bf,0x357dac47,0x3552a6f4,0xb511c466,0xb54b0946,0x3592217f,0x358c1dc7,0xb35758f4,0xb4a823aa,0x357b683c,0x354f8352,0xb515a424,0xb54e3e5b,
0x359202ab,0x358b747e,0xb38f868b,0xb4b0b775,0x3579150b,0x354c532f,0xb5197ade,0xb5516703,0x3591db0c,0x358ac2cf,0xb3b357f8,0xb4b9409a,0x3576b2da,0x354916be,0xb51d485a,0xb554830e,
0x3591aaa3,0x358a08c4,0xb3d71e96,0xb4c1be97,0x357441cc,0x3545ce2f,0xb5210c5c,0xb557924c,0x35917174,0x35894668,0xb3fad83f,0xb4ca30e8,0x3571c208,0x354279b6,0xb524c6aa,0xb55a948e,
0x35912f82,0x35887bc8,0xb40f4166,0xb4d2970b,0x356f33b3,0x353f1987,0xb528770c,0xb55d89a5,0x3590e4d1,0x3587a8ef,0xb4210e0b,0xb4daf07e,0x356c96f6,0x353badd4,0xb52c1d48,0xb5607164,
0x35909166,0x3586cde9,0xb432d0fd,0xb4e33cc2,0x3569ebf9,0x353836d3,0xb52fb925,0xb5634b9d,0x35903546,0x3585eac5,0xb4448929,0xb4eb7b55,0x356732e4,0x3534b4b9,0xb5334a6d,0xb5661826,
0x358fd075,0x3584ff90,0xb456357f,0xb4f3abb8,0x35646be1,0x353127bd,0xb536d0e8,0xb568d6d2,0x358f62fb,0x35840c58,0xb467d4ed,0xb4fbcd6e,0x3561971d,0x352d9015,0xb53a4c5f,0xb56b8777,
0x358eecde,0x3583112b,0xb4796664,0xb501effd,0x355eb4c1,0x3529edf8,0xb53dbc9e,0xb56e29ed,0x358e6e25,0x35820e19,0xb485746b,0xb505f16e,0x355bc4fb,0x3526419f,0xb541216f,0xb570be0a,
0x35228b42,0x34c520f2,0xb57343a6,0xb589bc05,0x33e56a87,0xb42bb772,0xb59194d1,0xb590b3d0,0x351ecb1a,0x34bca791,0xb575ba9b,0xb58a7965,0x33c1a8d5,0xb43d740e,0xb591c8bc,0xb5905b2b,
0x351b0162,0x34b422d2,0xb57822c3,0xb58b2e6d,0x339ddb79,0xb44f2540,0xb591f3e0,0xb58ff9d3,0x35172e53,0x34ab933a,0xb57a7bf8,0xb58bdb13,0x33740934,0xb460c9f8,0xb5921638,0xb58f8fcf,
0x35135229,0x34a2f94b,0xb57cc616,0xb58c7f4d,0x332c4cc4,0xb4726126,0xb5922fc4,0xb58f1d26,0x350f6d20,0x349a558c,0xb57f00fa,0xb58d1b10,0x32c90be6,0xb481f4dd,0xb5924082,0xb58ea1dd,
0x350b7f72,0x3491a880,0xb5809641,0xb58dae53,0x31e5c89b,0xb48ab153,0xb5924870,0xb58e1dfd,0x3507895d,0x3488f2af,0xb581a446,0xb58e390d,0xb22c561c,0xb493656e,0xb592478f,0xb58d918d,
0x35038b1e,0x3480349d,0xb582aa7c,0xb58ebb35,0xb2e5c312,0xb49c10a8,0xb5923ddd,0xb58cfc96,0x34ff09e5,0x346edda6,0xb583a8d3,0xb58f34c5,0xb33aa69f,0xb4a4b27c,0xb5922b5d,0xb58c5f21,
0x34f6ee32,0x345d43ad,0xb5849f3b,0xb58fa5b5,0xb381303c,0xb4ad4a63,0xb592100e,0xb58bb938,0x34eec39e,0x344b9c60,0xb5858da7,0xb5900dfd,0xb3a50560,0xb4b5d7db,0xb591ebf3,0xb58b0ae4,
0x34e68aa8,0x3439e8cf,0xb5867407,0xb5906d98,0xb3c8d093,0xb4be5a5e,0xb591bf0e,0xb58a542f,0x34de43cf,0x34282a0c,0xb587524d,0xb590c47f,0xb3ec8fad,0xb4c6d16a,0xb5918961,0xb5899525,
0x34d5ef93,0x34166127,0xb588286d,0xb59112ae,0xb4082044,0xb4cf3c7b,0xb5914af0,0xb588cdd2,0x34cd8e73,0x34048f33,0xb588f659,0xb5915820,0xb419f07e,0xb4d79b11,0xb59103be,0xb587fe41,
0xb4dfecab,0xb52e48db,0xb587267f,0xb56afede,0xb5622928,0xb5843da0,0xb5399af8,0xb4fa2e49,0xb4e830c7,0xb531de66,0xb5864699,0xb5684b67,0xb564fb30,0xb5852f3f,0xb5361d48,0xb4f2099a,
0xb4f066e6,0xb5356939,0xb5855e9c,0xb56589f2,0xb567bf6d,0xb58618d8,0xb53294a0,0xb4e9d656,0xb4f88e8a,0xb538e91f,0xb5846e96,0xb562baa9,0xb56a75b4,0xb586fa5e,0xb52f0136,0xb4e194fc,
0xb500539b,0xb53c5de1,0xb5837697,0xb55fddb7,0xb56d1ddb,0xb587d3c2,0xb52b6341,0xb4d9460b,0xb5045835,0xb53fc74a,0xb58276ac,0xb55cf34a,0xb56fb7ba,0xb588a4f7,0xb527bafa,0xb4d0ea04,
0xb50854d7,0xb5432527,0xb5816ee5,0xb559fb8d,0xb5724328,0xb5896df1,0xb5240897,0xb4c88167,0xb50c4943,0xb5467741,0xb5805f52,0xb556f6af,0xb574bffe,0xb58a2ea4,0xb5204c53,0xb4c00cb6,
0xb510353b,0xb549bd68,0xb57e9006,0xb553e4dd,0xb5772e15,0xb58ae704,0xb51c8668,0xb4b78c73,0xb5141883,0xb54cf767,0xb57c5213,0xb550c649,0xb5798d49,0xb58b9706,0xb518b70e,0xb4af0122,
0xb517f2e0,0xb550250d,0xb57a04ee,0xb54d9b20,0xb57bdd75,0xb58c3ea0,0xb514de81,0xb4a66b46,0xb51bc415,0xb553462a,0xb577a8b8,0xb54a6395,0xb57e1e75,0xb58cddc6,0xb510fcfd,0xb49dcb64,
0xb51f8be8,0xb5565a8d,0xb5753d98,0xb5471fd9,0xb5802813,0xb58d7471,0xb50d12bd,0xb4952201,0xb5234a1f,0xb5596206,0xb572c3b2,0xb543d01e,0xb5813933,0xb58e0296,0xb5091ffd,0xb48c6fa1,
0xb526fe80,0xb55c5c66,0xb5703b2c,0xb5407498,0xb582428b,0xb58e882d,0xb50524fb,0xb483b4cc,0xb52aa8d1,0xb55f4981,0xb56da42d,0xb53d0d7a,0xb5834409,0xb58f052e,0xb50121f4,0xb475e411,
0xb58f7991,0xb5921c09,0xb4644fb9,0x3365b15b,0xb58bfc98,0xb57af267,0x34a9dba6,0x3516696a,0xb58fe551,0xb591fb72,0xb452ada0,0x3396b131,0xb58b51a1,0xb5789c34,0x34b26d5a,0x351a3e50,
0xb5904865,0xb591d211,0xb440fed7,0x33ba80a0,0xb58a9e45,0xb5763706,0x34baf44f,0x351e09eb,0xb590a2c8,0xb5919fe7,0xb42f446d,0x33de44d3,0xb589e28f,0xb573c304,0x34c37001,0x3521cc01,
0xb590f474,0xb59164f7,0xb41d7f75,0x3400fdd1,0xb5891e8b,0xb5714053,0x34cbdfec,0x35258458,0xb5913d66,0xb5912145,0xb40bb100,0x3412d174,0xb5885244,0xb56eaf1a,0x34d44390,0x352932b6,
0xb5917d97,0xb590d4d5,0xb3f3b441,0x34249c3e,0xb5877dc8,0xb56c0f80,0x34dc9a6b,0x352cd6e4,0xb591b505,0xb5907fab,0xb3cff7d5,0x34365d1f,0xb586a121,0xb56961ad,0x34e4e3fb,0x353070a8,
0xb591e3ac,0xb59021ce,0xb3ac2ee1,0x34481302,0xb585bc5f,0xb566a5cc,0x34ed1fc2,0x3533ffcb,0xb592098a,0xb58fbb41,0xb3885b8e,0x3459bcd9,0xb584cf8f,0xb563dc06,0x34f54d41,0x35378416,
0xb592269b,0xb58f4c0c,0xb3490008,0x346b5992,0xb583dabe,0xb5610486,0x34fd6bf8,0x353afd53,0xb5923ade,0xb58ed436,0xb3013cd8,0x347ce81d,0xb582ddfc,0xb55e1f77,0x3502bdb6,0x353e6b4d,
0xb5924653,0xb58e53c4,0xb265c780,0x348733b6,0xb581d958,0xb55b2d08,0x3506bd8f,0x3541cdce,0xb59248f7,0xb58dcac0,0x3165c8e2,0x348feb39,0xb580cce1,0xb5582d65,0x350ab54b,0x354524a3,
0xb59242cc,0xb58d3932,0x32ac543e,0x34989a11,0xb57f7151,0xb55520bc,0x350ea4ac,0x35486f98,0xb59233d2,0xb58c9f21,0x331df27f,0x34a13fb7,0xb57d397d,0xb552073c,0x35128b75,0x354bae79,
0x35924925,0x358bfc98,0,0xb4a9dba6,0x357343a6,0x35399af8,0xb5228b42,0xb5622928,0x359242cc,0x358b2e6d,0xb2ac543e,0xb4b422d2,0x35703b2c,0x35356939,0xb526fe80,0xb56589f2,
0x35922fc4,0x358a542f,0xb32c4cc4,0xb4be5a5e,0x356d1ddb,0x353127bd,0xb52b6341,0xb568d6d2,0x3592100e,0x35896df1,0xb381303c,0xb4c88167,0x3569ebf9,0x352cd6e4,0xb52fb925,0xb56c0f80,
0x3591e3ac,0x35887bc8,0xb3ac2ee1,0xb4d2970b,0x3566a5cc,0x3528770c,0xb533ffcb,0xb56f33b3,0x3591aaa3,0x35877dc8,0xb3d71e96,0xb4dc9a6b,0x35634b9d,0x35240897,0xb53836d3,0xb5724328,
0x359164f7,0x35867407,0xb400fdd1,0xb4e68aa8,0x355fddb7,0x351f8be8,0xb53c5de1,0xb5753d98,0x359112ae,0x35855e9c,0xb4166127,0xb4f066e6,0x355c5c66,0x351b0162,0xb5407498,0xb57822c3,
0x3590b3d0,0x35843da0,0xb42bb772,0xb4fa2e49,0x3558c7f8,0x3516696a,0xb5447a9d,0xb57af267,0x35904865,0x3583112b,0xb440fed7,0xb501effd,0x355520bc,0x3511c466,0xb5486f98,0xb57dac47,
0x358fd075,0x3581d958,0xb456357f,0xb506bd8f,0x35516703,0x350d12bd,0xb54c532f,0xb5802813,0x358f4c0c,0x35809641,0xb46b5992,0xb50b7f72,0x354d9b20,0x350854d7,0xb550250d,0xb5816ee5,
0x358ebb35,0x357e9006,0xb480349d,0xb510353b,0x3549bd68,0x35038b1e,0xb553e4dd,0xb582aa7c,0x358e1dfd,0x357bdd75,0xb48ab153,0xb514de81,0x3545ce2f,0x34fd6bf8,0xb557924c,0xb583dabe,
0x358d7471,0x3579150b,0xb4952201,0xb5197ade,0x3541cdce,0x34f3abb8,0xb55b2d08,0xb584ff90,0x358cbe9f,0x35763706,0xb49f85bf,0xb51e09eb,0x353dbc9e,0x34e9d656,0xb55eb4c1,0xb58618d8,
0x34dfecab,0x33e56a87,0xb587267f,0xb59194d1,0xb4644fb9,0xb509eacf,0xb58f7991,0xb5810332,0x34d5ef93,0x33ba80a0,0xb588286d,0xb591d211,0xb4796664,0xb50ea4ac,0xb58eecde,0xb57f7151,
0x34cbdfec,0x338f868b,0xb5891e8b,0xb59202ab,0xb48733b6,0xb5135229,0xb58e53c4,0xb57cc616,0x34c1be97,0x33490008,0xb58a08c4,0xb592269b,0xb491a880,0xb517f2e0,0xb58dae53,0xb57a04ee,
0x34b78c73,0x32e5c312,0xb58ae704,0xb5923ddd,0xb49c10a8,0xb51c8668,0xb58cfc96,0xb5772e15,0x34ad4a63,0x31e5c89b,0xb58bb938,0xb5924870,0xb4a66b46,0xb5210c5c,0xb58c3ea0,0xb57441cc,
0x34a2f94b,0xb265c780,0xb58c7f4d,0xb5924653,0xb4b0b775,0xb5258458,0xb58b747e,0xb5714053,0x34989a11,0xb30f97d8,0xb58d3932,0xb5923785,0xb4baf44f,0xb529edf8,0xb58a9e45,0xb56e29ed,
0x348e2d99,0xb365b15b,0xb58de6d7,0xb5921c09,0xb4c520f2,0xb52e48db,0xb589bc05,0xb56afede,0x3483b4cc,0xb39ddb79,0xb58e882d,0xb591f3e0,0xb4cf3c7b,0xb53294a0,0xb588cdd2,0xb567bf6d,
0x34726126,0xb3c8d093,0xb58f1d26,0xb591bf0e,0xb4d9460b,0xb536d0e8,0xb587d3c2,0xb5646be1,0x345d43ad,0xb3f3b441,0xb58fa5b5,0xb5917d97,0xb4e33cc2,0xb53afd53,0xb586cde9,0xb5610486,
0x34481302,0xb40f4166,0xb59021ce,0xb5912f82,0xb4ed1fc2,0xb53f1987,0xb585bc5f,0xb55d89a5,0x3432d0fd,0xb4249c3e,0xb5909166,0xb590d4d5,0xb4f6ee32,0xb5432527,0xb5849f3b,0xb559fb8d,
0x341d7f75,0xb439e8cf,0xb590f474,0xb5906d98,0xb500539b,0xb5471fd9,0xb5837697,0xb5565a8d,0x34082044,0xb44f2540,0xb5914af0,0xb58ff9d3,0xb50524fb,0xb54b0946,0xb582428b,0xb552a6f4,
0xb54ee116,0xb5810332,0xb54ee116,0xb509eacf,0xb58f7991,0xb59194d1,0xb4644fb9,0x33e56a87,0xb552a6f4,0xb582428b,0xb54b0946,0xb50524fb,0xb58ff9d3,0xb5914af0,0xb44f2540,0x34082044,
0xb5565a8d,0xb5837697,0xb5471fd9,0xb500539b,0xb5906d98,0xb590f474,0xb439e8cf,0x341d7f75,0xb559fb8d,0xb5849f3b,0xb5432527,0xb4f6ee32,0xb590d4d5,0xb5909166,0xb4249c3e,0x3432d0fd,
0xb55d89a5,0xb585bc5f,0xb53f1987,0xb4ed1fc2,0xb5912f82,0xb59021ce,0xb40f4166,0x34481302,0xb5610486,0xb586cde9,0xb53afd53,0xb4e33cc2,0xb5917d97,0xb58fa5b5,0xb3f3b441,0x345d43ad,
0xb5646be1,0xb587d3c2,0xb536d0e8,0xb4d9460b,0xb591bf0e,0xb58f1d26,0xb3c8d093,0x34726126,0xb567bf6d,0xb588cdd2,0xb53294a0,0xb4cf3c7b,0xb591f3e0,0xb58e882d,0xb39ddb79,0x3483b4cc,
0xb56afede,0xb589bc05,0xb52e48db,0xb4c520f2,0xb5921c09,0xb58de6d7,0xb365b15b,0x348e2d99,0xb56e29ed,0xb58a9e45,0xb529edf8,0xb4baf44f,0xb5923785,0xb58d3932,0xb30f97d8,0x34989a11,
0xb5714053,0xb58b747e,0xb5258458,0xb4b0b775,0xb5924653,0xb58c7f4d,0xb265c780,0x34a2f94b,0xb57441cc,0xb58c3ea0,0xb5210c5c,0xb4a66b46,0xb5924870,0xb58bb938,0x31e5c89b,0x34ad4a63,
0xb5772e15,0xb58cfc96,0xb51c8668,0xb49c10a8,0xb5923ddd,0xb58ae704,0x32e5c312,0x34b78c73,0xb57a04ee,0xb58dae53,0xb517f2e0,0xb491a880,0xb592269b,0xb58a08c4,0x33490008,0x34c1be97,
0xb57cc616,0xb58e53c4,0xb5135229,0xb48733b6,0xb59202ab,0xb5891e8b,0x338f868b,0x34cbdfec,0xb57f7151,0xb58eecde,0xb50ea4ac,0xb4796664,0xb591d211,0xb588286d,0x33ba80a0,0x34d5ef93,
0xb587267f,0xb5622928,0x34dfecab,0x35399af8,0xb5228b42,0xb4a9dba6,0x357343a6,0x358bfc98,0xb58618d8,0xb55eb4c1,0x34e9d656,0x353dbc9e,0xb51e09eb,0xb49f85bf,0x35763706,0x358cbe9f,
0xb584ff90,0xb55b2d08,0x34f3abb8,0x3541cdce,0xb5197ade,0xb4952201,0x3579150b,0x358d7471,0xb583dabe,0xb557924c,0x34fd6bf8,0x3545ce2f,0xb514de81,0xb48ab153,0x357bdd75,0x358e1dfd,
0xb582aa7c,0xb553e4dd,0x35038b1e,0x3549bd68,0xb510353b,0xb480349d,0x357e9006,0x358ebb35,0xb5816ee5,0xb550250d,0x350854d7,0x354d9b20,0xb50b7f72,0xb46b5992,0x35809641,0x358f4c0c,
0xb5802813,0xb54c532f,0x350d12bd,0x35516703,0xb506bd8f,0xb456357f,0x3581d958,0x358fd075,0xb57dac47,0xb5486f98,0x3511c466,0x355520bc,0xb501effd,0xb440fed7,0x3583112b,0x35904865,
0xb57af267,0xb5447a9d,0x3516696a,0x3558c7f8,0xb4fa2e49,0xb42bb772,0x35843da0,0x3590b3d0,0xb57822c3,0xb5407498,0x351b0162,0x355c5c66,0xb4f066e6,0xb4166127,0x35855e9c,0x359112ae,
0xb5753d98,0xb53c5de1,0x351f8be8,0x355fddb7,0xb4e68aa8,0xb400fdd1,0x35867407,0x359164f7,0xb5724328,0xb53836d3,0x35240897,0x35634b9d,0xb4dc9a6b,0xb3d71e96,0x35877dc8,0x3591aaa3,
0xb56f33b3,0xb533ffcb,0x3528770c,0x3566a5cc,0xb4d2970b,0xb3ac2ee1,0x35887bc8,0x3591e3ac,0xb56c0f80,0xb52fb925,0x352cd6e4,0x3569ebf9,0xb4c88167,0xb381303c,0x35896df1,0x3592100e,
0xb568d6d2,0xb52b6341,0x353127bd,0x356d1ddb,0xb4be5a5e,0xb32c4cc4,0x358a542f,0x35922fc4,0xb56589f2,0xb526fe80,0x35356939,0x35703b2c,0xb4b422d2,0xb2ac543e,0x358b2e6d,0x359242cc,
0x35924925,0x3589bc05,0,0xb4c520f2,0x35622928,0x3516696a,0xb5399af8,0xb57af267,0x35924082,0x3588a4f7,0xb2c90be6,0xb4d0ea04,0x355e1f77,0x3510fcfd,0xb53e6b4d,0xb57e1e75,
0x3592269b,0x35877dc8,0xb3490008,0xb4dc9a6b,0x3559fb8d,0x350b7f72,0xb5432527,0xb5809641,0x3591fb72,0x35864699,0xb396b131,0xb4e830c7,0x3555bde6,0x3505f16e,0xb547c7f6,0xb5820e19,
0x3591bf0e,0x3584ff90,0xb3c8d093,0xb4f3abb8,0x35516703,0x3500539b,0xb54c532f,0xb5837697,0x35917174,0x3583a8d3,0xb3fad83f,0xb4ff09e5,0x354cf767,0x34f54d41,0xb550c649,0xb584cf8f,
0x359112ae,0x3582428b,0xb4166127,0xb50524fb,0x35486f98,0x34e9d656,0xb55520bc,0xb58618d8,0x3590a2c8,0x3580cce1,0xb42f446d,0xb50ab54b,0x3543d01e,0x34de43cf,0xb5596206,0xb587524d,
0x359021ce,0x357e9006,0xb4481302,0xb510353b,0x353f1987,0x34d2970b,0xb55d89a5,0xb5887bc8,0x358f8fcf,0x357b683c,0xb460c9f8,0xb515a424,0x353a4c5f,0x34c6d16a,0xb561971d,0xb5899525,
0x358eecde,0x357822c3,0xb4796664,0xb51b0162,0x35356939,0x34baf44f,0xb56589f2,0xb58a9e45,0x358e390d,0x3574bffe,0xb488f2af,0xb5204c53,0x353070a8,0x34af0122,0xb56961ad,0xb58b9706,
0x358d7471,0x35714053,0xb4952201,0xb5258458,0x352b6341,0x34a2f94b,0xb56d1ddb,0xb58c7f4d,0x358c9f21,0x356da42d,0xb4a13fb7,0xb52aa8d1,0x3526419f,0x3496de37,0xb570be0a,0xb58d56fd,
0x358bb938,0x3569ebf9,0xb4ad4a63,0xb52fb925,0x35210c5c,0x348ab153,0xb57441cc,0xb58e1dfd,0x358ac2cf,0x35661826,0xb4b9409a,0xb534b4b9,0x351bc415,0x347ce81d,0xb577a8b8,0xb58ed436,
0x34644fb9,0xb42bb772,0xb58f7991,0xb590b3d0,0xb509eacf,0xb558c7f8,0xb5810332,0xb5447a9d,0x344b9c60,0xb4448929,0xb5900dfd,0xb5903546,0xb50f6d20,0xb55cf34a,0xb57f00fa,0xb53fc74a,
0x3432d0fd,0xb45d43ad,0xb5909166,0xb58fa5b5,0xb514de81,0xb5610486,0xb57bdd75,0xb53afd53,0x3419f07e,0xb475e411,0xb59103be,0xb58f052e,0xb51a3e50,0xb564fb30,0xb5789c34,0xb5361d48,
0x3400fdd1,0xb48733b6,0xb59164f7,0xb58e53c4,0xb51f8be8,0xb568d6d2,0xb5753d98,0xb53127bd,0x33cff7d5,0xb493656e,0xb591b505,0xb58d918d,0xb524c6aa,0xb56c96f6,0xb571c208,0xb52c1d48,
0x339ddb79,0xb49f85bf,0xb591f3e0,0xb58cbe9f,0xb529edf8,0xb5703b2c,0xb56e29ed,0xb526fe80,0x335758f4,0xb4ab933a,0xb592217f,0xb58bdb13,0xb52f0136,0xb573c304,0xb56a75b4,0xb521cc01,
0x32e5c312,0xb4b78c73,0xb5923ddd,0xb58ae704,0xb533ffcb,0xb5772e15,0xb566a5cc,0xb51c8668,0x3165c8e2,0xb4c37001,0xb59248f7,0xb589e28f,0xb538e91f,0xb57a7bf8,0xb562baa9,0xb5172e53,
0xb2ac543e,0xb4cf3c7b,0xb59242cc,0xb588cdd2,0xb53dbc9e,0xb57dac47,0xb55eb4c1,0xb511c466,0xb33aa69f,0xb4daf07e,0xb5922b5d,0xb587a8ef,0xb54279b6,0xb5805f52,0xb55a948e,0xb50c4943,
0xb38f868b,0xb4e68aa8,0xb59202ab,0xb5867407,0xb5471fd9,0xb581d958,0xb5565a8d,0xb506bd8f,0xb3c1a8d5,0xb4f2099a,0xb591c8bc,0xb5852f3f,0xb54bae79,0xb5834409,0xb552073c,0xb50121f4,
0xb3f3b441,0xb4fd6bf8,0xb5917d97,0xb583dabe,0xb550250d,0xb5849f3b,0xb54d9b20,0xb4f6ee32,0xb412d174,0xb5045835,0xb5912145,0xb58276ac,0xb554830e,0xb585eac5,0xb54916be,0xb4eb7b55,
0xb587267f,0xb5921c09,0xb4dfecab,0xb365b15b,0xb58bfc98,0xb56afede,0x34a9dba6,0x352e48db,0xb5885244,0xb5923ade,0xb4d44390,0xb3013cd8,0xb58b0ae4,0xb56732e4,0x34b5d7db,0x35334a6d,
0xb5896df1,0xb5924870,0xb4c88167,0xb1e5c89b,0xb58a08c4,0xb5634b9d,0x34c1be97,0x353836d3,0xb58a7965,0xb59244bd,0xb4bca791,0x328f9c2b,0xb588f659,0xb55f4981,0x34cd8e73,0x353d0d7a,
0xb58b747e,0xb5922fc4,0xb4b0b775,0x332c4cc4,0xb587d3c2,0xb55b2d08,0x34d9460b,0x3541cdce,0xb58c5f21,0xb592098a,0xb4a4b27c,0x33885b8e,0xb586a121,0xb556f6af,0x34e4e3fb,0x35467741,
0xb58d3932,0xb591d211,0xb4989a11,0x33ba80a0,0xb5855e9c,0xb552a6f4,0x34f066e6,0x354b0946,0xb58e0296,0xb5918961,0xb48c6fa1,0x33ec8fad,0xb5840c58,0xb54e3e5b,0x34fbcd6e,0x354f8352,
0xb58ebb35,0xb5912f82,0xb480349d,0x340f4166,0xb582aa7c,0xb549bd68,0x35038b1e,0x3553e4dd,0xb58f62fb,0xb590c47f,0xb467d4ed,0x34282a0c,0xb5813933,0xb54524a3,0x35091ffd,0x35582d65,
0xb58ff9d3,0xb5904865,0xb44f2540,0x3440fed7,0xb57f7151,0xb5407498,0x350ea4ac,0x355c5c66,0xb5907fab,0xb58fbb41,0xb4365d1f,0x3459bcd9,0xb57c5213,0xb53badd4,0x35141883,0x35607164,
0xb590f474,0xb58f1d26,0xb41d7f75,0x34726126,0xb579150b,0xb536d0e8,0x35197ade,0x35646be1,0xb5915820,0xb58e6e25,0xb4048f33,0x3485746b,0xb575ba9b,0xb531de66,0x351ecb1a,0x35684b67,
0xb591aaa3,0xb58dae53,0xb3d71e96,0x3491a880,0xb5724328,0xb52cd6e4,0x35240897,0x356c0f80,0xb591ebf3,0xb58cddc6,0xb3a50560,0x349dcb64,0xb56eaf1a,0xb527bafa,0x352932b6,0x356fb7ba,
0xb5228b42,0xb48e2d99,0x357343a6,0x358de6d7,0x33e56a87,0x34fa2e49,0x359194d1,0x35843da0,0xb51d485a,0xb481f4dd,0x3576b2da,0x358ea1dd,0x340bb100,0x3502bdb6,0x35913d66,0x3582ddfc,
0xb517f2e0,0xb46b5992,0x357a04ee,0x358f4c0c,0x34249c3e,0x350854d7,0x3590d4d5,0x35816ee5,0xb5128b75,0xb452ada0,0x357d397d,0x358fe551,0x343d740e,0x350ddbe0,0x35905b2b,0x357fe10a,
0xb50d12bd,0xb439e8cf,0x35802813,0x35906d98,0x3456357f,0x35135229,0x358fd075,0x357cc616,0xb507895d,0xb4210e0b,0x3581a446,0x3590e4d1,0x346edda6,0x3518b70e,0x358f34c5,0x35798d49,
0xb501effd,0xb4082044,0x3583112b,0x35914af0,0x3483b4cc,0x351e09eb,0x358e882d,0x35763706,0xb4f88e8a,0xb3de44d3,0x35846e96,0x35919fe7,0x348feb39,0x35234a1f,0x358dcac0,0x3572c3b2,
0xb4ed1fc2,0xb3ac2ee1,0x3585bc5f,0x3591e3ac,0x349c10a8,0x3528770c,0x358cfc96,0x356f33b3,0xb4e194fc,0xb3740934,0x3586fa5e,0x35921638,0x34a823aa,0x352d9015,0x358c1dc7,0x356b8777,
0xb4d5ef93,0xb30f97d8,0x3588286d,0x35923785,0x34b422d2,0x353294a0,0x358b2e6d,0x3567bf6d,0xb4ca30e8,0xb22c561c,0x35894668,0x3592478f,0x34c00cb6,0x35378416,0x358a2ea4,0x3563dc06,
0xb4be5a5e,0x3265c780,0x358a542f,0x35924653,0x34cbdfec,0x353c5de1,0x35891e8b,0x355fddb7,0xb4b26d5a,0x331df27f,0x358b51a1,0x359233d2,0x34d79b11,0x3541216f,0x3587fe41,0x355bc4fb,
0xb4a66b46,0x3381303c,0x358c3ea0,0x3592100e,0x34e33cc2,0x3545ce2f,0x3586cde9,0x3557924c,0xb49a558c,0x33b357f8,0x358d1b10,0x3591db0c,0x34eec39e,0x354a6395,0x35858da7,0x3553462a,
0x3f800000,0x3f800000,0,0,0x3f800000,0x3f800000,0,0,0x3f800000,0x3f7fe638,0,0x3ce5c143,0x3f7ff98e,0x3f7fc600,0x3c65c70c,0x3d2c49b7,
0x3f800000,0x3f7f98e6,0,0x3d65aa20,0x3f7fe638,0x3f7f181a,0x3ce5c143,0x3dac22ae,0x3f800000,0x3f7f181a,0,0x3dac22ae,0x3f7fc600,0x3f7df69c,0x3d2c49b7,0x3e00e941,
0x3f800000,0x3f7e63ec,0,0x3de54da2,0x3f7f98e6,0x3f7c620b,0x3d65aa20,0x3e2b86c0,0x3f800000,0x3f7d7c83,0,0x3e0f2533,0x3f7f5eee,0x3f7a5b1d,0x3d8f7f7c,0x3e55d687,
0x3f800000,0x3f7c620b,0,0x3e2b86c0,0x3f7f181a,0x3f77e2bd,0x3dac22ae,0x3e7fc568,0x3f800000,0x3f7b14be,0,0x3e47c5c2,0x3f7ec46d,0x3f74fa0b,0x3dc8bd36,0x3e94a031,
0x3f800000,0x3f7994e0,0,0x3e63dc87,0x3f7e63ec,0x3f71a257,0x3de54da2,0x3ea91a56,0x3f800000,0x3f77e2bd,0,0x3e7fc568,0x3f7df69c,0x3f6ddd24,0x3e00e941,0x3ebd47db,
0x3f800000,0x3f75fead,0,0x3e8dbd62,0x3f7d7c83,0x3f69ac2a,0x3e0f2533,0x3ed11f9b,0x3f800000,0x3f73e912,0,0x3e9b7b85,0x3f7cf5a5,0x3f65114d,0x3e1d59f0,0x3ee49898,
0x3f800000,0x3f71a257,0,0x3ea91a56,0x3f7c620b,0x3f600ea4,0x3e2b86c0,0x3ef7aa01,0x3f800000,0x3f6f2af0,0,0x3eb69719,0x3f7bc1bb,0x3f5aa675,0x3e39aaee,0x3f052598,
0x3f800000,0x3f6c835e,0,0x3ec3ef15,0x3f7b14be,0x3f54db31,0x3e47c5c2,0x3f0e39da,0x3f800000,0x3f69ac2a,0,0x3ed11f9b,0x3f7a5b1d,0x3f4eaf7a,0x3e55d687,0x3f170daa,
0x3f800000,0x3f66a5e5,0,0x3ede2602,0x3f7994e0,0x3f48261c,0x3e63dc87,0x3f1f9d07,0x3f800000,0x3f63712c,0,0x3eeaffac,0x3f78c212,0x3f41420b,0x3e71d70e,0x3f27e411,
0x3f800000,0x3f600ea4,0,0x3ef7aa01,0x3f77e2bd,0x3f3a0669,0x3e7fc568,0x3f2fdf08,0x3f800000,0x3f5c7efc,0,0x3f02113a,0x3f76f6ed,0x3f32767c,0x3e86d370,0x3f378a4d,
0x3f800000,0x3f58c2eb,0,0x3f083341,0x3f75fead,0x3f2a95b0,0x3e8dbd62,0x3f3ee268,0x3f800000,0x3f54db31,0,0x3f0e39da,0x3f74fa0b,0x3f226799,0x3e94a031,0x3f45e403,
0x3f800000,0x3f50c899,0,0x3f1423cd,0x3f73e912,0x3f19efeb,0x3e9b7b85,0x3f4c8bf3,0x3f800000,0x3f4c8bf3,0,0x3f19efeb,0x3f72cbd1,0x3f11327b,0x3ea24f04,0x3f52d734,
0x3f800000,0x3f48261c,0,0x3f1f9d07,0x3f71a257,0x3f083341,0x3ea91a56,0x3f58c2eb,0x3f800000,0x3f4397f4,0,0x3f2529fe,0x3f706cb1,0x3efdec9f,0x3eafdd25,0x3f5e4c69,
0x3f800000,0x3f3ee268,0,0x3f2a95b0,0x3f6f2af0,0x3eeaffac,0x3eb69719,0x3f63712c,0x3f800000,0x3f3a0669,0,0x3f2fdf08,0x3f6ddd24,0x3ed7a83d,0x3ebd47db,0x3f682ee0,
0x3f800000,0x3f3504f3,0,0x3f3504f3,0x3f6c835e,0x3ec3ef15,0x3ec3ef15,0x3f6c835e,0x3f800000,0x3f2fdf08,0,0x3f3a0669,0x3f6b1db0,0x3eafdd25,0x3eca8c72,0x3f706cb1,
0x3f800000,0x3f2a95b0,0,0x3f3ee268,0x3f69ac2a,0x3e9b7b85,0x3ed11f9b,0x3f73e912,0x3f800000,0x3f2529fe,0,0x3f4397f4,0x3f682ee0,0x3e86d370,0x3ed7a83d,0x3f76f6ed,
0x3f800000,0x3f1f9d07,0,0x3f48261c,0x3f66a5e5,0x3e63dc87,0x3ede2602,0x3f7994e0,0x3f800000,0x3f19efeb,0,0x3f4c8bf3,0x3f65114d,0x3e39aaee,0x3ee49898,0x3f7bc1bb,
0x3f800000,0x3f1423cd,0,0x3f50c899,0x3f63712c,0x3e0f2533,0x3eeaffac,0x3f7d7c83,0x3f800000,0x3f0e39da,0,0x3f54db31,0x3f61c598,0x3dc8bd36,0x3ef15aea,0x3f7ec46d,
0x3f800000,0x3f083341,0,0x3f58c2eb,0x3f600ea4,0x3d65aa20,0x3ef7aa01,0x3f7f98e6,0x3f800000,0x3f02113a,0,0x3f5c7efc,0x3f5e4c69,0x3c65c70c,0x3efdec9f,0x3f7ff98e,
0x3f800000,0x3ef7aa01,0,0x3f600ea4,0x3f5c7efc,0xbce5c143,0x3f02113a,0x3f7fe638,0x3f800000,0x3eeaffac,0,0x3f63712c,0x3f5aa675,0xbd8f7f7c,0x3f052598,0x3f7f5eee,
0x3f800000,0x3ede2602,0,0x3f66a5e5,0x3f58c2eb,0xbde54da2,0x3f083341,0x3f7e63ec,0x3f800000,0x3ed11f9b,0,0x3f69ac2a,0x3f56d477,0xbe1d59f0,0x3f0b3a0f,0x3f7cf5a5,
0x3f800000,0x3ec3ef15,0,0x3f6c835e,0x3f54db31,0xbe47c5c2,0x3f0e39da,0x3f7b14be,0x3f800000,0x3eb69719,0,0x3f6f2af0,0x3f52d734,0xbe71d70e,0x3f11327b,0x3f78c212,
0x3f800000,0x3ea91a56,0,0x3f71a257,0x3f50c899,0xbe8dbd62,0x3f1423cd,0x3f75fead,0x3f800000,0x3e9b7b85,0,0x3f73e912,0x3f4eaf7a,0xbea24f04,0x3f170daa,0x3f72cbd1,
0x3f800000,0x3e8dbd62,0,0x3f75fead,0x3f4c8bf3,0xbeb69719,0x3f19efeb,0x3f6f2af0,0x3f800000,0x3e7fc568,0,0x3f77e2bd,0x3f4a5e20,0xbeca8c72,0x3f1cca6b,0x3f6b1db0,
0x3f800000,0x3e63dc87,0,0x3f7994e0,0x3f48261c,0xbede2602,0x3f1f9d07,0x3f66a5e5,0x3f800000,0x3e47c5c2,0,0x3f7b14be,0x3f45e403,0xbef15aea,0x3f226799,0x3f61c598,
0x3f800000,0x3e2b86c0,0,0x3f7c620b,0x3f4397f4,0xbf02113a,0x3f2529fe,0x3f5c7efc,0x3f800000,0x3e0f2533,0,0x3f7d7c83,0x3f41420b,0xbf0b3a0f,0x3f27e411,0x3f56d477,
0x3f800000,0x3de54da2,0,0x3f7e63ec,0x3f3ee268,0xbf1423cd,0x3f2a95b0,0x3f50c899,0x3f800000,0x3dac22ae,0,0x3f7f181a,0x3f3c7927,0xbf1cca6b,0x3f2d3eb9,0x3f4a5e20,
0x3f800000,0x3d65aa20,0,0x3f7f98e6,0x3f3a0669,0xbf2529fe,0x3f2fdf08,0x3f4397f4,0x3f800000,0x3ce5c143,0,0x3f7fe638,0x3f378a4d,0xbf2d3eb9,0x3f32767c,0x3f3c7927,
0x3f800000,0,0,0x3f800000,0x3f3504f3,0xbf3504f3,0x3f3504f3,0x3f3504f3,0x3f800000,0xbce5c143,0,0x3f7fe638,0x3f32767c,0xbf3c7927,0x3f378a4d,0x3f2d3eb9,
0x3f800000,0xbd65aa20,0,0x3f7f98e6,0x3f2fdf08,0xbf4397f4,0x3f3a0669,0x3f2529fe,0x3f800000,0xbdac22ae,0,0x3f7f181a,0x3f2d3eb9,0xbf4a5e20,0x3f3c7927,0x3f1cca6b,
0x3f800000,0xbde54da2,0,0x3f7e63ec,0x3f2a95b0,0xbf50c899,0x3f3ee268,0x3f1423cd,0x3f800000,0xbe0f2533,0,0x3f7d7c83,0x3f27e411,0xbf56d477,0x3f41420b,0x3f0b3a0f,
0x3f800000,0xbe2b86c0,0,0x3f7c620b,0x3f2529fe,0xbf5c7efc,0x3f4397f4,0x3f02113a,0x3f800000,0xbe47c5c2,0,0x3f7b14be,0x3f226799,0xbf61c598,0x3f45e403,0x3ef15aea,
};

#define BST 0
#define Z_SLEEP 3
#define ATOM_SLEEP 2
#define ZRETRY_SLEEP 1
#define YRETRY_SLEEP 1
#define RETRY_SLEEP 1
#define V_SLEEP 0
#define POLL_SLEEP 1
#define A_MID 1
#define PRIOA 3
#define PRIOB 1
constexpr int S_ = 2048, D_ = 448;
constexpr size_t WS_EP = 0;
constexpr size_t WS_CV = 4096, WS_CY = WS_CV + 256 * 128, WS_Z = WS_CY + 256 * 4 * 128;
constexpr size_t WS_V = WS_Z + 256 * 16384, WS_XT = WS_V + 64 * 32768, WS_Y = WS_XT + 448 * 8192, WS_END = WS_Y + 256 * 7168;
constexpr unsigned SENT = 0xffffffffu;

struct Params {
    const u16 *x, *wr, *wi, *are, *aim, *bre, *bim, *cre, *cim, *ldt, *dskip, *gw;
    u16* out;
    char* ws;
};

DI unsigned pk(float a, float b) { f2 v = {a, b}; return __builtin_bit_cast(unsigned, __builtin_convertvector(v, b2)); }
DI unsigned pkx(float a, float b) { unsigned r; asm("v_cvt_pk_bf16_f32 %0, %1, %2" : "=v"(r) : "v"(a), "v"(b)); return r; }
DI float bflo(unsigned u) { return __builtin_bit_cast(float, u << 16); }
DI float bfhi(unsigned u) { return __builtin_bit_cast(float, u & 0xffff0000u); }
DI float bf2f(u16 h) { return __builtin_bit_cast(float, (unsigned)h << 16); }
DI f4 mfma32(u4 a, u4 b, f4 c) { return __builtin_amdgcn_mfma_f32_16x16x32_bf16(__builtin_bit_cast(s8, a), __builtin_bit_cast(s8, b), c, 0, 0, 0); }
DI f4 mfma16(u2 a, u2 b, f4 c) { return __builtin_amdgcn_mfma_f32_16x16x16bf16_1k(__builtin_bit_cast(s4, a), __builtin_bit_cast(s4, b), c, 0, 0, 0); }
DI u2 trread(lptr p) { return __builtin_bit_cast(u2, __builtin_amdgcn_ds_read_tr16_b64_v4i16((LAS s4*)p)); }
DI u4 cat(u2 a, u2 b) { return u4{a.x, a.y, b.x, b.y}; }
DI f2 lo2(f4 v) { return f2{v.x, v.y}; }
DI f2 hi2(f4 v) { return f2{v.z, v.w}; }
DI float sinr(float rev) { return __builtin_amdgcn_sinf(rev); }
DI float cosr(float rev) { return __builtin_amdgcn_cosf(rev); }
DI const u4* tab4(int off) { return (const u4*)(g_tab + off); }
DI float sadd(float a, float b) { float r; asm("v_add_f32 %0, %1, %2" : "=v"(r) : "v"(a), "v"(b)); return r; }
DI float ssub(float a, float b) { float r; asm("v_sub_f32 %0, %1, %2" : "=v"(r) : "v"(a), "v"(b)); return r; }
DI float smul(float a, float b) { float r; asm("v_mul_f32 %0, %1, %2" : "=v"(r) : "v"(a), "v"(b)); return r; }
DI void glds16(const void* gsrc, unsigned lds_dst) {
    unsigned keep;
    asm volatile("s_mov_b32 %0, m0\n\ts_mov_b32 m0, %2\n\ts_nop 0\n\tglobal_load_lds_dwordx4 %1, off\n\ts_mov_b32 m0, %0" : "=&s"(keep) : "v"(gsrc), "s"(lds_dst) : "memory");
}
DI void glds16s(const void* sbase, unsigned voff, unsigned lds_dst) {
    unsigned keep;
    asm volatile("s_mov_b32 %0, m0\n\ts_mov_b32 m0, %3\n\ts_nop 0\n\tglobal_load_lds_dwordx4 %1, %2\n\ts_mov_b32 m0, %0" : "=&s"(keep) : "v"(voff), "s"(sbase), "s"(lds_dst) : "memory");
}
DI void glds4(const void* gsrc, unsigned lds_dst) {
    unsigned keep;
    asm volatile("s_mov_b32 %0, m0\n\ts_mov_b32 m0, %2\n\ts_nop 0\n\tglobal_load_lds_dword %1, off\n\ts_mov_b32 m0, %0" : "=&s"(keep) : "v"(gsrc), "s"(lds_dst) : "memory");
}
#define WAIT_VM0() asm volatile("s_waitcnt vmcnt(0)" ::: "memory")
#define WAIT_LGKM0() asm volatile("s_waitcnt lgkmcnt(0)" ::: "memory")
constexpr int SC1 = 16;
typedef __amdgpu_buffer_rsrc_t rsrc_t;
DI rsrc_t mkrsrc(const void* p, unsigned bytes) { return __builtin_amdgcn_make_buffer_rsrc((void*)p, 0, bytes, 0x00020000); }
DI void st16(rsrc_t r, unsigned off, u4 v) { __builtin_amdgcn_raw_buffer_store_b128(v, r, off, 0, SC1); }
DI u4 ld16(rsrc_t r, unsigned off) { return __builtin_amdgcn_raw_buffer_load_b128(r, off, 0, SC1); }
DI u2 ld8(rsrc_t r, unsigned off) { return __builtin_amdgcn_raw_buffer_load_b64(r, off, 0, SC1); }
DI u16 ld2(rsrc_t r, unsigned off) { return __builtin_amdgcn_raw_buffer_load_b16(r, off, 0, SC1); }
constexpr unsigned OOB = 0x80000000u;
template <class T> DI void pin(T& v) { asm volatile("" : "+v"(v)); }
DI float nadd(float a, float b) { float r; asm("v_sub_f32 %0, -%1, %2" : "=v"(r) : "v"(a), "v"(b)); return r; }
DI u2 ld8s(rsrc_t r, unsigned off, unsigned soff) { return __builtin_amdgcn_raw_buffer_load_b64(r, off, soff, SC1); }
DI u4 ldt16(rsrc_t r, unsigned off, unsigned soff) { return __builtin_amdgcn_raw_buffer_load_b128(r, off, soff, 0); }
DI f4 ldf16(rsrc_t r, unsigned off, unsigned soff) { return __builtin_bit_cast(f4, __builtin_amdgcn_raw_buffer_load_b128(r, off, soff, 0)); }
DI const f4* tw4(int off) { return (const f4*)(g_tw + off); }
DI unsigned perm(unsigned hi_src, unsigned lo_src, unsigned sel) { return __builtin_amdgcn_perm(hi_src, lo_src, sel); }

#define STAMP(i)
#define STAMPX(i)
#define STAMP_ARG
#define STAMP_PASS

constexpr int XS_PITCH = 928;
constexpr int L_XS = 0;
constexpr int L_GATE = L_XS + 32 * XS_PITCH;
constexpr int L_CTR = L_GATE + 256;
constexpr int L_ASM = L_CTR + 64;
constexpr int L_WI = L_ASM + 8192;
constexpr int L_TR = L_WI + 8192;
constexpr int L_GW = L_TR;
constexpr int L_KG = L_TR + 10240;
constexpr int L_EI = L_KG;
constexpr int KF_PITCH = 2688;
constexpr int L_KF = L_KG + 4 * 18432;
constexpr int L_UI = L_KF + 4 * KF_PITCH;
constexpr int L_TT = L_UI;
constexpr int L_T3 = L_ASM;
constexpr int L_VA = L_WI;
constexpr int L_F8 = L_TR;
constexpr int L_VS = L_KG + 2 * 2 * 4160 + 16384 + 128 * 272 - 2 * 2 * 4160 - 16384;
constexpr int L_YS = L_UI;
constexpr int TC_PITCH = 4160;
constexpr int L_TC = L_KG;
constexpr int L_CR = L_KG + 2 * 2 * TC_PITCH;
constexpr int L_T1 = L_WI;
constexpr int L_F8A = L_UI + 4 * 4608;
constexpr int L_GW2 = L_F8A + 2048;
constexpr int L_END = L_GW2 + 896;
static_assert(L_VS >= L_CR + 16384 && L_VS >= L_EI + 128 * 272 && L_VS + 32768 <= L_UI, "alias");
static_assert(L_END + 256 <= 163840, "lds");
static_assert(L_ASM % 16 == 0 && L_KG % 16 == 0, "align");
enum { C_BARA = 0, C_BARB = 1, C_EXP = 2 };

constexpr unsigned SPIN_CAP = 1u << 20, GSPIN_CAP = 1u << 16;

DI void lds_inc(unsigned addr) {
    u64 ex; unsigned one = 1;
    asm volatile("s_mov_b64 %0, exec\n\ts_mov_b64 exec, 1\n\tds_add_u32 %1, %2\n\ts_mov_b64 exec, %0" : "=&s"(ex) : "v"(addr), "v"(one) : "memory");
}
struct TeamBar {
    unsigned addr; unsigned tgt;
    DI void operator()() {
        tgt += 4;
        u64 ex; unsigned v, sv, cnt; unsigned one = 1;
        asm volatile("s_mov_b64 %0, exec\n\ts_mov_b64 exec, 1\n\tds_add_u32 %4, %5\n\ts_mov_b64 exec, %0\n\ts_mov_b32 %3, 0x100000\n"
                     "1:\n\tds_read_b32 %1, %4\n\ts_waitcnt lgkmcnt(0)\n\tv_readfirstlane_b32 %2, %1\n\ts_sub_u32 %3, %3, 1\n\ts_cbranch_scc1 2f\n\ts_cmp_lt_u32 %2, %6\n\ts_cbranch_scc1 1b\n2:"
                     : "=&s"(ex), "=&v"(v), "=&s"(sv), "=&s"(cnt) : "v"(addr), "v"(one), "s"(tgt) : "memory", "scc");
    }
};
DI void poll_tag(rsrc_t r, unsigned off, unsigned tag) {
    const unsigned o = __builtin_amdgcn_readfirstlane(off);
    for (unsigned spin = 0; spin < GSPIN_CAP; ++spin) {
        asm volatile("" ::: "memory");
        if ((unsigned)__builtin_amdgcn_readfirstlane(__builtin_amdgcn_raw_buffer_load_b32(r, o, 0, SC1)) == tag) break;
        __builtin_amdgcn_s_sleep(POLL_SLEEP);
    }
    asm volatile("" ::: "memory");
}
DI void poll_tags(rsrc_t r, unsigned off, unsigned tag) {
    for (unsigned spin = 0; spin < GSPIN_CAP; ++spin) {
        asm volatile("" ::: "memory");
        if (__all(__builtin_amdgcn_raw_buffer_load_b32(r, off, 0, SC1) == tag)) break;
        __builtin_amdgcn_s_sleep(POLL_SLEEP);
    }
    asm volatile("" ::: "memory");
}
typedef unsigned short us2 __attribute__((ext_vector_type(2)));
DI unsigned san(unsigned v) { return __builtin_bit_cast(unsigned, __builtin_elementwise_min(__builtin_bit_cast(us2, v), us2{0xfffe, 0xfffe})); }
DI unsigned unfilled(u4 p) { return max(max(p.x, p.y), max(p.z, p.w)) == SENT; }
DI bool unfilled16(u4 p) {
    const us2 m = __builtin_elementwise_max(__builtin_elementwise_max(__builtin_bit_cast(us2, p.x), __builtin_bit_cast(us2, p.y)), __builtin_elementwise_max(__builtin_bit_cast(us2, p.z), __builtin_bit_cast(us2, p.w)));
    return m.x == 0xffff || m.y == 0xffff;
}
DI void poll_fill(rsrc_t r, unsigned off) {
    const unsigned o = __builtin_amdgcn_readfirstlane(off);
    for (unsigned spin = 0; spin < GSPIN_CAP; ++spin) {
        asm volatile("" ::: "memory");
        if ((unsigned)__builtin_amdgcn_readfirstlane(__builtin_amdgcn_raw_buffer_load_b32(r, o, 0, SC1)) != SENT) break;
        __builtin_amdgcn_s_sleep(POLL_SLEEP);
    }
    asm volatile("" ::: "memory");
}
DI void poll_words_atomic(char* ws, unsigned off, unsigned want, bool equal) {
    for (unsigned spin = 0; spin < GSPIN_CAP; ++spin) {
        unsigned v; unsigned zero = 0;
        asm volatile("global_atomic_add %0, %1, %2, %3 sc0 sc1\n\ts_waitcnt vmcnt(0)" : "=&v"(v) : "v"(off), "v"(zero), "s"(ws) : "memory");
        if (__all((v == want) == equal)) break;
        __builtin_amdgcn_s_sleep(ATOM_SLEEP);
    }
    asm volatile("" ::: "memory");
}
DI u4 tagged(u2 v, unsigned tag) { return u4{v.x, tag, v.y, tag}; }
DI unsigned untagged(u4 p, unsigned tag) { return (p.y ^ tag) | (p.w ^ tag); }
DI u64 load_epoch(const char* ws) { return __hip_atomic_load((gu64*)(ws + WS_EP), RLX_AGENT); }
DI u64 next_epoch(u64 e) {
    return (((u64)(unsigned)__builtin_amdgcn_readfirstlane((unsigned)(e >> 32)) << 32) | (unsigned)__builtin_amdgcn_readfirstlane((unsigned)e)) + 1;
}
DI unsigned tag_of(u64 ep) { return (unsigned)ep * 0x9E3779B1u + 0x7F4A7C15u; }

template <int V> struct IC { static constexpr int value = V; };
struct C2 { f2 re, im; };
DI C2 cmul(C2 a, C2 e) { return C2{a.re * e.re - a.im * e.im, a.re * e.im + a.im * e.re}; }
DI C2 cexpl(f2 lr2, f2 lirev, float l) {
    const f2 a = lr2 * l, b = lirev * l;
    const f2 m = {__builtin_amdgcn_exp2f(a.x), __builtin_amdgcn_exp2f(a.y)};
    const float r0 = __builtin_amdgcn_fractf(b.x), r1 = __builtin_amdgcn_fractf(b.y);
    return C2{m * f2{cosr(r0), cosr(r1)}, m * f2{sinr(r0), sinr(r1)}};
}
DI f2 pmul(f2 a, f2 b) { f2 r; asm volatile("v_pk_mul_f32 %0, %1, %2" : "=v"(r) : "v"(a), "v"(b)); return r; }
DI f2 pfma(f2 a, f2 b, f2 c) { f2 r; asm volatile("v_pk_fma_f32 %0, %1, %2, %3" : "=v"(r) : "v"(a), "v"(b), "v"(c)); return r; }
DI f2 pfms(f2 a, f2 b, f2 c) { f2 r; asm volatile("v_pk_fma_f32 %0, %1, %2, %3 neg_lo:[0,0,1] neg_hi:[0,0,1]" : "=v"(r) : "v"(a), "v"(b), "v"(c)); return r; }
DI void cmul3(C2& r1, const C2& a1, const C2& e1, C2& r2, const C2& a2, const C2& e2, C2& r3, const C2& a3, const C2& e3) {
    const f2 p1 = pmul(a1.im, e1.im), p2 = pmul(a2.im, e2.im), p3 = pmul(a3.im, e3.im);
    const f2 q1 = pmul(a1.im, e1.re), q2 = pmul(a2.im, e2.re), q3 = pmul(a3.im, e3.re);
    const f2 x1 = pfms(a1.re, e1.re, p1), x2 = pfms(a2.re, e2.re, p2), x3 = pfms(a3.re, e3.re, p3);
    const f2 y1 = pfma(a1.re, e1.im, q1), y2 = pfma(a2.re, e2.im, q2), y3 = pfma(a3.re, e3.im, q3);
    r1 = C2{x1, y1}; r2 = C2{x2, y2}; r3 = C2{x3, y3};
}
DI void cmul2(C2& r1, const C2& a1, const C2& e1, C2& r2, const C2& a2, const C2& e2) {
    const f2 p1 = pmul(a1.im, e1.im), p2 = pmul(a2.im, e2.im), q1 = pmul(a1.im, e1.re), q2 = pmul(a2.im, e2.re);
    const f2 x1 = pfms(a1.re, e1.re, p1), x2 = pfms(a2.re, e2.re, p2), y1 = pfma(a1.re, e1.im, q1), y2 = pfma(a2.re, e2.im, q2);
    r1 = C2{x1, y1}; r2 = C2{x2, y2};
}
DI void put8(lptr re, lptr im, const C2* v, u4& kre, u4& kim) {
    u4 a, b;
#pragma unroll
    for (int i = 0; i < 4; ++i) {
        a[i] = pk(v[2 * i].re.x, v[2 * i + 1].re.x);
        kre[i] = pk(v[2 * i].re.y, v[2 * i + 1].re.y);
        b[i] = pk(v[2 * i].im.x, v[2 * i + 1].im.x);
        kim[i] = pk(v[2 * i].im.y, v[2 * i + 1].im.y);
    }
    pin(kre); pin(kim);
    *(LAS u4*)re = a; *(LAS u4*)im = b;
    asm volatile("" ::: "memory");
}
DI unsigned umax3(unsigned a, unsigned b, unsigned c) { return max(max(a, b), c); }
DI unsigned sel(bool c, unsigned a, unsigned b) { asm("" : "+v"(a), "+v"(b)); return c ? a : b; }
DI unsigned laddr(lptr p) { return (unsigned)(size_t)p; }

struct Late { f4 w02, w13; u2 dv0, dv1; u64 ep; unsigned clr, clo, vsr, eiw, eir, yr0, yr1, xr0, xr1, gta, oro; };
DI void load_late(const Params& p, int beta, int w8, int lane, Late& t) {
    const int n = lane & 15, j = w8 >> 1, jd = beta >> 6;
    const rsrc_t rtw = mkrsrc(g_tw, sizeof(g_tw)), rds = mkrsrc(p.dskip, 448 * 2);
    t.w02 = ldf16(rtw, n * 32, TW_5 * 4 + j * 512); t.w13 = ldf16(rtw, n * 32, TW_5 * 4 + j * 512 + 16);
    t.dv0 = __builtin_amdgcn_raw_buffer_load_b64(rds, n * 8, jd * 224, 0);
    t.dv1 = __builtin_amdgcn_raw_buffer_load_b64(rds, min(16 + n, 27) * 8, jd * 224, 0);
}
DI void load_notes(const Params& p, int beta, int lane, Late& t) {
    const int sig = beta & 63, jd = beta >> 6, k = min(lane, 11);
    t.clo = k < 4 ? (unsigned)WS_CV + (sig * 4 + k) * 128 : (unsigned)WS_CY + (((sig >> 1) * 8 + 2 * jd) * 4 + (k - 4)) * 128;
    t.clr = __builtin_amdgcn_raw_buffer_load_b32(mkrsrc(p.ws, (unsigned)WS_END), t.clo, 0, SC1);
}
DI void addr_late(int beta, int w8, int lane, lptr L, Late& t) {
    const int sig = beta & 63, jd = beta >> 6, q4 = sig >> 4, b = sig & 15, n = lane & 15, g = lane >> 4;
    const int i2 = w8 >> 1, t2 = w8 & 1, j = w8 >> 1, ip = w8 & 1, s1 = 4 * t2 + g;
    t.vsr = laddr(L + L_VS) + (4 * (g & 1) * 32 + j * 8 + (g >> 1) * 2) * 128 + n * 16 + 8 * ip;
    t.eiw = laddr(L + L_EI) + (2 * ip * 2 * 16 + 4 * g) * 272 + (16 * j + n) * 4;
    t.eir = laddr(L + L_EI) + (w8 * 16 + n) * 272 + 16 * g;
    t.yr0 = laddr(L + L_YS) + n * 272 + i2 * 16 + s1 * 2;
    t.yr1 = laddr(L + L_YS) + min(16 + n, 27) * 272 + i2 * 16 + s1 * 2;
    t.xr0 = laddr(L + L_XS) + (i2 * 8 + s1) * XS_PITCH + (112 * jd + 4 * n) * 2;
    t.xr1 = laddr(L + L_XS) + (i2 * 8 + s1) * XS_PITCH + (112 * jd + 4 * min(16 + n, 27)) * 2;
    t.gta = laddr(L + L_GATE) + (i2 * 8 + s1) * 8;
    t.oro = ((256 * s1 + 16 * (4 * q4 + i2) + b) * D_ + 112 * jd + 4 * n) * 2;
    pin(t.vsr); pin(t.eiw); pin(t.eir); pin(t.yr0); pin(t.yr1); pin(t.xr0); pin(t.xr1); pin(t.gta); pin(t.oro);
}
DI void teamA(const Params& p, const int beta, const int w, const int lane, lptr L, Late& late, u4 (&yp)[2] STAMP_ARG) {
    TeamBar bar{laddr(L + L_CTR + 4 * C_BARA), 0u};
    const int n = lane & 15, g = lane >> 4, tid = w * 64 + lane;
    const f4 z4 = {0, 0, 0, 0};
    const rsrc_t rws = mkrsrc(p.ws, (unsigned)WS_END), rtab = mkrsrc(g_tab, sizeof(g_tab)), rtw = mkrsrc(g_tw, sizeof(g_tw));
    const int sig = beta & 63, j = beta >> 6, q4 = sig >> 4, b = sig & 15;
    const int s2 = 16 * (4 * q4 + w) + b;
    lptr xs = L + L_XS + w * 8 * XS_PITCH;
    unsigned lane16 = lane * 16;
    pin(lane16);
    const u64 ep0 = load_epoch(p.ws);
    {
        const char* src = w == 0 ? (const char*)(g_tab + TAB_G) : w == 1 ? (const char*)(g_tab + TAB_H) : w == 2 ? (const char*)(g_tw + TW_3) : (const char*)(g_tw + TW_4 + (beta & 7) * 512);
        glds16s(src, lane16, laddr(L + L_TR + w * 2048)); glds16s(src + 1024, lane16, laddr(L + L_TR + w * 2048 + 1024));
        if (lane < 56) glds16s(p.gw, lane16, laddr(L + L_GW2));
    }
    {
        const unsigned o = (unsigned)WS_V + sig * 32768 + (w * 32 + j * 8) * 128 + lane16;
        st16(rws, o, u4{SENT, SENT, SENT, SENT}); st16(rws, o + 4 * 32 * 128, u4{SENT, SENT, SENT, SENT});
    }
    asm volatile("" ::: "memory");
    f2 wre01, wim01, wre23, wim23, tre[2], tim[2];
    {
        const float th = (float)(16 * j + n) * (1.0f / 448.0f);
        wre01 = f2{1.0f, cosr(th)}; wim01 = f2{0.0f, -sinr(th)};
        wre23 = f2{cosr(2.f * th), cosr(3.f * th)}; wim23 = f2{-sinr(2.f * th), -sinr(3.f * th)};
#pragma unroll
        for (int h = 0; h < 2; ++h) {
            const int kk = 4 * (g & 1) + 2 * h;
            const float r0 = (float)(kk * s2) * (1.0f / 2048.0f), r1 = (float)((kk + 1) * s2) * (1.0f / 2048.0f);
            tre[h] = f2{cosr(r0), cosr(r1)}; tim[h] = f2{-sinr(r0), -sinr(r1)};
            pin(tre[h]); pin(tim[h]);
        }
        pin(wre01); pin(wim01); pin(wre23); pin(wim23);
    }
    const int k1 = beta & 7, cg = beta >> 3, c2 = k1 * 32 + cg;
    const int j2 = cg >> 3, cls2 = (cg >> 1) & 3, half = cg & 1;
    const int f0 = 16 * j2 + 8 * half;
    const int wlo = (cls2 & 1 ? 112 : 0) + (cls2 < 2 ? f0 : (cls2 == 2 ? 217 : -7) - f0);
    unsigned we = ((unsigned)(k1 + 8 * tid) * 225 + wlo) * 2;
    pin(we);
    unsigned zoff = 16 * j + ((tid >> 1) & 15) <= 56 ? (unsigned)WS_Z + ((tid >> 7) * 32 + j * 8 + ((tid >> 5) & 3) * 2 + ((tid >> 4) & 1)) * 16384 + sig * 256 + (tid & 15) * 16 : OOB;
    unsigned imga = laddr(L + L_ASM) + ((4 * (g & 1) * 4 + (g >> 1)) * 64 + n * 4 + w) * 4;
    unsigned rpa = laddr(xs) + (lane & 3) * XS_PITCH + 8 * (8 * g + (n >> 2)), rpb = laddr(xs) + (lane & 3) * XS_PITCH + 8 * (96 + 4 * g + (n >> 2));
    unsigned asrd = laddr(L + L_ASM) + tid * 8;
    pin(zoff); pin(imga); pin(rpa); pin(rpb); pin(asrd);
    const bool zval = f0 + ((tid >> 1) & 7) <= 56;
    unsigned zin = zval ? (unsigned)WS_Z + c2 * 16384 + tid * 16 : OOB;
    unsigned zsw = laddr(L + L_ASM) + ((tid >> 4) * 8 + (((tid >> 1) & 7) ^ ((tid >> 4) & 7))) * 16 + (tid & 1) * 8;
    unsigned zsr = laddr(L + L_ASM) + lane * 128;
    unsigned tra = laddr(L + L_TR + w * 2560) + n * 40 + 8 * g, trb = laddr(L + L_TR + w * 2560) + (4 * g + (n >> 2)) * 40 + 8 * (n & 3);
    unsigned asa = laddr(L + L_VA) + (lane * 8 + ((2 * w) ^ (lane & 7))) * 16, asb = laddr(L + L_VA) + (lane * 8 + ((2 * w + 1) ^ (lane & 7))) * 16;
    unsigned asr = laddr(L + L_VA) + ((tid >> 3) * 8 + ((tid & 7) ^ ((tid >> 3) & 7))) * 16;
    unsigned voff = f0 + (tid & 7) <= 56 ? (unsigned)WS_V + (tid >> 3) * 32768 + c2 * 128 + (tid & 7) * 16 : OOB;
    unsigned wia = laddr(L + L_WI) + (n + 64 * g) * 32;
    pin(zin); pin(zsw); pin(zsr); pin(tra); pin(trb); pin(asa); pin(asb); pin(asr); pin(voff); pin(wia);
    addr_late(beta, w, lane, L, late);
    STAMP(1);
    asm volatile("s_waitcnt vmcnt(6)" ::: "memory");
    bar();
    u4 tb[2][3]; u2 tl[2];
#pragma unroll
    for (int c = 0; c < 2; ++c) {
#pragma unroll
        for (int tau = 0; tau < 3; ++tau) tb[c][tau] = *(LAS u4*)(L + L_T1 + c * 3584 + tau * 1024 + lane16);
        tl[c] = *(LAS u2*)(L + L_T1 + c * 3584 + 3072 + lane * 8);
    }
    const u4 f8re = *(LAS u4*)(L + L_F8A + lane16), f8im = *(LAS u4*)(L + L_F8A + 1024 + lane16);
    const u64 ep = next_epoch(ep0);
    const unsigned tag = tag_of(ep);
    STAMP(2);
    u4 wre, wim, gre, gim, hre, him; f4 t3a, t3b, t4a, t4b;
    {
        f4 acc[2][2];
        u4 xa[2][3]; u2 xb[2];
#pragma unroll
        for (int t = 0; t < 2; ++t) {
            lptr rp = (lptr)(size_t)rpa + 4 * t * XS_PITCH;
#pragma unroll
            for (int tau = 0; tau < 3; ++tau) xa[t][tau] = cat(trread(rp + 256 * tau), trread(rp + 256 * tau + 32));
            xb[t] = trread((lptr)(size_t)rpb + 4 * t * XS_PITCH);
        }
        wre = __builtin_amdgcn_raw_buffer_load_b128(mkrsrc(p.wr, 2048 * 225 * 2), we, 0, 0);
        wim = __builtin_amdgcn_raw_buffer_load_b128(mkrsrc(p.wi, 2048 * 225 * 2), we, 0, 0);
        __builtin_amdgcn_sched_barrier(0);
#pragma unroll
        for (int t = 0; t < 2; ++t) {
            acc[t][0] = z4; acc[t][1] = z4;
#pragma unroll
            for (int tau = 0; tau < 3; ++tau) {
                acc[t][0] = mfma32(xa[t][tau], tb[0][tau], acc[t][0]);
                acc[t][1] = mfma32(xa[t][tau], tb[1][tau], acc[t][1]);
            }
            acc[t][0] = mfma16(xb[t], tl[0], acc[t][0]);
            acc[t][1] = mfma16(xb[t], tl[1], acc[t][1]);
        }
        u4 fr[2];
#pragma unroll
        for (int t = 0; t < 2; ++t) {
            const f2 er01 = lo2(acc[t][0]), er23 = hi2(acc[t][0]), ei01 = lo2(acc[t][1]), ei23 = hi2(acc[t][1]);
            const f2 tr01 = er01 * wre01 - ei01 * wim01, ti01 = er01 * wim01 + ei01 * wre01;
            const f2 tr23 = er23 * wre23 - ei23 * wim23, ti23 = er23 * wim23 + ei23 * wre23;
            const f2 sr = tr01 + tr23, si = ti01 + ti23, dr = tr01 - tr23, di = ti01 - ti23;
            fr[0][t] = pk(sadd(sr.x, sr.y), sadd(si.x, si.y));
            fr[0][2 + t] = pk(sadd(dr.x, di.y), ssub(di.x, dr.y));
            fr[1][t] = pk(ssub(sr.x, sr.y), ssub(si.y, si.x));
            fr[1][2 + t] = pk(ssub(dr.x, di.y), nadd(di.x, dr.y));
        }
#pragma unroll
        for (int pr = 0; pr < 2; ++pr) {
            const f4 zr = mfma32(f8re, fr[pr], z4), zi = mfma32(f8im, fr[pr], z4);
#pragma unroll
            for (int h = 0; h < 2; ++h) {
                const f2 a = h ? hi2(zr) : lo2(zr), c = h ? hi2(zi) : lo2(zi);
                const f2 re = a * tre[h] - c * tim[h], im = a * tim[h] + c * tre[h];
                LAS unsigned* img = (LAS unsigned*)(size_t)(imga + (2 * h * 4 + 2 * pr) * 256);
                img[0] = pk(re.x, im.x);
                img[256] = pk(re.y, im.y);
            }
        }
        STAMP(3);
        asm volatile("s_waitcnt vmcnt(2)" ::: "memory");
        bar();
        if (tid == 0) st16(rws, (unsigned)WS_CV + (sig * 4 + j) * 128, u4{tag, tag, tag, tag});
        u2 zv[4];
#pragma unroll
        for (int u = 0; u < 4; ++u) zv[u] = *(LAS u2*)(size_t)(asrd + u * 2048);
#pragma unroll
        for (int u = 0; u < 4; ++u) st16(rws, zoff + u * 2 * 32 * 16384, tagged(zv[u], tag));
        __builtin_amdgcn_s_setprio(A_MID);
        STAMP(4);
    }
    gre = *(LAS u4*)(L + L_TR + lane16); gim = *(LAS u4*)(L + L_TR + 1024 + lane16);
    hre = *(LAS u4*)(L + L_TR + 2048 + lane16); him = *(LAS u4*)(L + L_TR + 3072 + lane16);
    t3a = *(LAS f4*)(L + L_TR + 4096 + lane * 32); t3b = *(LAS f4*)(L + L_TR + 4096 + lane * 32 + 16);
    t4a = *(LAS f4*)(L + L_TR + 6144 + lane * 32); t4b = *(LAS f4*)(L + L_TR + 6144 + lane * 32 + 16);
    {
        const unsigned LO = 0x05040100u, HI = 0x07060302u;
        const u4 r = wre, im = wim;
        *(LAS u4*)(L + L_WI + tid * 32) = u4{perm(im.x, r.x, LO), perm(im.x, r.x, HI), perm(im.y, r.y, LO), perm(im.y, r.y, HI)};
        *(LAS u4*)(L + L_WI + tid * 32 + 16) = u4{perm(im.z, r.z, LO), perm(im.z, r.z, HI), perm(im.w, r.w, LO), perm(im.w, r.w, HI)};
    }
    bar();
    f2 wr_[2][2], wi_[2][2];
#pragma unroll
    for (int u = 0; u < 2; ++u) {
        const int col = cls2 < 2 ? 2 * w + u : 7 - (2 * w + u);
#pragma unroll
        for (int h = 0; h < 2; ++h) {
            const unsigned d0 = *(LAS unsigned*)(size_t)(wia + col * 4 + h * 1024), d1 = *(LAS unsigned*)(size_t)(wia + col * 4 + h * 1024 + 512);
            wr_[u][h] = f2{bflo(d0), bflo(d1)}; wi_[u][h] = f2{bfhi(d0), bfhi(d1)};
            pin(wr_[u][h]); pin(wi_[u][h]);
        }
    }
    pin(gre); pin(gim); pin(hre); pin(him); pin(t3a); pin(t3b); pin(t4a); pin(t4b);
    STAMP(5);
    STAMP(6);
    {
        __builtin_amdgcn_s_setprio(PRIOA);
        u4 a[2], zp[4];
        for (int i = 0; i < Z_SLEEP; ++i) __builtin_amdgcn_s_sleep(8);
#pragma unroll
        for (int u = 0; u < 4; ++u) zp[u] = ld16(rws, zin + u * 4096);
        for (unsigned spin = 0; spin < GSPIN_CAP; ++spin) {
            unsigned bad = 0;
#pragma unroll
            for (int u = 0; u < 4; ++u) bad |= min(untagged(zp[u], tag), 1u) << u;
            bad = zval ? bad : 0u;
            if (!__any(bad != 0)) break;
            __builtin_amdgcn_s_sleep(ZRETRY_SLEEP);
            asm volatile("" ::: "memory");
#pragma unroll
            for (int u = 0; u < 4; ++u) {
                const bool again = bad >> u & 1;
                const u4 nv = ld16(rws, again ? zin + u * 4096 : OOB);
                zp[u] = again ? nv : zp[u];
            }
        }
        __builtin_amdgcn_fence(__ATOMIC_ACQUIRE, "wavefront");
        __builtin_amdgcn_s_setprio(PRIOA);
        STAMP(7);
#pragma unroll
        for (int u = 0; u < 4; ++u) *(LAS u2*)(size_t)(zsw + u * 2048) = u2{zp[u].x, zp[u].z};
        bar();
        a[0] = *(LAS u4*)(size_t)(zsr + (((2 * w) ^ (lane & 7)) << 4)); a[1] = *(LAS u4*)(size_t)(zsr + (((2 * w + 1) ^ (lane & 7)) << 4));
        f4 dr[2], di[2];
        u4 fr[2];
        const f2 twr[2] = {lo2(t3a), lo2(t3b)}, twi[2] = {hi2(t3a), hi2(t3b)}, ftr[2] = {lo2(t4a), lo2(t4b)}, fti[2] = {hi2(t4a), hi2(t4b)};
#pragma unroll
        for (int u = 0; u < 2; ++u) { dr[u] = mfma32(a[u], gre, z4); di[u] = mfma32(a[u], gim, z4); }
#pragma unroll
        for (int u = 0; u < 2; ++u) {
#pragma unroll
            for (int h = 0; h < 2; ++h) {
                const f2 x = h ? hi2(dr[u]) : lo2(dr[u]), y = h ? hi2(di[u]) : lo2(di[u]);
                const f2 re = x * twr[h] - y * twi[h], im = x * twi[h] + y * twr[h];
                fr[u][2 * h] = pk(re.x, im.x); fr[u][2 * h + 1] = pk(re.y, im.y);
            }
            dr[u] = mfma32(gre, fr[u], z4); di[u] = mfma32(gim, fr[u], z4);
        }
#pragma unroll
        for (int u = 0; u < 2; ++u) {
#pragma unroll
            for (int h = 0; h < 2; ++h) {
                const f2 x = h ? hi2(dr[u]) : lo2(dr[u]), y = h ? hi2(di[u]) : lo2(di[u]);
                const f2 re = x * wr_[u][h] - y * wi_[u][h], im = __builtin_elementwise_fma(-x, wi_[u][h], -(y * wr_[u][h]));
                fr[u][2 * h] = pk(re.x, im.x); fr[u][2 * h + 1] = pk(re.y, im.y);
            }
            dr[u] = mfma32(gre, fr[u], z4); di[u] = mfma32(gim, fr[u], z4);
        }
#pragma unroll
        for (int u = 0; u < 2; ++u) {
            u2 pre, pim;
#pragma unroll
            for (int h = 0; h < 2; ++h) {
                const f2 x = h ? hi2(dr[u]) : lo2(dr[u]), y = h ? hi2(di[u]) : lo2(di[u]);
                const f2 re = x * twr[h] - y * twi[h], im = x * twi[h] + y * twr[h];
                pre[h] = pk(re.x, re.y); pim[h] = pk(im.x, im.y);
            }
            *(LAS u2*)(size_t)(tra + u * 1280) = pre;
            *(volatile LAS u2*)(size_t)(tra + u * 1280 + 640) = pim;
        }
#pragma unroll
        for (int u = 0; u < 2; ++u) {
            const u4 bb = cat(trread((lptr)(size_t)(trb + u * 1280)), trread((lptr)(size_t)(trb + u * 1280 + 640)));
            dr[u] = mfma32(hre, bb, z4); di[u] = mfma32(him, bb, z4);
        }
#pragma unroll
        for (int u = 0; u < 2; ++u) {
            u4 o;
#pragma unroll
            for (int h = 0; h < 2; ++h) {
                const f2 x = h ? hi2(dr[u]) : lo2(dr[u]), y = h ? hi2(di[u]) : lo2(di[u]);
                const f2 re = x * ftr[h] - y * fti[h], im = __builtin_elementwise_fma(-x, fti[h], -(y * ftr[h]));
                o[2 * h] = san(pk(re.x, im.x)); o[2 * h + 1] = san(pk(re.y, im.y));
            }
            *(LAS u4*)(size_t)(u ? asb : asa) = o;
        }
        STAMP(8);
        {
            WAIT_LGKM0();
            const char* src = (const char*)(g_tab + TAB_F8I) + w * 2048;
            glds16s(src, lane16, laddr(L + L_F8 + w * 2560)); glds16s(src + 1024, lane16, laddr(L + L_F8 + w * 2560 + 1024));
        }
        bar();
        const u4 v0 = *(LAS u4*)(size_t)asr, v1 = *(LAS u4*)(size_t)(asr + 4096);
        st16(rws, voff, v0);
        st16(rws, voff + 32 * 32768, v1);
        load_late(p, beta, w, lane, late);
        load_notes(p, beta, lane, late);
        {
            const char* src = (const char*)(g_tab + TAB_T3 + (beta >> 6) * 2048) + w * 2048;
            glds16s(src, lane16, laddr(L + L_T3 + w * 2048)); glds16s(src + 1024, lane16, laddr(L + L_T3 + w * 2048 + 1024));
        }
        STAMP(9);
    }
    {
        f4 acc = z4;
#pragma unroll
        for (int tau = 0; tau < 14; ++tau) acc = mfma32(*(LAS u4*)(xs + (n & 7) * XS_PITCH + 64 * tau + 16 * g), *(LAS u4*)(L + L_GW2 + 64 * tau + 16 * g), acc);
        if (n == 0 && g < 2) {
#pragma unroll
            for (int r = 0; r < 4; ++r) {
                const float z = bflo(pk(acc[r], 0.f) & 0xffffu);
                const float gv = bflo(pk(1.0f / (1.0f + __expf(-z)), 0.f) & 0xffffu);
                const float om = bflo(pk(1.0f - gv, 0.f) & 0xffffu);
                *(LAS f2*)(L + L_GATE + (w * 8 + 4 * g + r) * 8) = f2{gv, om};
            }
        }
    }
    late.ep = ep;
    const bool vok = 16 * w + 8 * ((tid >> 3) & 1) + (tid & 7) <= 56;
    const unsigned vin = (unsigned)WS_V + sig * 32768 + tid * 16, vin1 = vok ? vin : OOB, vsw = laddr(L + L_VS) + tid * 16;
    STAMP(10);
    WAIT_VM0();
    {
        if (!__all(late.clr == tag)) poll_tags(rws, late.clo, tag);
        u4 vp[8];
        for (int i = 0; i < V_SLEEP; ++i) __builtin_amdgcn_s_sleep(8);
#pragma unroll
        for (int u = 0; u < 8; ++u) vp[u] = ld16(rws, vin1 + u * 4096);
        for (unsigned spin = 0; spin < GSPIN_CAP; ++spin) {
            unsigned bad = 0;
#pragma unroll
            for (int u = 0; u < 8; ++u) bad |= unfilled(vp[u]) << u;
            if (!__any(bad != 0)) break;
            __builtin_amdgcn_s_sleep(RETRY_SLEEP);
            asm volatile("" ::: "memory");
#pragma unroll
            for (int u = 0; u < 8; ++u) {
                const bool again = bad >> u & 1;
                const u4 nv = ld16(rws, again ? vin + u * 4096 : OOB);
                vp[u] = again ? nv : vp[u];
            }
        }
        __builtin_amdgcn_fence(__ATOMIC_ACQUIRE, "wavefront");
        const unsigned yin = (unsigned)WS_Y + beta * 7168 + tid * 16;
        yp[0] = ld16(rws, yin); yp[1] = ld16(rws, tid < 192 ? yin + 4096 : OOB);
        const unsigned ca = laddr(L + L_CTR + 4 * C_EXP);
        for (unsigned spin = 0; spin < SPIN_CAP; ++spin) {
            unsigned v;
            asm volatile("ds_read_b32 %0, %1\n\ts_waitcnt lgkmcnt(0)" : "=v"(v) : "v"(ca) : "memory");
            if (__builtin_amdgcn_readfirstlane(v) != 0) break;
            __builtin_amdgcn_s_sleep(1);
        }
#pragma unroll
        for (int u = 0; u < 8; ++u) *(LAS u4*)(size_t)(vsw + u * 4096) = vp[u];
    }
}

DI void teamB(const Params& p, const int beta, const int w, const int lane, lptr L, Late& late STAMP_ARG) {
    TeamBar bar{laddr(L + L_CTR + 4 * C_BARB), 0u};
    const int tid = w * 64 + lane, n = lane & 15, g = lane >> 4;
    const unsigned lane16 = lane * 16;
    const rsrc_t rws = mkrsrc(p.ws, (unsigned)WS_END);
    const bool has = beta < 224;
    const int cc = w >> 1, ch = has ? 2 * beta + cc : 0;
    const int s0 = 64 * (beta >> 3), c0 = 56 * (beta & 7);
    const int idx = ch * 256 + 128 * (w & 1) + 2 * lane;
    const unsigned are = *(const unsigned*)(p.are + idx), aim = *(const unsigned*)(p.aim + idx), bre = *(const unsigned*)(p.bre + idx);
    const unsigned bim = *(const unsigned*)(p.bim + idx), cre = *(const unsigned*)(p.cre + idx), cim = *(const unsigned*)(p.cim + idx);
    const u16 ldt = p.ldt[ch];
    const u64 ep0 = load_epoch(p.ws);
    asm volatile("" ::: "memory");
    {
        const int cl0 = 56 * (beta & 1) + 16 * w;
        const unsigned o = (unsigned)WS_Y + (((beta & 7) >> 1) * 64 + 2 * (beta >> 3)) * 7168 + cl0 * 64 + lane16;
        const unsigned oo = (w < 3 || lane < 32) ? o : OOB;
        st16(rws, oo, u4{SENT, SENT, SENT, SENT}); st16(rws, oo + 7168, u4{SENT, SENT, SENT, SENT});
    }
    asm volatile("" ::: "memory");
    const unsigned tt = laddr(L + L_TT) + w * 4352;
    unsigned t1r = tt + (8 * g + (n >> 2)) * 32 + 8 * (lane & 3);
    unsigned t2w = tt + 2048 + n * 144 + 16 * g;
    unsigned t2r = tt + 2048 + (lane >> 4) * 144 + (lane & 15) * 8;
    unsigned xo0 = (unsigned)WS_XT + (c0 + 16 * w + (lane >> 4)) * 8192 + (beta >> 3) * 256 + (lane & 15) * 16;
    if (tid < 4) *(LAS u4*)(L + L_KF + tid * KF_PITCH) = u4{0, 0, 0, 0};
    pin(t1r); pin(t2w); pin(t2r); pin(xo0);
    const int m = 255 - tid, h = w & 1, jd = ch / 112, cl = ch - 112 * jd;
    const unsigned imgb = laddr(L + L_KG) + w * 18432, kfb = laddr(L + L_KF) + 16;
    unsigned rea = imgb + lane * 144;
    unsigned r0a = imgb + ((g >> 1) * 64 + 2 * (4 * (g & 1) + (n >> 2))) * 144 + 8 * (lane & 3);
    unsigned ka0 = kfb + w * KF_PITCH + 4 * (128 * (g >> 1) + 4 * (g & 1) + 8 * n);
    unsigned ka1 = kfb + w * KF_PITCH + 4 * sel(g < 2, 256 + 64 * g + 4 * n, 384 + (g - 2) + 2 * n), ks1 = sel(g < 2, 4, 128);
    unsigned ka2 = kfb + w * KF_PITCH + 4 * sel(g < 2, 512 + 64 * g + n, sel(lane == 32, 640, 648)), ks2 = sel(g < 2, 64, sel(lane == 32, 8, 0));
    unsigned kea = kfb + 4 * (w < 2 ? 512 + (m - 128) : w == 2 ? 384 + 2 * (m - 64) : (int)sel(m >= 32, 256 + 4 * (m - 32), 8 * m)), tca = laddr(L + L_TC) + tid * 16;
    unsigned xin = (unsigned)WS_XT + (2 * beta) * 8192 + tid * 16, uiw = laddr(L + L_UI) + 512 + tid * 8;
    unsigned tb = laddr(L + L_TC) + (cc * 2 + 1 - (n & 1)) * TC_PITCH + 2 * (2046 - 16 * (g >> 1) + 8 * (g & 1) - 2 * (n >> 1)) - 32 * (8 * h + 6 + 112);
    unsigned ub = laddr(L + L_UI) + cc * 4608 + 512 + 2 * (16 * (n - (g >> 1)) + 8 * (g & 1)) - 32 * (8 * h + 6);
    unsigned cra = laddr(L + L_CR) + cc * 8192 + lane * 8;
    unsigned yo = (unsigned)WS_Y + (jd * 64 + (n >> 2) * 16 + 4 * g) * 7168 + cl * 64 + (n & 3) * 16;
    pin(rea); pin(r0a); pin(ka0); pin(ka1); pin(ks1); pin(ka2); pin(ks2); pin(kea); pin(tca); pin(xin); pin(uiw); pin(tb); pin(ub); pin(cra); pin(yo);
    addr_late(beta, w + 4, lane, L, late);
    asm volatile("s_waitcnt vmcnt(10)" ::: "memory");
    u2 xv[4];
    STAMP(1);
    u4 tv[2];
    auto t_step1 = [&]() {
#pragma unroll
        for (int it = 0; it < 2; ++it) tv[it] = cat(trread((lptr)(size_t)(t1r + 1024 * it)), trread((lptr)(size_t)(t1r + 1024 * it + 128)));
    };
    auto t_step2 = [&]() {
        asm volatile("" ::: "memory");
#pragma unroll
        for (int it = 0; it < 2; ++it) *(LAS u4*)(size_t)(t2w + 64 * it) = tv[it];
        asm volatile("" ::: "memory");
    };
    auto t_step3 = [&]() {
#pragma unroll
        for (int u = 0; u < 4; ++u) xv[u] = *(LAS u2*)(size_t)(t2r + u * 4 * 144);
        asm volatile("" ::: "memory");
    };
    t_step1();
    const u64 ep = next_epoch(ep0);
    const unsigned tag = tag_of(ep);
    auto publish_xt = [&]() {
        WAIT_VM0();
        if (lane == 0) st16(rws, (unsigned)WS_CY + (beta * 4 + w) * 128, u4{tag, tag, tag, tag});
#pragma unroll
        for (int u = 0; u < 4; ++u) st16(rws, (w < 3 || u < 2) ? xo0 + u * 4 * 8192 : OOB, tagged(xv[u], tag));
    };
    STAMP(2);
    late.ep = ep;
    if (!has) {
        t_step2(); t_step3();
        if (w == 0) lds_inc(laddr(L + L_CTR + 4 * C_EXP));
        for (int i = 0; i < 8; ++i) __builtin_amdgcn_s_sleep(2);
        publish_xt();
        load_late(p, beta, w + 4, lane, late);
        return;
    }
    {
        const float dt0 = bflo(pk(__expf(bf2f(ldt)), 0.f) & 0xffffu);
        float dt1; asm("v_mov_b32 %0, %1" : "=v"(dt1) : "v"(dt0));
        const f2 dt = {dt0, dt1};
        const f2 lr2 = f2{bflo(are), bfhi(are)} * dt * 1.44269504088896341f, lirev = f2{bflo(aim), bfhi(aim)} * dt * 0.15915494309189535f;
        const C2 dB = {f2{bflo(bre), bfhi(bre)} * dt, f2{bflo(bim), bfhi(bim)} * dt};
        const C2 c = cmul(C2{f2{bflo(cre), bfhi(cre)}, f2{bflo(cim), bfhi(cim)}}, dB);
        const C2 e1 = cexpl(lr2, lirev, 1.f), e128 = cexpl(lr2, lirev, 128.f);
        const C2 e8c = cexpl(lr2, -lirev, 8.f);
        const C2 e2 = cmul(e1, e1), e4 = cmul(e2, e2);
        t_step2();
        lptr re = (lptr)(size_t)rea, im = (lptr)(size_t)(rea + 64 * 144);
        u4 kre[8], kim[8];
        C2 R[8], Q[8], B[17], G2[8], G3[8];
        R[0] = c; Q[0] = C2{f2{1.f, 1.f}, f2{0.f, 0.f}}; Q[1] = e8c; B[0] = c;
#pragma unroll
        for (int i = 1; i < 7; ++i) {
            cmul3(R[i], R[i - 1], e1, B[i], B[i - 1], e128, Q[i + 1], Q[i], e8c);
            if (i == 2) t_step3();
        }
        C2 Q8;
        const C2 Q7 = Q[7];
        put8(re + 96, im + 96, Q, kre[6], kim[6]);
        cmul3(R[7], R[6], e1, B[7], B[6], e128, Q8, Q7, e8c);
        put8(re, im, R, kre[0], kim[0]);
        publish_xt();
        STAMP(3);
        G2[0] = B[2]; G2[4] = B[3];
        cmul3(G2[1], G2[0], e2, B[8], B[7], e128, G2[5], G2[4], e2);
        cmul3(G2[2], G2[1], e2, B[9], B[8], e128, G2[6], G2[5], e2);
        cmul3(G2[3], G2[2], e2, B[10], B[9], e128, G2[7], G2[6], e2);
        put8(re + 32, im + 32, G2, kre[2], kim[2]);
        cmul3(G3[4], B[4], e4, B[11], B[10], e128, G3[5], B[5], e4);
        cmul3(G3[6], B[6], e4, B[12], B[11], e128, G3[7], B[7], e4);
        G3[0] = B[4]; G3[1] = B[5]; G3[2] = B[6]; G3[3] = B[7];
        put8(re + 48, im + 48, G3, kre[3], kim[3]);
        R[0] = B[1]; Q[0] = Q8;
        cmul3(R[1], R[0], e1, B[13], B[12], e128, Q[1], Q[0], e8c);
        cmul3(R[2], R[1], e1, B[14], B[13], e128, Q[2], Q[1], e8c);
        cmul3(R[3], R[2], e1, B[15], B[14], e128, Q[3], Q[2], e8c);
        put8(re + 64, im + 64, B + 8, kre[4], kim[4]);
        cmul3(R[4], R[3], e1, B[16], B[15], e128, Q[4], Q[3], e8c);
        *(LAS unsigned*)(re + 80) = pk(B[16].re.x, 0.f); *(LAS unsigned*)(im + 80) = pk(B[16].im.x, 0.f);
        const unsigned k5re = pk(B[16].re.y, 0.f), k5im = pk(B[16].im.y, 0.f);
#pragma unroll
        for (int i = 5; i < 8; ++i) cmul2(R[i], R[i - 1], e1, Q[i], Q[i - 1], e8c);
        put8(re + 16, im + 16, R, kre[1], kim[1]);
        put8(re + 112, im + 112, Q, kre[7], kim[7]);
        STAMPX(10);
        f4 acc[3] = {f4{0, 0, 0, 0}, f4{0, 0, 0, 0}, f4{0, 0, 0, 0}};
        lptr r0 = (lptr)(size_t)r0a;
        u4 fa[3][4];
        auto ldk = [&](int tau, int s) {
            lptr r1 = r0 + 16 * 144 * tau;
            fa[s][3] = cat(trread(r1 + 96), trread(r1 + 96 + 144));
#pragma unroll
            for (int mt = 0; mt < 3; ++mt) fa[s][mt] = cat(trread(r1 + 32 * mt), trread(r1 + 32 * mt + 144));
        };
        ldk(0, 0); ldk(1, 1);
#pragma unroll
        for (int st = 0; st < 8; ++st) {
            if (st + 2 < 8) ldk((st + 2) & 3, (st + 2) % 3);
            if (st == 1) {
#pragma unroll
                for (int i = 0; i < 8; ++i) if (i != 5) { *(LAS u4*)(re + 16 * i) = kre[i]; *(LAS u4*)(im + 16 * i) = kim[i]; }
                *(LAS unsigned*)(re + 80) = k5re; *(LAS unsigned*)(im + 80) = k5im;
            }
            if (st == 2) load_late(p, beta, w + 4, lane, late);
            __builtin_amdgcn_sched_barrier(0);
#pragma unroll
            for (int mt = 0; mt < 3; ++mt) acc[mt] = mfma32(fa[st % 3][mt], fa[st % 3][3], acc[mt]);
            __builtin_amdgcn_sched_barrier(0);
        }
        STAMP(4);
        u4 xp[4];
#pragma unroll
        for (int u = 0; u < 4; ++u) xp[u] = ld16(rws, xin + (u >> 1) * 8192 + (u & 1) * 4096);
        *(LAS f4*)(size_t)ka0 = acc[0];
#pragma unroll
        for (int r = 0; r < 4; ++r) { *(LAS float*)(size_t)(ka1 + r * ks1) = acc[1][r]; *(LAS float*)(size_t)(ka2 + r * ks2) = acc[2][r]; }
        bar();
        auto put2 = [&](int c2, u4 o0, u4 o1) {
            const unsigned tc = tca + c2 * 2 * TC_PITCH;
            *(LAS u4*)(size_t)tc = o0; *(LAS u4*)(size_t)(tc + TC_PITCH) = o1;
        };
        if (w < 2) {
            float e0[2], e8[2];
#pragma unroll
            for (int c2 = 0; c2 < 2; ++c2) {
                LAS float* k = (LAS float*)(size_t)(kea + 2 * c2 * KF_PITCH); LAS float* k2 = (LAS float*)(size_t)(kea + (2 * c2 + 1) * KF_PITCH);
                e0[c2] = k[0] + k2[0]; e8[c2] = k[1] + k2[1];
            }
#pragma unroll
            for (int c2 = 0; c2 < 2; ++c2) {
                const float mid = (m & 1) ? e8[c2] : e0[c2];
                const unsigned A = pkx(e8[c2], e8[c2]), B = pkx(e0[c2], e0[c2]), M1 = pkx(e8[c2], mid), M2 = pkx(mid, e0[c2]);
                put2(c2, u4{A, M1, B, B}, u4{A, M2, B, B});
            }
        } else if (w == 2) {
            float e0[2], e4v[2], e8[2];
#pragma unroll
            for (int c2 = 0; c2 < 2; ++c2) {
                LAS float* k = (LAS float*)(size_t)(kea + 2 * c2 * KF_PITCH); LAS float* k2 = (LAS float*)(size_t)(kea + (2 * c2 + 1) * KF_PITCH);
                e0[c2] = k[0] + k2[0]; e4v[c2] = k[1] + k2[1]; e8[c2] = k[2] + k2[2];
            }
#pragma unroll
            for (int c2 = 0; c2 < 2; ++c2) {
                const unsigned AA = pkx(e8[c2], e8[c2]), FF = pkx(e4v[c2], e4v[c2]), DD = pkx(e0[c2], e0[c2]), FD = pkx(e4v[c2], e0[c2]), NF = pkx(e8[c2], e4v[c2]);
                put2(c2, u4{AA, FF, FD, DD}, u4{NF, FF, DD, DD});
            }
        } else {
            f4 wa[2], wb[2], wc[2];
#pragma unroll
            for (int c2 = 0; c2 < 2; ++c2) {
                LAS float* k = (LAS float*)(size_t)(kea + 2 * c2 * KF_PITCH); LAS float* k2 = (LAS float*)(size_t)(kea + (2 * c2 + 1) * KF_PITCH);
                wa[c2] = *(LAS f4*)(k - 4) + *(LAS f4*)(k2 - 4); wb[c2] = *(LAS f4*)k + *(LAS f4*)k2; wc[c2] = *(LAS f4*)(k + 4) + *(LAS f4*)(k2 + 4);
            }
            const bool sg = m >= 32, in = m > 32;
#pragma unroll
            for (int c2 = 0; c2 < 2; ++c2) {
                const f4 a = wa[c2], b4 = wb[c2], c4 = wc[c2];
                const float s0v = b4.x, s1 = sg ? b4.x : b4.y, s2v = sg ? b4.y : b4.z, s3 = sg ? b4.z : b4.w, s4 = sg ? b4.z : c4.x, s5 = sg ? b4.z : c4.y, s6 = sg ? b4.w : c4.z, s7 = sg ? c4.x : c4.w;
                const float m1 = in ? b4.x : a.w;
                put2(c2, u4{pkx(s7, s6), pkx(s5, s4), pkx(s3, s2v), pkx(s1, s0v)}, u4{pkx(s6, s5), pkx(s4, s3), pkx(s2v, s1), pkx(s0v, m1)});
            }
        }
        if (tid < 16) *(LAS u4*)(L + L_TC + (tid >> 2) * TC_PITCH + 4096 + (tid & 3) * 16) = u4{0, 0, 0, 0};
        if (tid >= 64 && tid < 128) *(LAS u4*)(L + L_UI + ((tid >> 5) & 1) * 4608 + (tid & 31) * 16) = u4{0, 0, 0, 0};
        for (unsigned spin = 0; spin < GSPIN_CAP; ++spin) {
            unsigned bad = 0;
#pragma unroll
            for (int u = 0; u < 4; ++u) bad |= min(untagged(xp[u], tag), 1u) << u;
            if (!__any(bad != 0)) break;
            __builtin_amdgcn_s_sleep(ZRETRY_SLEEP);
            asm volatile("" ::: "memory");
#pragma unroll
            for (int u = 0; u < 4; ++u) {
                const bool again = bad >> u & 1;
                const u4 nv = ld16(rws, again ? xin + (u >> 1) * 8192 + (u & 1) * 4096 : OOB);
                xp[u] = again ? nv : xp[u];
            }
        }
        __builtin_amdgcn_fence(__ATOMIC_ACQUIRE, "wavefront");
#pragma unroll
        for (int u = 0; u < 4; ++u) *(LAS u2*)(size_t)(uiw + (u >> 1) * 4608 + (u & 1) * 2048) = u2{xp[u].x, xp[u].z};
    }
    STAMP(5);
    bar();
    if (w == 0) lds_inc(laddr(L + L_CTR + 4 * C_EXP));
    STAMP(7);
    {
        f4 acc[8];
#pragma unroll
        for (int t = 0; t < 8; ++t) acc[t] = f4{0, 0, 0, 0};
        u4 U[2][8], T[2][8];
        auto load1 = [&](int rr, int s, int t) {
            const unsigned ta = tb + 32 * (6 - 2 * rr + 16 * (7 - t));
            U[s][t] = *(LAS u4*)(size_t)(ub + 512 * t + 32 * (6 - 2 * rr));
            LAS unsigned* tp = (LAS unsigned*)(size_t)ta;
            T[s][t] = u4{tp[0], tp[1], tp[2], tp[3]};
        };
#pragma unroll
        for (int t = 0; t < 8; ++t) load1(0, 0, t);
#pragma unroll
        for (int rr = 0; rr < 4; ++rr) {
#pragma unroll
            for (int m = 0; m < 8; ++m) {
                __builtin_amdgcn_sched_barrier(0);
#pragma unroll
                for (int t = m; t < 8; ++t) acc[t] = mfma32(T[rr & 1][m], U[rr & 1][t - m], acc[t]);
                if (rr < 3) load1(rr + 1, (rr + 1) & 1, m);
            }
        }
        __builtin_amdgcn_sched_barrier(0);
        STAMP(8);
        auto finish = [&](auto H) {
            constexpr int hh = decltype(H)::value;
#pragma unroll
            for (int t = 0; t < 8; ++t) *(LAS f2*)(size_t)(cra + hh * 4096 + t * 512) = hh ? lo2(acc[t]) : hi2(acc[t]);
            bar();
            f2 y[8];
#pragma unroll
            for (int t = 0; t < 8; ++t) y[t] = (hh ? hi2(acc[t]) : lo2(acc[t])) + *(LAS f2*)(size_t)(cra + (1 - hh) * 4096 + t * 512);
#pragma unroll
            for (int rr = 0; rr < 2; ++rr)
                st16(rws, yo + (2 * hh + rr) * 7168, u4{san(pk(y[0][rr], y[1][rr])), san(pk(y[2][rr], y[3][rr])), san(pk(y[4][rr], y[5][rr])), san(pk(y[6][rr], y[7][rr]))});
        };
        if (h) finish(IC<1>{}); else finish(IC<0>{});
        STAMP(9);
    }
}

DI void phaseA3(const Params& p, const int beta, const int w8, const int lane, lptr L, Late& late, u4 (&yp)[2] STAMP_ARG) {
    const u64 ep = late.ep;
    const int n = lane & 15;
    const f4 z4 = {0, 0, 0, 0};
    const rsrc_t rws = mkrsrc(p.ws, (unsigned)WS_END);
    const unsigned eiw = late.eiw, eir = late.eir, xr0 = late.xr0, xr1 = late.xr1;
    const unsigned tag = tag_of(ep);
    WAIT_LGKM0();
    __builtin_amdgcn_s_barrier();
    u4 fi[2][2][2], t3[2][4];
#pragma unroll
    for (int i = 0; i < 8; ++i) {
        fi[i >> 2][(i >> 1) & 1][i & 1] = *(LAS u4*)(L + L_F8 + (i >> 1) * 2560 + (i & 1) * 1024 + lane * 16);
        t3[i >> 2][i & 3] = *(LAS u4*)(L + L_T3 + i * 1024 + lane * 16);
    }
    f4 w02 = late.w02, w13 = late.w13;
    f2 ds[2][2] = {{f2{bflo(late.dv0.x), bfhi(late.dv0.x)}, f2{bflo(late.dv0.y), bfhi(late.dv0.y)}}, {f2{bflo(late.dv1.x), bfhi(late.dv1.x)}, f2{bflo(late.dv1.y), bfhi(late.dv1.y)}}};
#pragma unroll
    for (int i = 0; i < 8; ++i) { pin(fi[i >> 2][(i >> 1) & 1][i & 1]); pin(t3[i >> 2][i & 3]); }
    pin(ds[0][0]); pin(ds[0][1]); pin(ds[1][0]); pin(ds[1][1]); pin(w02); pin(w13);
    STAMP(11);
    STAMP(12);
    {
        u2 v[2][4];
#pragma unroll
        for (int pr = 0; pr < 2; ++pr)
#pragma unroll
            for (int reg = 0; reg < 4; ++reg) v[pr][reg] = *(LAS u2*)(size_t)(late.vsr + reg * 4096 + pr * 512);
        const f2 wre02 = lo2(w02), wim02 = hi2(w02), wre13 = lo2(w13), wim13 = hi2(w13);
        f4 rr[4], ri[4];
#pragma unroll
        for (int ii = 0; ii < 2; ++ii) {
            const u4 fr0 = {v[0][0][ii], v[0][1][ii], v[0][2][ii], v[0][3][ii]};
            const u4 fr1 = {v[1][0][ii], v[1][1][ii], v[1][2][ii], v[1][3][ii]};
#pragma unroll
            for (int t = 0; t < 2; ++t) { rr[2 * ii + t] = mfma32(fi[t][0][0], fr0, z4); ri[2 * ii + t] = mfma32(fi[t][1][0], fr0, z4); }
#pragma unroll
            for (int t = 0; t < 2; ++t) { rr[2 * ii + t] = mfma32(fi[t][0][1], fr1, rr[2 * ii + t]); ri[2 * ii + t] = mfma32(fi[t][1][1], fr1, ri[2 * ii + t]); }
        }
        __builtin_amdgcn_sched_barrier(0);
        f2 ar[4], mr[4], ai[4], mi[4], t02r[4], t02i[4], t13r[4], t13i[4];
#pragma unroll
        for (int k = 0; k < 4; ++k) {
            ar[k] = lo2(rr[k]) + hi2(rr[k]); mr[k] = lo2(rr[k]) - hi2(rr[k]);
            ai[k] = lo2(ri[k]) - hi2(ri[k]); mi[k] = lo2(ri[k]) + hi2(ri[k]);
        }
#pragma unroll
        for (int k = 0; k < 4; ++k) {
            t02r[k] = f2{sadd(ar[k].x, ar[k].y), ssub(ar[k].x, ar[k].y)}; t02i[k] = f2{sadd(ai[k].x, ai[k].y), ssub(ai[k].x, ai[k].y)};
            t13r[k] = f2{ssub(mr[k].x, mi[k].y), sadd(mr[k].x, mi[k].y)}; t13i[k] = f2{sadd(mi[k].x, mr[k].y), ssub(mi[k].x, mr[k].y)};
        }
        __builtin_amdgcn_sched_barrier(0);
#pragma unroll
        for (int k = 0; k < 4; ++k) {
            const f2 e02r = t02r[k] * wre02 - t02i[k] * wim02, e02i = t02r[k] * wim02 + t02i[k] * wre02;
            const f2 e13r = t13r[k] * wre13 - t13i[k] * wim13, e13i = t13r[k] * wim13 + t13i[k] * wre13;
            LAS unsigned* e = (LAS unsigned*)(size_t)(eiw + k * 16 * 272);
            e[0] = pk(e02r.x, e02i.x); e[68] = pk(e13r.x, e13i.x); e[136] = pk(e02r.y, e02i.y); e[204] = pk(e13r.y, e13i.y);
        }
    }
    STAMP(13);
    if (w8 < 4) {
        const int tid = w8 * 64 + lane;
        const unsigned yin = (unsigned)WS_Y + beta * 7168 + tid * 16;
        for (unsigned spin = 0; spin < GSPIN_CAP; ++spin) {
            us2 mx = {0, 0};
#pragma unroll
            for (int u = 0; u < 2; ++u)
#pragma unroll
                for (int i = 0; i < 4; ++i) mx = __builtin_elementwise_max(mx, __builtin_bit_cast(us2, yp[u][i]));
            if (!__any(mx.x == 0xffff || mx.y == 0xffff)) break;
            __builtin_amdgcn_s_sleep(YRETRY_SLEEP);
            asm volatile("" ::: "memory");
            const bool ag0 = unfilled16(yp[0]), ag1 = unfilled16(yp[1]);
            const u4 n0 = ld16(rws, ag0 ? yin : OOB), n1 = ld16(rws, (ag1 && tid < 192) ? yin + 4096 : OOB);
            yp[0] = ag0 ? n0 : yp[0]; yp[1] = ag1 ? n1 : yp[1];
        }
        __builtin_amdgcn_fence(__ATOMIC_ACQUIRE, "wavefront");
        const unsigned ysw = laddr(L + L_YS) + (tid >> 4) * 272 + (tid & 15) * 16;
        *(LAS u4*)(size_t)ysw = yp[0];
        if (tid < 192) *(LAS u4*)(size_t)(ysw + 16 * 272) = yp[1];
    }
    WAIT_LGKM0();
    __builtin_amdgcn_s_barrier();
    u16 yv[2][4];
#pragma unroll
    for (int r = 0; r < 4; ++r) { yv[0][r] = *(LAS u16*)(size_t)(late.yr0 + r * 64); yv[1][r] = *(LAS u16*)(size_t)(late.yr1 + r * 64); }
    STAMP(14);
    f4 o[2] = {z4, z4};
#pragma unroll
    for (int tau = 0; tau < 4; ++tau) {
        const u4 a = *(LAS u4*)(size_t)(eir + 64 * tau);
        o[0] = mfma32(a, t3[0][tau], o[0]);
        o[1] = mfma32(a, t3[1][tau], o[1]);
    }
    const f2 gt = *(LAS f2*)(size_t)late.gta;
    float g0 = gt.x, g1 = gt.x, o0 = gt.y, o1 = gt.y;
    asm("v_mov_b32 %0, %1" : "=v"(g1) : "v"(g0)); asm("v_mov_b32 %0, %1" : "=v"(o1) : "v"(o0));
    const f2 gg = {g0, g1}, om = {o0, o1};
    char* orow = (char*)p.out + late.oro;
#pragma unroll
    for (int nt = 0; nt < 2; ++nt) {
        const u2 xv = *(LAS u2*)(size_t)(nt ? xr1 : xr0);
        const f2 b01 = f2{bflo(xv.x), bfhi(xv.x)} * ds[nt][0] + f2{bf2f(yv[nt][0]), bf2f(yv[nt][1])};
        const f2 b23 = f2{bflo(xv.y), bfhi(xv.y)} * ds[nt][1] + f2{bf2f(yv[nt][2]), bf2f(yv[nt][3])};
        const f2 r01 = lo2(o[nt]) * gg + b01 * om, r23 = hi2(o[nt]) * gg + b23 * om;
        if (nt == 0 || n < 12) *(u2*)(orow + 128 * nt) = u2{pk(r01.x, r01.y), pk(r23.x, r23.y)};
    }
    if (beta == 0 && threadIdx.x == 0) __hip_atomic_store((gu64*)(p.ws + WS_EP), ep, RLX_AGENT);
}
__global__ void __launch_bounds__(512, 2) fused_kernel(Params p) {
    extern __shared__ __attribute__((aligned(16))) char smem[];
    lptr L = (lptr)smem;
    const int w8 = __builtin_amdgcn_readfirstlane(threadIdx.x >> 6), lane = threadIdx.x & 63, beta = blockIdx.x;
    STAMP(0);
    {
        u64 pc; asm volatile("s_getpc_b64 %0" : "=s"(pc));
        const char* c = (const char*)(pc & ~63ull) + min((int)threadIdx.x, PF_CODE - 1) * 64;
        glds4(c, laddr(L + L_END));
    }
    {
        const char* px = (const char*)p.x;
        const unsigned ma = w8 < 4 ? ~0u : 0u;
        const unsigned w = w8 & 3, sig = beta & 63;
        const unsigned offa = (16 * (4 * (sig >> 4) + w) + (sig & 15)) * (D_ * 2), offb = 64 * (beta >> 3) * (D_ * 2) + (56 * (beta & 7) + 16 * w) * 2;
        unsigned dst = ((laddr(L + L_XS) + w * 8 * XS_PITCH) & ma) | ((laddr(L + L_TT) + w * 4352) & ~ma);
        const unsigned sstep = ((256 * D_ * 2) & ma) | ((32 * D_ * 2) & ~ma), dstep = (XS_PITCH & ma) | (1024 & ~ma);
        unsigned cnt = (8 & ma) | (2 & ~ma);
        const unsigned voffb = (lane >> 1) * (D_ * 2) + (w < 3 ? 16 * (lane & 1) : 0);
        const unsigned voff = ((lane * 16) & ma) | (voffb & ~ma);
        const char* src = px + ((offa & ma) | (offb & ~ma));
        if (lane < 56 + (8 & ~ma)) {
#pragma unroll 1
            for (; cnt != 0; --cnt) { glds16s(src, voff, dst); src += sstep; dst += dstep; }
        }
        if (ma) {
#pragma unroll 1
            for (unsigned u = 0; u < 3; ++u) {
                const unsigned pc = min(w + 4 * u, 8u);
                const unsigned so = pc < 7 ? (TAB_T1 + (beta >> 6) * 1792) * 4 + pc * 1024 : TAB_F8A * 4 + (pc - 7) * 1024;
                glds16s((const char*)g_tab + so, lane * 16, pc < 7 ? laddr(L + L_T1) + pc * 1024 : laddr(L + L_F8A) + (pc - 7) * 1024);
            }
        }
    }
    if (threadIdx.x < 16) *((LAS unsigned*)(L + L_CTR) + threadIdx.x) = 0;
    WAIT_LGKM0();
    __builtin_amdgcn_s_barrier();
    Late late;
    u4 yp[2];
    if (w8 < 4) { __builtin_amdgcn_s_setprio(PRIOA); teamA(p, beta, w8, lane, L, late, yp STAMP_PASS); }
    else { __builtin_amdgcn_s_setprio(PRIOB); teamB(p, beta, w8 - 4, lane, L, late STAMP_PASS); __builtin_amdgcn_s_waitcnt(0x0F70); }
    phaseA3(p, beta, w8, lane, L, late, yp STAMP_PASS);
    STAMP(15);
}

extern "C" void kernel_launch(void* const* d_in, const int* in_sizes, int n_in, void* d_out, int out_size, void* d_ws, size_t ws_size, hipStream_t stream) {
    Params p;
    p.x = (const u16*)d_in[0]; p.wr = (const u16*)d_in[1]; p.wi = (const u16*)d_in[2];
    p.are = (const u16*)d_in[4]; p.aim = (const u16*)d_in[5]; p.bre = (const u16*)d_in[6]; p.bim = (const u16*)d_in[7];
    p.cre = (const u16*)d_in[8]; p.cim = (const u16*)d_in[9]; p.ldt = (const u16*)d_in[10]; p.dskip = (const u16*)d_in[11];
    p.gw = (const u16*)d_in[12]; p.out = (u16*)d_out; p.ws = (char*)d_ws;
    (void)hipFuncSetAttribute((const void*)fused_kernel, hipFuncAttributeMaxDynamicSharedMemorySize, L_END + 256);
    fused_kernel<<<256, 512, L_END + 256, stream>>>(p);
}
```
